```python
import torch

def _softmax(x, axis=-1):
    return torch.softmax(x, dim=axis)
def _gelu(x):
    return torch.nn.functional.gelu(x, approximate="tanh")
def _silu(x):
    return torch.nn.functional.silu(x)
def _sigmoid(x):
    return torch.sigmoid(x)
def _layernorm(x, axis=-1, eps=1e-5):
    m = x.mean(dim=axis, keepdim=True)
    v = x.var(dim=axis, keepdim=True, unbiased=False)
    return (x - m) / torch.sqrt(v + eps)
def _scatter_add(tgt, idx, src, axis=0):
    return tgt.clone().index_add_(axis, idx, src)
def _segment_sum(src, seg, n):
    out = torch.zeros((n,) + src.shape[1:], dtype=src.dtype)
    return out.index_add_(0, seg, src)
def _pad(x, pw):
    flat = tuple(v for pair in reversed(pw) for v in pair)
    return torch.nn.functional.pad(x, flat)

kernel_name = 'standalone__gqa_block_cached__near_ridge'

def setup_inputs():

    _w = (lambda a: (lambda t: t.to(torch.bfloat16) if t.is_floating_point() else t)(torch.as_tensor(a)))
    B, Sq, D = 8, 64, 576
    Hq, Hkv, Sk, Dh = 32, 16, 2048, 128
    assert Hq % Hkv == 0, (Hq, Hkv)
    return {
        "x": _w((lambda *s: torch.randn(*s, dtype=torch.bfloat16) * 0.8)(B, Sq, D)),
        "k_cache": _w((lambda *s: torch.randn(*s, dtype=torch.bfloat16) * 0.8)(B, Hkv, Sk, Dh)),
        "v_cache": _w((lambda *s: torch.randn(*s, dtype=torch.bfloat16) * 0.05)(B, Hkv, Sk, Dh)),
        "wq": _w((lambda *s: torch.randn(*s, dtype=torch.bfloat16) * 0.8)(D, Hq * Dh) * (D ** -0.5)),
        "wo": _w((lambda *s: torch.randn(*s, dtype=torch.bfloat16) * 0.05)(Hq * Dh, D) * ((Hq * Dh) ** -0.5)),
    }

def reference(x, k_cache, v_cache, wq, wo):
    B, Sq, D = x.shape
    Hq, Dh = 32, 128
    Hkv = k_cache.shape[1]
    rep = Hq // Hkv
    q = (lambda x, *d: x.permute(*d))((x @ wq).reshape(B, Sq, Hq, Dh), 0, 2, 1, 3)
    k = (lambda x, n, axis: torch.repeat_interleave(x, n, dim=axis))(k_cache, rep, axis=1)
    v = (lambda x, n, axis: torch.repeat_interleave(x, n, dim=axis))(v_cache, rep, axis=1)
    scores = torch.einsum("bhqd,bhkd->bhqk", q, k) * (Dh ** -0.5)
    p = _softmax(scores, axis=-1)
    o = torch.einsum("bhqk,bhkd->bhqd", p, v)
    return (lambda x, *d: x.permute(*d))(o, 0, 2, 1, 3).reshape(B, Sq, Hq * Dh) @ wo
```

```cpp
#include <hip/hip_runtime.h>
#include <hip/hip_bf16.h>
#include <stdint.h>

typedef short bf16x8 __attribute__((ext_vector_type(8)));
typedef short s16x4 __attribute__((ext_vector_type(4)));
typedef float f32x16 __attribute__((ext_vector_type(16)));
typedef float f32x4 __attribute__((ext_vector_type(4)));
typedef float f32x2 __attribute__((ext_vector_type(2)));
typedef unsigned u32x4 __attribute__((ext_vector_type(4)));
typedef unsigned u32x2 __attribute__((ext_vector_type(2)));
typedef __bf16 bf2_t __attribute__((ext_vector_type(2)));
#define LAS __attribute__((address_space(3)))
typedef LAS const char* lcptr;
typedef LAS char* lptr;
#define DI __device__ __forceinline__
#define MFMA32(a, b, c) __builtin_amdgcn_mfma_f32_32x32x16_bf16((a), (b), (c), 0, 0, 0)
#define MFMA16(a, b, c) __builtin_amdgcn_mfma_f32_16x16x32_bf16((a), (b), (c), 0, 0, 0)
#define WAITBAR(N) asm volatile("s_waitcnt vmcnt(" #N ") lgkmcnt(0)\n\ts_barrier" ::: "memory")
#define PIN(x) asm volatile("" : "+v"(x))

constexpr int NB = 8, SQ = 64, DM = 576, HQ = 32, HKV = 16, SK = 2048, DH = 128;
constexpr float QSCALE = 0.08838834764831845f * 1.4426950408889634f;
constexpr float THR = 8.f;
constexpr int SLOT = 32768, NSLOT = 5, LDS1 = 163840;
constexpr int STG0 = 65536, STGB = 20480, QIMG = 4 * SLOT;
constexpr size_t WS_PART = 0;
constexpr size_t WS_LSE = WS_PART + (size_t)2 * 8 * 32 * 16 * 1024;
constexpr size_t WS_SLAB = WS_LSE + (size_t)2 * 8 * 32 * 64 * 4;

DI unsigned pk2(float a, float b) { bf2_t r = __builtin_convertvector((f32x2){a, b}, bf2_t); return __builtin_bit_cast(unsigned, r); }
DI s16x4 trrd(lcptr p) { typedef short v4i16_t __attribute__((ext_vector_type(4))); return __builtin_bit_cast(s16x4, __builtin_amdgcn_ds_read_tr16_b64_v4i16((LAS v4i16_t*)p)); }
DI bf16x8 trrd2(lcptr p0, lcptr p1) { s16x4 a = trrd(p0), b = trrd(p1); return __builtin_shufflevector(a, b, 0, 1, 2, 3, 4, 5, 6, 7); }
DI bf16x8 ldsrd16(lcptr p) { return *(LAS const bf16x8*)p; }
DI float swap_max(float m) { auto rr = __builtin_amdgcn_permlane32_swap(__float_as_uint(m), __float_as_uint(m), false, false); return __builtin_fmaxf(__uint_as_float(rr[0]), __uint_as_float(rr[1])); }
DI float swap_add(float m) { auto rr = __builtin_amdgcn_permlane32_swap(__float_as_uint(m), __float_as_uint(m), false, false); return __uint_as_float(rr[0]) + __uint_as_float(rr[1]); }

DI void dma_nt(const void* sbase, unsigned voff, unsigned ldst) {
    unsigned sv; asm volatile("s_mov_b32 %0, m0\n\ts_mov_b32 m0, %3\n\ts_nop 0\n\tglobal_load_lds_dwordx4 %1, %2 nt\n\ts_mov_b32 m0, %0" : "=&s"(sv) : "v"(voff), "s"(sbase), "s"(__builtin_amdgcn_readfirstlane(ldst)) : "memory"); }
DI void dma_def(const void* sbase, unsigned voff, unsigned ldst) {
    unsigned sv; asm volatile("s_mov_b32 %0, m0\n\ts_mov_b32 m0, %3\n\ts_nop 0\n\tglobal_load_lds_dwordx4 %1, %2\n\ts_mov_b32 m0, %0" : "=&s"(sv) : "v"(voff), "s"(sbase), "s"(__builtin_amdgcn_readfirstlane(ldst)) : "memory"); }

__global__ __launch_bounds__(512, 2) void attn_k(const char* __restrict__ X, const char* __restrict__ Kc, const char* __restrict__ Vc, const char* __restrict__ Wq, char* __restrict__ ws) {
    extern __shared__ __attribute__((aligned(16))) char lds_[];
    const int tid = threadIdx.x, lane = tid & 63, r32 = lane & 31, hi = lane >> 5;
    const int wid = __builtin_amdgcn_readfirstlane(tid >> 6);
    const int bid = blockIdx.x, xcd = bid & 7, idx = bid >> 3;
    const int kvh = xcd * 2 + (idx & 1), b = (idx >> 1) & 7, split = idx >> 4;
    const lptr lds = (lptr)lds_;
    const unsigned lds0 = (unsigned)(uintptr_t)lds;
    constexpr int NT = 16;
    const char* kbase = Kc + ((size_t)(b * HKV + kvh) * SK + split * 1024) * 256;
    const char* vbase = Vc + ((size_t)(b * HKV + kvh) * SK + split * 1024) * 256;
    const int lr = lane >> 4, lc = lane & 15;
    const unsigned voffK = (unsigned)((4 * wid + lr) * 256 + 16 * (lc ^ ((4 * wid + lr) & 15)));
    const unsigned voffV = (unsigned)((4 * wid + lr) * 256 + 16 * (lc ^ (lr << 2)));
    const unsigned voffK2 = voffK + 8192, voffV2 = voffV + 8192;
    auto issue_kv = [&](int t, int slot) {
        const unsigned d = lds0 + slot * SLOT + wid * 1024;
        const char* kp = kbase + (size_t)t * 16384; const char* vp = vbase + (size_t)t * 16384;
        dma_nt(kp, voffK, d); dma_nt(kp, voffK2, d + 8192); dma_nt(vp, voffV, d + 16384); dma_nt(vp, voffV2, d + 16384 + 8192); };
    bf16x8 qf[8];
    {
        const char* wbase = Wq + kvh * 512;
        const char* xbase = X + (size_t)b * SQ * DM * 2;
        const unsigned voffW = (unsigned)((8 * (wid & 3) + lr) * 8192 + (wid >> 2) * 256 + 16 * (lc ^ (lr << 2)));
        const unsigned voffW2 = voffW + 4 * 8192;
        const int xrow = 8 * wid + (r32 >> 2);
        const unsigned voffX = (unsigned)(xrow * 1152 + 16 * ((lane & 3) ^ ((xrow >> 2) & 3)));
        const int rot = ((idx >> 1) * 18) >> 4;
        auto issue_st = [&](int i) { int st = i + rot; if (st >= 18) st -= 18;
            const unsigned d = lds0 + STG0 + (i & 3) * STGB;
            const char* wp = wbase + (size_t)st * 32 * 8192;
            dma_def(wp, voffW, d + wid * 2048); dma_def(wp, voffW2, d + wid * 2048 + 1024);
            if (lane < 32) dma_def(xbase + st * 64, voffX, d + 16384 + wid * 512); };
        const int hh = wid >> 2, dt = wid & 3;
        const int q4 = (lane & 15) >> 2, p4 = lane & 3, blk = (lane >> 4) & 1;
        const lcptr wrd = lds + STG0 + hh * 8192 + 256 * (8 * hi + q4) + 16 * ((4 * dt + 2 * blk + (p4 >> 1)) ^ (q4 << 2)) + 8 * (p4 & 1);
        const lcptr xrd0 = lds + STG0 + 16384 + r32 * 64 + 16 * ((0 + hi) ^ ((r32 >> 2) & 3));
        const lcptr xrd1 = lds + STG0 + 16384 + r32 * 64 + 16 * ((2 + hi) ^ ((r32 >> 2) & 3));
        f32x16 a0 = {}, a1 = {};
        auto comp = [&](int i) { const int o = (i & 3) * STGB;
            bf16x8 w0 = trrd2(wrd + o, wrd + o + 1024), w1 = trrd2(wrd + o + 4096, wrd + o + 4096 + 1024);
            bf16x8 x00 = ldsrd16(xrd0 + o), x01 = ldsrd16(xrd0 + o + 2048), x10 = ldsrd16(xrd1 + o), x11 = ldsrd16(xrd1 + o + 2048);
            a0 = MFMA32(w0, x00, a0); a1 = MFMA32(w0, x01, a1); a0 = MFMA32(w1, x10, a0); a1 = MFMA32(w1, x11, a1); };
        issue_st(0); issue_st(1); issue_st(2);
        for (int i = 0; i < 15; ++i) { WAITBAR(6); issue_st(i + 3); comp(i); }
        WAITBAR(6); issue_kv(0, 0); comp(15);
        WAITBAR(7); issue_kv(1, 1); comp(16);
        WAITBAR(8); comp(17);
        WAITBAR(8);
        issue_kv(2, 2); issue_kv(3, 3);
#pragma unroll
        for (int j = 0; j < 2; ++j) { const f32x16& a = j ? a1 : a0; const int row = hh * 64 + j * 32 + r32;
#pragma unroll
            for (int g = 0; g < 4; ++g) { u32x2 v = {pk2(a[4 * g] * QSCALE, a[4 * g + 1] * QSCALE), pk2(a[4 * g + 2] * QSCALE, a[4 * g + 3] * QSCALE)};
                *(LAS u32x2*)(lds + QIMG + row * 256 + 16 * ((4 * dt + g) ^ (row & 15)) + 8 * hi) = v; } }
        WAITBAR(63);
        const int qrow = (wid & 3) * 32 + r32;
#pragma unroll
        for (int s = 0; s < 8; ++s) qf[s] = ldsrd16(lds + QIMG + qrow * 256 + 16 * ((2 * s + hi) ^ (qrow & 15)));
    }
    const int rg = wid & 3, kh = wid >> 2;
    const int q4 = (lane & 15) >> 2, p4 = lane & 3, blk = (lane >> 4) & 1;
    const unsigned krd = (unsigned)(256 * (32 * kh + r32) + 16 * (hi ^ (r32 & 15)));
    unsigned vrd[4];
#pragma unroll
    for (int c = 0; c < 4; ++c) vrd[c] = (unsigned)(16384 + 256 * (32 * kh + 4 * hi + q4) + 16 * ((4 * c + 2 * blk + (p4 >> 1)) ^ (q4 << 2)) + 8 * (p4 & 1));
    f32x16 O[4] = {}; float lsum = 0.f, mrow = 0.f; f32x16 negm = {};
    auto step = [&](int slot, bool first) {
        const unsigned sb = slot * SLOT;
        bf16x8 kf[8];
#pragma unroll
        for (int s = 0; s < 8; ++s) kf[s] = ldsrd16(lds + ((sb + krd) ^ (s << 5)));
        f32x16 S = MFMA32(kf[0], qf[0], negm);
#pragma unroll
        for (int s = 1; s < 8; ++s) S = MFMA32(kf[s], qf[s], S);
        bf16x8 vf[4][2];
#pragma unroll
        for (int c = 0; c < 4; ++c)
#pragma unroll
            for (int s = 0; s < 2; ++s) vf[c][s] = trrd2(lds + sb + vrd[c] + s * 4096, lds + sb + vrd[c] + s * 4096 + 2048);
        float mx = __builtin_fmaxf(__builtin_fmaxf(S[0], S[1]), S[2]);
#pragma unroll
        for (int r = 3; r < 15; r += 2) mx = __builtin_fmaxf(__builtin_fmaxf(mx, S[r]), S[r + 1]);
        mx = __builtin_fmaxf(mx, S[15]);
        mx = swap_max(mx);
        if (first) { mrow = mx;
#pragma unroll
            for (int r = 0; r < 16; ++r) { S[r] -= mx; negm[r] = -mrow; }
        } else if (__builtin_expect(__any(mx > THR), 0)) {
            const float d = __builtin_fmaxf(mx, 0.f); mrow += d; const float a = __builtin_amdgcn_exp2f(-d); lsum *= a;
#pragma unroll
            for (int r = 0; r < 16; ++r) { S[r] -= d; negm[r] = -mrow; }
#pragma unroll
            for (int c = 0; c < 4; ++c)
#pragma unroll
                for (int r = 0; r < 16; ++r) O[c][r] *= a;
        }
        PIN(negm);
#pragma unroll
        for (int r = 0; r < 16; ++r) S[r] = __builtin_amdgcn_exp2f(S[r]);
        float ps0 = S[0], ps1 = S[1];
#pragma unroll
        for (int r = 2; r < 16; r += 2) { ps0 += S[r]; ps1 += S[r + 1]; }
        lsum += ps0 + ps1;
        u32x4 pw0 = {pk2(S[0], S[1]), pk2(S[2], S[3]), pk2(S[4], S[5]), pk2(S[6], S[7])};
        u32x4 pw1 = {pk2(S[8], S[9]), pk2(S[10], S[11]), pk2(S[12], S[13]), pk2(S[14], S[15])};
        const bf16x8 p0 = __builtin_bit_cast(bf16x8, pw0), p1 = __builtin_bit_cast(bf16x8, pw1);
#pragma unroll
        for (int c = 0; c < 4; ++c) { O[c] = MFMA32(vf[c][0], p0, O[c]); O[c] = MFMA32(vf[c][1], p1, O[c]); }
    };
    WAITBAR(12); issue_kv(4, 4); step(0, true);
    int slot = 1, fslot = 0;
    for (int t = 1; t < NT - 4; ++t) { WAITBAR(12); issue_kv(t + 4, fslot); step(slot, false); fslot = slot; slot = slot == NSLOT - 1 ? 0 : slot + 1; }
    for (int t = NT - 4; t < NT; ++t) {
        if (t == NT - 4) WAITBAR(12); else if (t == NT - 3) WAITBAR(8); else if (t == NT - 2) WAITBAR(4); else WAITBAR(0);
        step(slot, false); slot = slot == NSLOT - 1 ? 0 : slot + 1; }
    lsum = swap_add(lsum);
    WAITBAR(0);
    {
        const lptr xo = lds + wid * 8192 + lane * 16;
#pragma unroll
        for (int j = 0; j < 2; ++j)
#pragma unroll
            for (int g = 0; g < 4; ++g) { const f32x16& s0 = O[j], & s1 = O[2 + j];
                f32x4 v = kh ? (f32x4){s0[4 * g], s0[4 * g + 1], s0[4 * g + 2], s0[4 * g + 3]} : (f32x4){s1[4 * g], s1[4 * g + 1], s1[4 * g + 2], s1[4 * g + 3]};
                *(LAS f32x4*)(xo + (j * 4 + g) * 1024) = v; }
        *(LAS f32x2*)(lds + 65536 + wid * 512 + lane * 8) = (f32x2){mrow, lsum};
        WAITBAR(0);
        const int pw = wid ^ 4;
        const f32x2 ml = *(LAS const f32x2*)(lds + 65536 + pw * 512 + lane * 8);
        const float M = __builtin_fmaxf(mrow, ml[0]); const float a = __builtin_amdgcn_exp2f(mrow - M), a2 = __builtin_amdgcn_exp2f(ml[0] - M);
        const float L = a * lsum + a2 * ml[1]; const float inv = 1.f / L; const float f = a * inv, f2 = a2 * inv;
        const lcptr xi = lds + pw * 8192 + lane * 16;
        const int head = kvh * 2 + (rg >> 1), qh = rg & 1;
        char* po = ws + WS_PART + ((((size_t)(split * NB + b) * HQ + head) * 2 + qh) * 8) * 1024 + lane * 16;
#pragma unroll
        for (int j = 0; j < 2; ++j) { f32x16 own = kh ? O[2 + j] : O[j]; float v[16];
#pragma unroll
            for (int g = 0; g < 4; ++g) { const f32x4 o2 = *(LAS const f32x4*)(xi + (j * 4 + g) * 1024);
#pragma unroll
                for (int e = 0; e < 4; ++e) v[4 * g + e] = own[4 * g + e] * f + o2[e] * f2; }
            const int c = 2 * kh + j;
#pragma unroll
            for (int s = 0; s < 2; ++s) { u32x4 w = {pk2(v[8 * s], v[8 * s + 1]), pk2(v[8 * s + 2], v[8 * s + 3]), pk2(v[8 * s + 4], v[8 * s + 5]), pk2(v[8 * s + 6], v[8 * s + 7])};
                *(u32x4*)(po + (c * 2 + s) * 1024) = w; } }
        if (kh == 0 && hi == 0) ((float*)(ws + WS_LSE))[((split * NB + b) * HQ + head) * 64 + qh * 32 + r32] = M + __builtin_amdgcn_logf(L);
    }
}

__global__ __launch_bounds__(512, 2) void oproj_k(const char* __restrict__ Wo, char* __restrict__ ws) {
    extern __shared__ __attribute__((aligned(16))) char lds_[];
    const int tid = threadIdx.x, lane = tid & 63; const int wid = __builtin_amdgcn_readfirstlane(tid >> 6);
    const int bid = blockIdx.x, ks = bid & 7, nq = (bid >> 3) & 3, b = bid >> 5;
    const lptr lds = (lptr)lds_; const unsigned lds0 = (unsigned)(uintptr_t)lds;
    const char* wbase = Wo + (size_t)ks * 512 * 1152 + nq * 288;
#pragma unroll
    for (int p = 0; p < 9; ++p) { const unsigned byte = p * 1024 + lane * 16; const unsigned row = byte / 288, ch = byte - row * 288; const unsigned voff = row * 1152 + ch;
#pragma unroll
        for (int j = 0; j < 2; ++j) dma_def(wbase + (size_t)(2 * wid + j) * 32 * 1152, voff, lds0 + (2 * wid + j) * 9216 + p * 1024); }
    const int qt = wid & 3, kh2 = wid >> 2, g = lane >> 4, ql = lane & 15;
    bf16x8 of[8];
#pragma unroll
    for (int hl = 0; hl < 2; ++hl) { const int head = ks * 4 + kh2 * 2 + hl;
        const float* lse = (const float*)(ws + WS_LSE); const float l0 = lse[((0 * NB + b) * HQ + head) * 64 + qt * 16 + ql], l1 = lse[((1 * NB + b) * HQ + head) * 64 + qt * 16 + ql];
        const float M = __builtin_fmaxf(l0, l1), e0 = __builtin_amdgcn_exp2f(l0 - M), e1 = __builtin_amdgcn_exp2f(l1 - M), inv = 1.f / (e0 + e1), a0 = e0 * inv, a1 = e1 * inv;
#pragma unroll
        for (int c = 0; c < 4; ++c) {
            const size_t off = ((((size_t)b * HQ + head) * 2 + (qt >> 1)) * 8 + c * 2 + (g >> 1)) * 1024 + ((g & 1) * 32 + 16 * (qt & 1) + ql) * 16;
            const u32x4 f0 = *(const u32x4*)(ws + WS_PART + off), f1 = *(const u32x4*)(ws + WS_PART + (size_t)NB * HQ * 16 * 1024 + off); u32x4 r;
#pragma unroll
            for (int e = 0; e < 4; ++e) { const float x0 = __uint_as_float(f0[e] << 16), x1 = __uint_as_float(f0[e] & 0xffff0000u), y0 = __uint_as_float(f1[e] << 16), y1 = __uint_as_float(f1[e] & 0xffff0000u);
                r[e] = pk2(x0 * a0 + y0 * a1, x1 * a0 + y1 * a1); }
            of[hl * 4 + c] = __builtin_bit_cast(bf16x8, r); } }
    WAITBAR(0);
    f32x4 acc[9] = {};
    const int q4 = ql >> 2, p4 = lane & 3;
#pragma unroll
    for (int hl = 0; hl < 2; ++hl) { const lcptr base = lds + ((kh2 * 2 + hl) * 128 + 16 * (g >> 1) + 4 * (g & 1) + q4) * 288 + 8 * p4;
#pragma unroll
        for (int c = 0; c < 4; ++c)
#pragma unroll
            for (int nt = 0; nt < 9; ++nt) { const bf16x8 a = trrd2(base + (32 * c) * 288 + 32 * nt, base + (32 * c + 8) * 288 + 32 * nt); acc[nt] = MFMA16(a, of[hl * 4 + c], acc[nt]); } }
    WAITBAR(0);
    if (kh2) {
#pragma unroll
        for (int nt = 0; nt < 9; ++nt) *(LAS f32x4*)(lds + (qt * 9 + nt) * 1024 + lane * 16) = acc[nt]; }
    WAITBAR(0);
    if (!kh2) { char* sl = ws + WS_SLAB + ((((size_t)ks * NB + b) * 4 + nq) * 4 + qt) * 9 * 1024 + lane * 16;
#pragma unroll
        for (int nt = 0; nt < 9; ++nt) { const f32x4 o = *(LAS const f32x4*)(lds + (qt * 9 + nt) * 1024 + lane * 16); *(f32x4*)(sl + nt * 1024) = acc[nt] + o; } }
}

__global__ __launch_bounds__(288) void reduce_k(const char* __restrict__ ws, char* __restrict__ out) {
    const int i = blockIdx.x * 288 + threadIdx.x;
    const int lane = i & 63, tix = i >> 6, nt = tix % 9, r = tix / 9, qt = r & 3, nq = (r >> 2) & 3, b = r >> 4;
    f32x4 s = {};
#pragma unroll
    for (int ks = 0; ks < 8; ++ks) s += *(const f32x4*)(ws + WS_SLAB + ((size_t)ks * 73728 + i) * 16);
    *(u32x2*)(out + ((size_t)(b * 64 + qt * 16 + (lane & 15)) * DM + nq * 144 + nt * 16 + 4 * (lane >> 4)) * 2) = (u32x2){pk2(s[0], s[1]), pk2(s[2], s[3])};
}

extern "C" void kernel_launch(void* const* d_in, const int* in_sizes, int n_in, void* d_out, int out_size, void* d_ws, size_t ws_size, hipStream_t stream) {
    (void)hipFuncSetAttribute((const void*)attn_k, hipFuncAttributeMaxDynamicSharedMemorySize, LDS1);
    (void)hipFuncSetAttribute((const void*)oproj_k, hipFuncAttributeMaxDynamicSharedMemorySize, 147456);
    hipLaunchKernelGGL(attn_k, dim3(256), dim3(512), LDS1, stream, (const char*)d_in[0], (const char*)d_in[1], (const char*)d_in[2], (const char*)d_in[3], (char*)d_ws);
    hipLaunchKernelGGL(oproj_k, dim3(256), dim3(512), 147456, stream, (const char*)d_in[4], (char*)d_ws);
    hipLaunchKernelGGL(reduce_k, dim3(256), dim3(288), 0, stream, (const char*)d_ws, (char*)d_out);
}
```

```cpp
#include <hip/hip_runtime.h>
#include <hip/hip_bf16.h>
#include <stdint.h>

typedef short bf16x8 __attribute__((ext_vector_type(8)));
typedef short s16x4 __attribute__((ext_vector_type(4)));
typedef float f32x16 __attribute__((ext_vector_type(16)));
typedef float f32x4 __attribute__((ext_vector_type(4)));
typedef float f32x2 __attribute__((ext_vector_type(2)));
typedef unsigned u32x4 __attribute__((ext_vector_type(4)));
typedef unsigned u32x2 __attribute__((ext_vector_type(2)));
typedef __bf16 bf2_t __attribute__((ext_vector_type(2)));
typedef unsigned long long u64;
#define LAS __attribute__((address_space(3)))
#define GAS __attribute__((address_space(1)))
typedef LAS const char* lcptr;
typedef LAS char* lptr;
#define DI __device__ __forceinline__
#define MFMA32(a, b, c) __builtin_amdgcn_mfma_f32_32x32x16_bf16((a), (b), (c), 0, 0, 0)
#define MFMA16(a, b, c) __builtin_amdgcn_mfma_f32_16x16x32_bf16((a), (b), (c), 0, 0, 0)
#define WAITBAR(N) asm volatile("s_waitcnt vmcnt(" #N ") lgkmcnt(0)\n\ts_barrier" ::: "memory")
#define WAITV(N) asm volatile("s_waitcnt vmcnt(" #N ")" ::: "memory")
#define PIN(x) asm volatile("" : "+v"(x))
#define SBAR() __builtin_amdgcn_sched_barrier(0)
#define LGKM(n) do { __builtin_amdgcn_s_waitcnt(0xC07F | ((n) << 8)); SBAR(); } while (0)
#define LP(a) ((lcptr)(uintptr_t)(a))
#define RLX_AGENT __ATOMIC_RELAXED, __HIP_MEMORY_SCOPE_AGENT

constexpr int NB = 8, SQ = 64, DM = 576, HQ = 32, HKV = 16, SK = 2048, DH = 128;
constexpr float QSCALE = 0.08838834764831845f * 1.4426950408889634f;
constexpr float THR = 8.f;
#define TOUCH 2
#define TPOL " nt"
#define CPRIO 2
#define LPRIO 0
#define LSLEEP 1
#define QW 10
#define CTOUCH 1
#define HPRE 2
#define NSTG 4
#define NTA 14
#define TPOFF 2
constexpr int NT_A = NTA, NT_B = 32 - NTA, TP = NT_B - TPOFF;
constexpr int USLOT = 16384, NSL = 9, MISC = NSL * USLOT, LDS1 = 163840;
constexpr int L_LANDED = MISC, L_DONE = MISC + 16, L_CNT = MISC + 32, L_TAG = MISC + 64, L_LSE = MISC + 256;
constexpr int NQU = 23;
constexpr int SPIN_CAP = 1 << 17;
constexpr size_t WS_CT = 0;
constexpr size_t WS_EP = 128 * 1024, WS_FS = WS_EP + 8 * 256;
constexpr size_t WS_QX = 1 << 20;
constexpr size_t WS_PX = WS_QX + ((size_t)4 << 20);
constexpr size_t WS_PL = WS_PX + ((size_t)4 << 20);
constexpr size_t WS_OX = WS_PL + ((size_t)1 << 20);
constexpr size_t WS_SLAB = WS_OX + ((size_t)4 << 20);
constexpr int WS_USED = 32 << 20;

#define STAMP3(i)
#define STAMP2(i)
#define STAMP_DECL
#define STAMP(i)
#define STAMP_OUT(k, w)

DI unsigned pk2(float a, float b) { bf2_t r = __builtin_convertvector((f32x2){a, b}, bf2_t); return __builtin_bit_cast(unsigned, r); }
DI s16x4 trrd(lcptr p) { typedef short v4i16_t __attribute__((ext_vector_type(4))); return __builtin_bit_cast(s16x4, __builtin_amdgcn_ds_read_tr16_b64_v4i16((LAS v4i16_t*)p)); }
DI bf16x8 trrd2(lcptr p0, lcptr p1) { s16x4 a = trrd(p0), b = trrd(p1); return __builtin_shufflevector(a, b, 0, 1, 2, 3, 4, 5, 6, 7); }
DI bf16x8 ldsrd16(lcptr p) { return *(LAS const bf16x8*)p; }
DI float swap_max(float m) { auto rr = __builtin_amdgcn_permlane32_swap(__float_as_uint(m), __float_as_uint(m), false, false); return __builtin_fmaxf(__uint_as_float(rr[0]), __uint_as_float(rr[1])); }
DI float swap_add(float m) { auto rr = __builtin_amdgcn_permlane32_swap(__float_as_uint(m), __float_as_uint(m), false, false); return __uint_as_float(rr[0]) + __uint_as_float(rr[1]); }
DI u64 next_tag(u64 e) { return e * 6364136223846793005ull + 1442695040888963407ull; }
DI u64 rfl64(u64 v) { return ((u64)(unsigned)__builtin_amdgcn_readfirstlane((unsigned)(v >> 32)) << 32) | (unsigned)__builtin_amdgcn_readfirstlane((unsigned)v); }
DI int lds_min4(unsigned a) { u32x4 v; asm volatile("ds_read_b128 %0, %1\n\ts_waitcnt lgkmcnt(0)" : "=v"(v) : "v"(a) : "memory");
    unsigned m = v[0] < v[1] ? v[0] : v[1], n = v[2] < v[3] ? v[2] : v[3]; return __builtin_amdgcn_readfirstlane(m < n ? m : n); }
DI void lds_put(unsigned a, unsigned val) { asm volatile("ds_write_b32 %0, %1" :: "v"(a), "v"(val) : "memory"); }
DI void poll_flag(const char* p, u64 tag, int& budget) {
#pragma nounroll
    while (rfl64(__hip_atomic_load((GAS const u64*)p, RLX_AGENT)) != tag && --budget > 0) __builtin_amdgcn_s_sleep(1);
    asm volatile("" ::: "memory"); }

#define DMA4(POL, sbase, v0, v1, v2, v3, ldst) do { unsigned sv_; asm volatile("s_mov_b32 %0, m0\n\ts_mov_b32 m0, %6\n\ts_nop 0\n\t" \
    "global_load_lds_dwordx4 %1, %5" POL "\n\tglobal_load_lds_dwordx4 %2, %5 offset:1024" POL "\n\tglobal_load_lds_dwordx4 %3, %5 offset:2048" POL "\n\tglobal_load_lds_dwordx4 %4, %5 offset:3072" POL "\n\t" \
    "s_mov_b32 m0, %0" : "=&s"(sv_) : "v"(v0), "v"(v1), "v"(v2), "v"(v3), "s"(sbase), "s"(__builtin_amdgcn_readfirstlane(ldst)) : "memory"); } while (0)
#define DMA2(sbase, v0, v1, ldst) do { unsigned sv_; asm volatile("s_mov_b32 %0, m0\n\ts_mov_b32 m0, %4\n\ts_nop 0\n\t" \
    "global_load_lds_dwordx4 %1, %3\n\tglobal_load_lds_dwordx4 %2, %3 offset:1024\n\t" \
    "s_mov_b32 m0, %0" : "=&s"(sv_) : "v"(v0), "v"(v1), "s"(sbase), "s"(__builtin_amdgcn_readfirstlane(ldst)) : "memory"); } while (0)
DI void dma_def(const void* sbase, unsigned voff, unsigned ldst) {
    unsigned sv; asm volatile("s_mov_b32 %0, m0\n\ts_mov_b32 m0, %3\n\ts_nop 0\n\tglobal_load_lds_dwordx4 %1, %2\n\ts_mov_b32 m0, %0" : "=&s"(sv) : "v"(voff), "s"(sbase), "s"(__builtin_amdgcn_readfirstlane(ldst)) : "memory"); }

__global__ __launch_bounds__(512, 2) void attn_k(const char* __restrict__ X, const char* __restrict__ Kc, const char* __restrict__ Vc, const char* __restrict__ Wq, const char* __restrict__ Wo, char* __restrict__ out, char* __restrict__ ws) {
    extern __shared__ __attribute__((aligned(16))) char lds_[];
    const int tid = threadIdx.x, lane = tid & 63, r32 = lane & 31, hi = lane >> 5;
    const int wid = __builtin_amdgcn_readfirstlane(tid >> 6);
    const int bid = blockIdx.x, xcd = bid & 7, idx = bid >> 3;
    const int kvh = xcd * 2 + (idx & 1), b = (idx >> 1) & 7;
    const bool roleB = idx < 16;
    const int pair = b * HKV + kvh;
    const lptr lds = (lptr)lds_;
    const unsigned lds0 = (unsigned)(uintptr_t)lds;
    const int NT = roleB ? NT_B : NT_A;
    const int rot = b;
    char* const ctl = ws + WS_CT + (size_t)pair * 1024;
    char* const epo = ws + WS_EP + b * 256;
    const int ks = xcd, nq = ((idx & 1) + (roleB ? 0 : 2) + b) & 3;
    char* const fsl = ws + WS_FS + (size_t)(b * 4 + nq) * 8 * 256;
    const __amdgpu_buffer_rsrc_t wsr = __builtin_amdgcn_make_buffer_rsrc(ws, 0, WS_USED, 0x00020000);
    STAMP_DECL; STAMP(0);
    u64 pc0; unsigned code_len;
    asm volatile("s_getpc_b64 %0\n.Lcs%=:\n\ts_mov_b32 %1, .Lce%=-.Lcs%=\n\t.subsection 2\n.Lce%=:\n\t.fill 128, 4, 0xBF800000\n\t.subsection 0" : "=s"(pc0), "=s"(code_len));
    if (tid < 16) *(LAS unsigned*)(lds + MISC + tid * 4) = 0u;
    WAITBAR(63);
    int budget = SPIN_CAP;

    if (wid >= 4) {
        const int j = wid - 4, lr = lane >> 4, lc = lane & 15;
        __builtin_amdgcn_s_setprio(LPRIO);
        const size_t koff = ((size_t)pair * SK + (roleB ? NT_A * 64 : 0)) * 256;
        const char* kp = Kc + koff; const char* vp = Vc + koff;
        unsigned vK[4];
#pragma unroll
        for (int i = 0; i < 4; ++i) vK[i] = (unsigned)((16 * j + lr) * 256 + 16 * (lc ^ (4 * i + lr)));
        const unsigned vV = (unsigned)((16 * j + lr) * 256 + 16 * (lc ^ (lr << 2)));
        int u = 0, mind = 0, p1 = 0, pub = 0;
        unsigned so = lds0;
        unsigned a_landed = lds0 + L_LANDED + 4 * j, a_done = lds0 + L_DONE; PIN(a_landed); PIN(a_done);
        auto publish = [&](int c) __attribute__((always_inline)) { pub = c > pub ? c : pub; lds_put(a_landed, (unsigned)pub); };
        auto space = [&](int n) __attribute__((always_inline)) { if (__builtin_expect(u + n > mind + NSL, 0)) { mind = lds_min4(a_done);
                if (u + n > mind + NSL) { WAITV(4); publish(p1); WAITV(0); publish(u);
                    do { __builtin_amdgcn_s_sleep(LSLEEP); mind = lds_min4(a_done); } while (u + n > mind + NSL && --budget > 0); } } };
        auto adv = [&]() __attribute__((always_inline)) { STAMP3(8 + u); ++u; so = so == lds0 + (NSL - 1) * USLOT ? lds0 : so + USLOT; };
        if (roleB) {
            unsigned vX[2], vW[4];
#pragma unroll
            for (int i = 0; i < 2; ++i) { const int row = 16 * j + 8 * i + (lane >> 3); vX[i] = (unsigned)(row * 1152 + 16 * ((lane & 7) ^ ((row >> 1) & 7)) - 1024 * i); }
#pragma unroll
            for (int i = 0; i < 4; ++i) vW[i] = (unsigned)((8 * j + 2 * i + hi) * 8192 + 16 * (r32 ^ (((2 * i + hi) & 3) << 2)) - 1024 * i);
            const char* xb = X + (size_t)b * SQ * DM * 2; const char* wb = Wq + kvh * 512;
            int g = rot;
#pragma nounroll
            for (int gi = 0; gi < 9; ++gi) {
                const char* xp = xb + g * 128; const char* wp = wb + (size_t)g * 64 * 8192;
                space(1); DMA2(xp, vX[0], vX[1], so + j * 2048);
                WAITV(10); publish(u - 2); p1 = u - 1; ++u;
                space(1); DMA4("", wp, vW[0], vW[1], vW[2], vW[3], so + USLOT + j * 4096);
                WAITV(10); publish(u - 2); p1 = u; ++u;
                space(1); DMA4("", wp + 32 * 8192, vW[0], vW[1], vW[2], vW[3], so + 2 * USLOT + j * 4096);
                WAITV(10); publish(u - 2); p1 = u; ++u;
                STAMP3(8 + gi);
                so = so == lds0 + 6 * USLOT ? lds0 : so + 3 * USLOT;
                g = g == 8 ? 0 : g + 1;
            }
            WAITV(8); publish(u - 2);
        }
        STAMP(1);
        auto kv_dma = [&](int n) __attribute__((always_inline)) { space(1); const unsigned d = so + j * 4096;
            if (n & 1) DMA4(" nt", vp + (size_t)(n >> 1) * 16384, vV, vV, vV, vV, d); else DMA4(" nt", kp + (size_t)(n >> 1) * 16384, vK[0], vK[1], vK[2], vK[3], d); };
        auto kv_run = [&](int n0, int n1) __attribute__((always_inline)) {
#pragma nounroll
            for (int n = n0; n < n1; ++n) { kv_dma(n); WAITV(8); publish(p1); p1 = u; adv(); } };
        auto put4 = [&](const u32x4* v) __attribute__((always_inline)) { space(1);
#pragma unroll
            for (int i = 0; i < 4; ++i) *(LAS u32x4*)((lptr)(uintptr_t)(so + j * 4096 + lane * 16) + i * 1024) = v[i];
            asm volatile("s_waitcnt lgkmcnt(0)" ::: "memory"); adv(); };
        if (!roleB) {
            kv_run(0, NSL);
            u32x4 stg[NSTG][4];
#pragma unroll
            for (int k = 0; k < NSTG; ++k) { const int n = NSL + k; const char* src = (n & 1 ? vp : kp) + (size_t)(n >> 1) * 16384;
                asm volatile("global_load_dwordx4 %0, %4, %8 nt\n\tglobal_load_dwordx4 %1, %5, %8 offset:1024 nt\n\tglobal_load_dwordx4 %2, %6, %8 offset:2048 nt\n\tglobal_load_dwordx4 %3, %7, %8 offset:3072 nt"
                             : "=&v"(stg[k][0]), "=&v"(stg[k][1]), "=&v"(stg[k][2]), "=&v"(stg[k][3]) : "v"(n & 1 ? vV : vK[0]), "v"(n & 1 ? vV : vK[1]), "v"(n & 1 ? vV : vK[2]), "v"(n & 1 ? vV : vK[3]), "s"(src) : "memory");
                WAITV(8); STAMP3(40 + k); if (k == 0) publish(NSL - 1); if (k == 1) publish(NSL); }
            WAITV(0); publish(NSL);
            STAMP(2);
#pragma unroll
            for (int k = 0; k < NSTG; ++k) { asm volatile("" : "+v"(stg[k][0]), "+v"(stg[k][1]), "+v"(stg[k][2]), "+v"(stg[k][3])); put4(stg[k]); publish(u); }
            p1 = u;
            kv_run(NSL + NSTG, 2 * NT_A);
        } else {
            WAITV(4); publish(p1); WAITV(0); publish(u); p1 = u;
            kv_run(0, HPRE);
            WAITV(4); publish(p1); WAITV(0); publish(u);
            while (__builtin_amdgcn_readfirstlane(*(volatile LAS unsigned*)(lds + L_CNT)) < 4 && --budget > 0) __builtin_amdgcn_s_sleep(1);
            kv_run(HPRE, 2 * NT_B);
            WAITV(4); publish(p1); WAITV(0); publish(u);
            STAMP(5);
            const u64 tag = rfl64(*(volatile LAS const u64*)(lds + L_TAG));
            poll_flag(ctl + 512, tag, budget);
            STAMP(2);
            u32x4 pf[8];
#pragma unroll
            for (int i = 0; i < 8; ++i) pf[i] = __builtin_amdgcn_raw_buffer_load_b128(wsr, (unsigned)WS_PX + (pair * 4 + j) * 8192 + lane * 16 + i * 1024, 0, 16);
            *(LAS unsigned*)(lds + L_LSE + j * 256 + lane * 4) = __builtin_amdgcn_raw_buffer_load_b32(wsr, (unsigned)WS_PL + (pair * 4 + j) * 256 + lane * 4, 0, 16);
            put4(pf); put4(pf + 4); p1 = u;
        }
        STAMP(3);
        {
            const char* wb = Wo + (size_t)ks * 512 * 1152 + nq * 288;
#pragma nounroll
            for (int n = 0; n < NSL; ++n) {
                unsigned v[4];
#pragma unroll
                for (int i = 0; i < 4; ++i) { const unsigned c = (so - lds0) / 16 + j * 256 + i * 64 + lane, grp = c / 72, r = c - grp * 72;
                    const unsigned q = r < 64 ? (r >> 2) & 3 : (r - 64) >> 1, cc = r < 64 ? (r >> 4) * 4 + (r & 3) : 16 + (r & 1);
                    v[i] = (grp * 4 + q) * 1152 + cc * 16 + 3072 - 1024 * i; }
                space(1); DMA4("", wb - 3072, v[0], v[1], v[2], v[3], so + j * 4096); WAITV(8); publish(p1); p1 = u; adv();
            }
        }
        WAITV(4); publish(p1); WAITV(0); publish(u);
        STAMP(4);
        STAMP_OUT(1, 4);
        if (j != 0) return;
        WAITBAR(63); WAITBAR(63); WAITBAR(63);
    } else {

    const int rg = wid;
    __builtin_amdgcn_s_setprio(CPRIO);
    if (!roleB) {
        unsigned d[4];
        const unsigned i0 = (idx - 16) * 288 + rg * 72 + lane;
#pragma unroll
        for (int k = 0; k < 2; ++k) { const unsigned i = k < 1 ? i0 : i0 - lane + 64 + (lane & 7);
            asm volatile("global_load_dword %0, %2, %3" TPOL "\n\tglobal_load_dword %1, %4, %5" : "=&v"(d[2 * k]), "=&v"(d[2 * k + 1]) : "v"((i >> 3) * 8192 + xcd * 1024 + (i & 7) * 128), "s"(Wq), "v"(i * 128), "s"(X) : "memory"); }
        asm volatile("s_waitcnt vmcnt(0)" :: "v"(d[0]), "v"(d[1]), "v"(d[2]), "v"(d[3]) : "memory");
    }
    if (!roleB && (idx & 3) == 0) {
        unsigned d; unsigned o = (rg * 64 + lane) * 128; o = o < code_len ? o : 0;
        asm volatile("global_load_dword %0, %1, %2\n\ts_waitcnt vmcnt(0)" : "=&v"(d) : "v"(o), "s"(pc0) : "memory");
    }
    const u64 epoch = __hip_atomic_load((GAS const u64*)epo, RLX_AGENT);
    u64 tag;
    const int q4 = (lane & 15) >> 2, p4 = lane & 3, blk = (lane >> 4) & 1;
    int u = 0, avail = 0;
    unsigned a_landed = lds0 + L_LANDED, a_done = lds0 + L_DONE + 4 * rg; PIN(a_landed); PIN(a_done);
    unsigned so = lds0;
    auto acquire = [&]() __attribute__((always_inline)) -> unsigned {
        avail = __builtin_amdgcn_readfirstlane(avail); budget = __builtin_amdgcn_readfirstlane(budget);
        asm volatile("s_cmp_gt_i32 %0, %2\n\ts_cbranch_scc0 .Las%=\n"
                     ".Lab%=:\n\t.subsection 1\n"
                     ".Las%=:\n\tds_read_b128 v[140:143], %3\n\ts_waitcnt lgkmcnt(0)\n\tv_min_u32 v142, v142, v143\n\tv_min3_u32 v140, v140, v141, v142\n\ts_nop 1\n\tv_readfirstlane_b32 %0, v140\n\ts_nop 3\n\t"
                     "s_cmp_gt_i32 %0, %2\n\ts_cbranch_scc1 .Lab%=\n\ts_sub_i32 %1, %1, 1\n\ts_cmp_lt_i32 %1, 1\n\ts_cbranch_scc1 .Lab%=\n\ts_sleep 1\n\ts_branch .Las%=\n\t.subsection 0"
                     : "+&s"(avail), "+&s"(budget) : "s"(__builtin_amdgcn_readfirstlane(u)), "v"(a_landed) : "memory", "scc", "v140", "v141", "v142", "v143");
        const unsigned o = so; ++u; so = so == lds0 + (NSL - 1) * USLOT ? lds0 : so + USLOT; return o; };
    auto release = [&](int c) { lds_put(a_done, (unsigned)c); };
    auto signoff = [&](int cnt_idx, char* flag) __attribute__((always_inline)) { WAITV(0);
        unsigned old = 0; if (lane == 0) old = __hip_atomic_fetch_add((LAS unsigned*)(lds + L_CNT + 4 * cnt_idx), 1u, __ATOMIC_RELAXED, __HIP_MEMORY_SCOPE_WORKGROUP);
        if (__builtin_amdgcn_readfirstlane(old) == 3 && lane == 0) __hip_atomic_store((GAS u64*)flag, tag, RLX_AGENT); };
    const unsigned krd = (unsigned)(256 * r32 + 16 * (hi ^ (r32 & 15)));
    bf16x8 qf[8];
    const unsigned fx = (unsigned)((pair * 4 + rg) * 8192 + lane * 16);
    if (roleB) {
        const int p4s = ((p4 & 1) << 1) | (p4 >> 1);
        unsigned wrd[4];
#pragma unroll
        for (int dt = 0; dt < 4; ++dt) wrd[dt] = (unsigned)((rg >> 1) * 256 + 512 * (8 * hi + q4) + 16 * ((4 * dt + 2 * blk + (p4s >> 1)) ^ (q4 << 2)) + 8 * (p4s & 1));
        f32x16 acc[4] = {};
        bf16x8 xf[4], xn[4] = {}, wf[8];
        unsigned xa[4];
        { const int row = (rg & 1) * 32 + r32;
#pragma unroll
          for (int s = 0; s < 4; ++s) xa[s] = (unsigned)(row * 128 + 16 * ((2 * s + hi) ^ ((row >> 1) & 7))); }
#define WRD(i, wo) wf[i] = trrd2(LP((wo) + wrd[(i) & 3]) + ((i) >> 2) * 8192, LP((wo) + wrd[(i) & 3]) + ((i) >> 2) * 8192 + 2048)
#define QG(i, wi, wo, X) do { acc[(i) & 3] = MFMA32(wf[i], xf[2 * (wi) + ((i) >> 2)], acc[(i) & 3]); PIN(acc[(i) & 3]); SBAR(); WRD(i, wo); X; SBAR(); } while (0)
#define XRD(s, xo) ldsrd16(LP((xo) + xa[s]))
        { const unsigned xo = acquire();
#pragma unroll
          for (int s = 0; s < 4; ++s) xf[s] = XRD(s, xo);
          const unsigned wo = acquire();
#pragma unroll
          for (int i = 0; i < 8; ++i) WRD(i, wo); }
#pragma nounroll
        for (int gi = 0; gi < 9; ++gi) {
            unsigned wo = acquire(); SBAR();
            QG(0, 0, wo, (void)0); QG(1, 0, wo, (void)0); QG(2, 0, wo, (void)0); QG(3, 0, wo, (void)0); QG(4, 0, wo, (void)0); QG(5, 0, wo, (void)0); QG(6, 0, wo, (void)0); QG(7, 0, wo, (void)0);
            release(u - 1);
            unsigned xo = lds0; wo = lds0; const int urel = u;
            if (gi < 8) { xo = acquire(); wo = acquire(); }
            SBAR();
            QG(0, 1, wo, xn[0] = XRD(0, xo)); QG(1, 1, wo, xn[1] = XRD(1, xo)); QG(2, 1, wo, (void)0); QG(3, 1, wo, (void)0);
            QG(4, 1, wo, xn[2] = XRD(2, xo)); QG(5, 1, wo, (void)0); QG(6, 1, wo, (void)0); QG(7, 1, wo, xn[3] = XRD(3, xo));
            release(urel);
            xf[0] = xn[0]; xf[1] = xn[1]; xf[2] = xn[2]; xf[3] = xn[3];
        }
        STAMP(1);
        tag = next_tag(rfl64(epoch));
        *(volatile LAS u64*)(lds + L_TAG) = tag;
#pragma unroll
        for (int dt = 0; dt < 4; ++dt)
#pragma unroll
            for (int s = 0; s < 2; ++s) { const f32x16& a = acc[dt];
                u32x4 w = {pk2(a[8 * s] * QSCALE, a[8 * s + 1] * QSCALE), pk2(a[8 * s + 2] * QSCALE, a[8 * s + 3] * QSCALE), pk2(a[8 * s + 4] * QSCALE, a[8 * s + 5] * QSCALE), pk2(a[8 * s + 6] * QSCALE, a[8 * s + 7] * QSCALE)};
                qf[2 * dt + s] = __builtin_bit_cast(bf16x8, w);
                __builtin_amdgcn_raw_buffer_store_b128(w, wsr, (unsigned)WS_QX + fx + (2 * dt + s) * 1024, 0, 16); }
    } else {
        tag = next_tag(rfl64(epoch));
        poll_flag(ctl + 256, tag, budget);
        STAMP(1);
#pragma unroll
        for (int s = 0; s < 8; ++s) qf[s] = __builtin_bit_cast(bf16x8, __builtin_amdgcn_raw_buffer_load_b128(wsr, (unsigned)WS_QX + fx + s * 1024, 0, 16));
    }
    STAMP(2);
    unsigned vrd[4];
#pragma unroll
    for (int c = 0; c < 4; ++c) vrd[c] = (unsigned)(256 * (4 * hi + q4) + 16 * ((4 * c + 2 * blk + (p4 >> 1)) ^ (q4 << 2)) + 8 * (p4 & 1));
    f32x16 O0 = {}, O1 = {}, O2 = {}, O3 = {}; float lsum = 0.f, mrow; f32x16 negm, SA, SB; bf16x8 kf[8]; u32x4 pw0, pw1;
#define R_O0 "{v[192:207]}"
#define R_O1 "{v[208:223]}"
#define R_O2 "{v[224:239]}"
#define R_O3 "{v[240:255]}"
#define R_SA "{v[176:191]}"
#define R_SB "{v[160:175]}"
#define N_SA "176"
#define N_SB "160"
#define R_NM "{v[144:159]}"
#define PINR(x) asm volatile("" : "+" R_##x(x))
#define EXP2(x) __builtin_amdgcn_exp2f(x)
#define MX3(a, b, c) __builtin_fmaxf(__builtin_fmaxf((a), (b)), (c))
#define KRD(s, off) kf[s] = ldsrd16(LP(ka[s]) + (off))
#define VRD(g, off) vf[g] = trrd2(lds + va[(g) & 3] + (off) + ((g) >> 2) * 4096, lds + va[(g) & 3] + (off) + ((g) >> 2) * 4096 + 2048)
#define VRDH(g, t, off) vh[2 * (g) + (t)] = trrd(LP(va[(g) & 3]) + (off) + ((g) >> 2) * 4096 + (t) * 2048)
#define VF(g) __builtin_shufflevector(vh[2 * (g)], vh[2 * (g) + 1], 0, 1, 2, 3, 4, 5, 6, 7)

#define KADDR(s, o) do { ka[s] = (krd ^ ((s) << 5)) + (o); PIN(ka[s]); } while (0)
#define VADDR(c, o) do { va[c] = vrd[c] + (o); PIN(va[c]); } while (0)
#define RESCALE(S, mx, mxb, thr) do { float t0_; asm volatile( \
        "v_max_f32 %8, %8, %11\n\tv_cmp_lt_f32 vcc, %10, %8\n\ts_cbranch_vccnz .Lrs%=\n" \
        ".Lrb%=:\n\t.subsection 1\n" \
        ".Lrs%=:\n\ts_nop 15\n\tv_mov_b32 %9, %8\n\ts_nop 1\n\tv_permlane32_swap_b32 %8, %9\n\ts_nop 1\n\tv_max3_f32 %8, %8, %9, 0\n\t" \
        "v_add_f32 %6, %6, %8\n\tv_exp_f32 %9, -%8\n\t" \
        ".set r_, 0\n\t.rept 16\n\tv_sub_f32 v[" N_##S "+r_], v[" N_##S "+r_], %8\n\tv_sub_f32 v[144+r_], 0, %6\n\t.set r_, r_+1\n\t.endr\n\t" \
        "v_mul_f32 %7, %7, %9\n\t" \
        ".set r_, 192\n\t.rept 64\n\tv_mul_f32 v[r_], v[r_], %9\n\t.set r_, r_+1\n\t.endr\n\ts_nop 1\n\ts_branch .Lrb%=\n\t.subsection 0" \
        : "+" R_O0(O0), "+" R_O1(O1), "+" R_O2(O2), "+" R_O3(O3), "+" R_##S(S), "+" R_NM(negm), "+v"(mrow), "+v"(lsum), "+v"(mx), "=&v"(t0_) : "s"(thr), "v"(mxb) : "vcc"); } while (0)
#define G1(SN, SO, s, vo, CIN, PK) do { SN = MFMA32(kf[s], qf[s], CIN); PINR(SN); SBAR(); PINR(SO); VRDH((s) >> 1, (s) & 1, vo); SO[2 * (s)] = EXP2(SO[2 * (s)]); SO[2 * (s) + 1] = EXP2(SO[2 * (s) + 1]); PK; PINR(SO); SBAR(); } while (0)
#define PKW(SO, W, i, r) do { W[i] = pk2(SO[r], SO[(r) + 1]); PIN(W); } while (0)
#define PH1(SN, SO, vo) do { SBAR(); LGKM(4); \
    G1(SN, SO, 0, vo, negm, (void)0);            G1(SN, SO, 1, vo, SN, PKW(SO, pw0, 0, 0)); G1(SN, SO, 2, vo, SN, PKW(SO, pw0, 1, 2));  G1(SN, SO, 3, vo, SN, PKW(SO, pw0, 2, 4)); LGKM(4); \
    G1(SN, SO, 4, vo, SN, PKW(SO, pw0, 3, 6));   G1(SN, SO, 5, vo, SN, PKW(SO, pw1, 0, 8)); G1(SN, SO, 6, vo, SN, PKW(SO, pw1, 1, 10)); G1(SN, SO, 7, vo, SN, PKW(SO, pw1, 2, 12)); } while (0)
#define G2(g, OC, W, X) do { OC = MFMA32(VF(g), __builtin_bit_cast(bf16x8, W), OC); PINR(OC); SBAR(); X; SBAR(); } while (0)
#define ADD4(SO, r) do { ls0 += SO[r]; ls1 += SO[(r) + 1]; ls0 += SO[(r) + 2]; ls1 += SO[(r) + 3]; PIN(ls0); PIN(ls1); } while (0)
#define V2(g, vo) do { VRDH(g, 0, vo); VRDH(g, 1, vo); } while (0)
#define K2(s, ko) do { KRD(s, ko); KRD((s) + 1, ko); } while (0)
#define PH2A(SN, SO, vo) SBAR(); LGKM(4); \
    G2(0, O0, pw0, { V2(4, vo); PKW(SO, pw1, 3, 14); ls0 = lsum + SO[0]; ls1 = SO[1] + SO[3]; ls0 += SO[2]; PIN(ls0); PIN(ls1); }); G2(1, O1, pw0, { V2(5, vo); ADD4(SO, 4); }); LGKM(4); \
    G2(2, O2, pw0, { V2(6, vo); ADD4(SO, 8); }); G2(3, O3, pw0, { V2(7, vo); ADD4(SO, 12); });
#define PH2B(SN, SO, ko, A0, A1, A2, A3) SBAR(); LGKM(4); \
    G2(4, O0, pw1, { A0; K2(0, ko); mx0 = MX3(SN[0], SN[1], SN[2]); mx1 = MX3(SN[3], SN[4], SN[5]); lsum = ls0 + ls1; PIN(mx0); PIN(mx1); PIN(lsum); }); \
    G2(5, O1, pw1, { A1; K2(2, ko); mx0 = MX3(mx0, SN[6], SN[7]); mx1 = MX3(mx1, SN[8], SN[9]); PIN(mx0); PIN(mx1); }); LGKM(4); \
    G2(6, O2, pw1, { A2; K2(4, ko); mx0 = MX3(mx0, SN[10], SN[11]); mx1 = MX3(mx1, SN[12], SN[13]); PIN(mx0); PIN(mx1); }); \
    G2(7, O3, pw1, { A3; K2(6, ko); mx0 = MX3(mx0, SN[14], SN[15]); }); \
    RESCALE(SN, mx0, mx1, THR)
    unsigned ka[8], va[4]; s16x4 vh[16]; float ls0, ls1, mx0, mx1;
    {
        const unsigned ko = acquire();
#pragma unroll
        for (int s = 0; s < 8; ++s) { KADDR(s, ko); KRD(s, 0); }
        SA = MFMA32(kf[0], qf[0], (f32x16){});
#pragma unroll
        for (int s = 1; s < 8; ++s) SA = MFMA32(kf[s], qf[s], SA);
#pragma unroll
        for (int s = 0; s < 8; ++s) KRD(s, 8192);
        float mx = MX3(SA[0], SA[1], SA[2]);
#pragma unroll
        for (int r = 3; r < 15; r += 2) mx = MX3(mx, SA[r], SA[r + 1]);
        mx = swap_max(__builtin_fmaxf(mx, SA[15])); mrow = mx;
#pragma unroll
        for (int r = 0; r < 16; ++r) { SA[r] -= mx; negm[r] = -mx; }
        asm volatile("" : "+" R_NM(negm));
    }
    if (roleB) signoff(0, ctl + 256);
    STAMP(6);
#define FOLD1(OC, f, ob) do { const u32x4 v_ = *(LAS const u32x4*)(LP(ob) + rg * 4096 + ((f) & 3) * 1024 + lane * 16); _Pragma("unroll") for (int e = 0; e < 4; ++e) { \
        OC[8 * ((f) & 1) + 2 * e] += w_ * __uint_as_float(v_[e] << 16); OC[8 * ((f) & 1) + 2 * e + 1] += w_ * __uint_as_float(v_[e] & 0xffff0000u); } PINR(OC); } while (0)
#define FOLD() do { const unsigned o0_ = acquire(), o1_ = acquire(); const float lse_ = *(LAS const float*)(lds + L_LSE + rg * 256 + lane * 4); float d_ = lse_ - mrow; \
        const float d2_ = d_; RESCALE(SA, d_, d2_, 64.f); const float w_ = EXP2(lse_ - mrow); lsum += 0.5f * w_; \
        FOLD1(O0, 0, o0_); FOLD1(O0, 1, o0_); FOLD1(O1, 2, o0_); FOLD1(O1, 3, o0_); FOLD1(O2, 4, o1_); FOLD1(O2, 5, o1_); FOLD1(O3, 6, o1_); FOLD1(O3, 7, o1_); } while (0)
#define NOP_ (void)0
#pragma nounroll
    for (int t = 0; t < NT - 1; ++t) {
        { const unsigned vo = acquire(); VADDR(0, vo); VADDR(1, vo); VADDR(2, vo); VADDR(3, vo); }
        STAMP2(8 + t);
        PH1(SB, SA, 0);
        release(u - 1);
        PH2A(SB, SA, 0);
        const unsigned ko = acquire();
        PH2B(SB, SA, 0, { KADDR(0, ko); KADDR(1, ko); }, { KADDR(2, ko); KADDR(3, ko); }, { KADDR(4, ko); KADDR(5, ko); }, { KADDR(6, ko); KADDR(7, ko); });
        PH1(SA, SB, 8192);
        PH2A(SA, SB, 8192);
        PH2B(SA, SB, 8192, NOP_, NOP_, NOP_, NOP_);
        release(u - 1);
    }
    { const unsigned vo = acquire(); VADDR(0, vo); VADDR(1, vo); VADDR(2, vo); VADDR(3, vo); }
    STAMP(8 + NT - 1);
    PH1(SB, SA, 0);
    PH2A(SB, SA, 0);
    PH2B(SB, SA, 0, NOP_, NOP_, NOP_, NOP_);
    {
#pragma unroll
        for (int g = 0; g < 8; ++g) V2(g, 8192);
#pragma unroll
        for (int r = 0; r < 16; ++r) { SB[r] = EXP2(SB[r]); lsum += SB[r]; }
        pw0 = (u32x4){pk2(SB[0], SB[1]), pk2(SB[2], SB[3]), pk2(SB[4], SB[5]), pk2(SB[6], SB[7])};
        pw1 = (u32x4){pk2(SB[8], SB[9]), pk2(SB[10], SB[11]), pk2(SB[12], SB[13]), pk2(SB[14], SB[15])};
        const bf16x8 p0 = __builtin_bit_cast(bf16x8, pw0), p1 = __builtin_bit_cast(bf16x8, pw1);
        O0 = MFMA32(VF(0), p0, O0); O1 = MFMA32(VF(1), p0, O1); O2 = MFMA32(VF(2), p0, O2); O3 = MFMA32(VF(3), p0, O3);
        O0 = MFMA32(VF(4), p1, O0); O1 = MFMA32(VF(5), p1, O1); O2 = MFMA32(VF(6), p1, O2); O3 = MFMA32(VF(7), p1, O3);
    }
    STAMP(3);
    release(u);
    if (roleB) FOLD();
    STAMP(5);
    lsum = swap_add(lsum);
    const float inv = __builtin_amdgcn_rcpf(lsum);
    const unsigned dst = (unsigned)(roleB ? WS_OX : WS_PX) + fx;
#pragma unroll
    for (int f = 0; f < 8; ++f) { const f32x16& a = f < 2 ? O0 : f < 4 ? O1 : f < 6 ? O2 : O3; const int r = 8 * (f & 1);
        u32x4 w = {pk2(a[r] * inv, a[r + 1] * inv), pk2(a[r + 2] * inv, a[r + 3] * inv), pk2(a[r + 4] * inv, a[r + 5] * inv), pk2(a[r + 6] * inv, a[r + 7] * inv)};
        __builtin_amdgcn_raw_buffer_store_b128(w, wsr, dst + f * 1024, 0, 16); }
    if (!roleB) __builtin_amdgcn_raw_buffer_store_b32(__float_as_uint(mrow + __builtin_amdgcn_logf(lsum)), wsr, (unsigned)WS_PL + (pair * 4 + rg) * 256 + lane * 4, 0, 16);
    if (roleB) release(u);
    signoff(1, roleB ? ctl + 768 : ctl + 512);
    STAMP(4);
    {
        const int qh = rg & 1, kh2 = rg >> 1;
        const int pr2 = b * HKV + ks * 2 + kh2;
        u += NSL - 1; (void)acquire();
        STAMP(7);
        unsigned wa[2], wh[2];
#pragma unroll
        for (int hl = 0; hl < 2; ++hl) { wa[hl] = lds0 + (kh2 * 64 + hl * 32 + hi) * 1152 + 64 * q4 + 32 * blk + 8 * p4; wh[hl] = lds0 + (kh2 * 64 + hl * 32 + hi) * 1152 + 1024 + 32 * q4 + 128 * blk + 8 * p4; PIN(wa[hl]); PIN(wh[hl]); }
#define ARD(i) ((i) % 5 < 4 ? trrd2(LP(wa[(i) / 40]) + (((i) / 5) & 7) * 4608 + ((i) % 5) * 256, LP(wa[(i) / 40]) + (((i) / 5) & 7) * 4608 + 2304 + ((i) % 5) * 256) \
                            : trrd2(LP(wh[(i) / 40]) + (((i) / 5) & 7) * 4608, LP(wh[(i) / 40]) + (((i) / 5) & 7) * 4608 + 2304))
        constexpr int AD = 7;
        bf16x8 af[AD];
#pragma unroll
        for (int i = 0; i < AD; ++i) af[i] = ARD(i);
        poll_flag(ws + WS_CT + (size_t)pr2 * 1024 + 768, tag, budget);
        STAMP(40);
        bf16x8 of[16];
#pragma unroll
        for (int f = 0; f < 16; ++f) of[f] = __builtin_bit_cast(bf16x8, __builtin_amdgcn_raw_buffer_load_b128(wsr, (unsigned)WS_OX + (pr2 * 4 + (f >> 3) * 2 + qh) * 8192 + (f & 7) * 1024 + lane * 16, 0, 16));
        f32x16 acc[5] = {};
        SBAR();
#pragma unroll
        for (int i = 0; i < 80; ++i) { acc[i % 5] = MFMA32(af[i % AD], of[i / 5], acc[i % 5]); PIN(acc[i % 5]); SBAR(); if (i + AD < 80) af[i % AD] = ARD(i + AD); SBAR(); }
        STAMP(41);
        WAITBAR(63);
#define QUAD(k) ((f32x4){acc[(k) >> 2][4 * ((k) & 3)], acc[(k) >> 2][4 * ((k) & 3) + 1], acc[(k) >> 2][4 * ((k) & 3) + 2], acc[(k) >> 2][4 * ((k) & 3) + 3]})
        f32x4 keep[9];
#pragma unroll
        for (int k = 0; k < 9; ++k) { const f32x4 lo = QUAD(k), hi4 = QUAD(9 + k); keep[k] = kh2 ? hi4 : lo; *(LAS f32x4*)(lds + rg * 9216 + k * 1024 + lane * 16) = kh2 ? lo : hi4; }
        WAITBAR(63);
        const unsigned sb = (unsigned)WS_SLAB + ((b * 4 + nq) * 8 + (r32 >> 2)) * 36864 + ks * 4608 + (qh * 18 + kh2 * 9) * 128 + ((lane & 3) * 2 + hi) * 16;
#pragma unroll
        for (int k = 0; k < 9; ++k) { const f32x4 v = keep[k] + *(LAS const f32x4*)(lds + (rg ^ 2) * 9216 + k * 1024 + lane * 16);
            __builtin_amdgcn_raw_buffer_store_b128(__builtin_bit_cast(u32x4, v), wsr, sb + k * 128, 0, 16); }
        STAMP(42);
        signoff(2, fsl + ks * 256);
        STAMP(43);
        if (rg == 0) {
#pragma nounroll
            while (!__all(__hip_atomic_load((GAS const u64*)(fsl + (lane & 7) * 256), RLX_AGENT) == tag) && --budget > 0) __builtin_amdgcn_s_sleep(1);
            if (ks == 0 && nq == 0 && lane == 0) __hip_atomic_store((GAS u64*)epo, tag, RLX_AGENT);
        }
        WAITBAR(63);
        STAMP(44);
    }
    }
    if (tid < 288) {
        const unsigned sb = (unsigned)WS_SLAB + ((b * 4 + nq) * 8 + ks) * 36864 + tid * 16;
        u32x4 v[8];
#pragma unroll
        for (int k = 0; k < 8; ++k) v[k] = __builtin_amdgcn_raw_buffer_load_b128(wsr, sb + k * 4608, 0, 16);
        f32x4 s = __builtin_bit_cast(f32x4, v[0]);
#pragma unroll
        for (int k = 1; k < 8; ++k) s += __builtin_bit_cast(f32x4, v[k]);
        const int unit = tid >> 3, qh = unit >= 18, quad = unit - 18 * qh;
        *(u32x2*)(out + ((size_t)(b * SQ + qh * 32 + ks * 4 + ((tid >> 1) & 3)) * DM + nq * 144 + quad * 8 + (tid & 1) * 4) * 2) = (u32x2){pk2(s[0], s[1]), pk2(s[2], s[3])};
    }
    STAMP(45);
    STAMP_OUT(0, 0);
}

extern "C" void kernel_launch(void* const* d_in, const int* in_sizes, int n_in, void* d_out, int out_size, void* d_ws, size_t ws_size, hipStream_t stream) {
    (void)hipFuncSetAttribute((const void*)attn_k, hipFuncAttributeMaxDynamicSharedMemorySize, LDS1);
    hipLaunchKernelGGL(attn_k, dim3(256), dim3(512), LDS1, stream, (const char*)d_in[0], (const char*)d_in[1], (const char*)d_in[2], (const char*)d_in[3], (const char*)d_in[4], (char*)d_out, (char*)d_ws);
}
```

```cpp
#include <hip/hip_runtime.h>
#include <hip/hip_bf16.h>
#include <stdint.h>

typedef short bf16x8 __attribute__((ext_vector_type(8)));
typedef short s16x4 __attribute__((ext_vector_type(4)));
typedef float f32x16 __attribute__((ext_vector_type(16)));
typedef float f32x4 __attribute__((ext_vector_type(4)));
typedef float f32x2 __attribute__((ext_vector_type(2)));
typedef unsigned u32x4 __attribute__((ext_vector_type(4)));
typedef unsigned u32x2 __attribute__((ext_vector_type(2)));
typedef __bf16 bf2_t __attribute__((ext_vector_type(2)));
typedef unsigned long long u64;
#define LAS __attribute__((address_space(3)))
#define GAS __attribute__((address_space(1)))
typedef LAS const char* lcptr;
typedef LAS char* lptr;
#define DI __device__ __forceinline__
#define MFMA32(a, b, c) __builtin_amdgcn_mfma_f32_32x32x16_bf16((a), (b), (c), 0, 0, 0)
#define MFMA16(a, b, c) __builtin_amdgcn_mfma_f32_16x16x32_bf16((a), (b), (c), 0, 0, 0)
#define WAITBAR(N) asm volatile("s_waitcnt vmcnt(" #N ") lgkmcnt(0)\n\ts_barrier" ::: "memory")
#define WAITV(N) asm volatile("s_waitcnt vmcnt(" #N ")" ::: "memory")
#define PIN(x) asm volatile("" : "+v"(x))
#define SBAR() __builtin_amdgcn_sched_barrier(0)
#define LGKM(n) do { __builtin_amdgcn_s_waitcnt(0xC07F | ((n) << 8)); SBAR(); } while (0)
#define LP(a) ((lcptr)(uintptr_t)(a))
#define STR2(x) #x
#define STR(x) STR2(x)
#define CLOB_40_255 "v40", "v41", "v42", "v43", "v44", "v45", "v46", "v47", "v48", "v49", "v50", "v51", "v52", "v53", "v54", "v55", "v56", "v57", "v58", "v59", "v60", "v61", "v62", "v63", "v64", "v65", "v66", "v67", "v68", "v69", "v70", "v71", "v72", "v73", "v74", "v75", "v76", "v77", "v78", "v79", "v80", "v81", "v82", "v83", "v84", "v85", "v86", "v87", "v88", "v89", "v90", "v91", "v92", "v93", "v94", "v95", "v96", "v97", "v98", "v99", "v100", "v101", "v102", "v103", "v104", "v105", "v106", "v107", "v108", "v109", "v110", "v111", "v112", "v113", "v114", "v115", "v116", "v117", "v118", "v119", "v120", "v121", "v122", "v123", "v124", "v125", "v126", "v127", "v128", "v129", "v130", "v131", "v132", "v133", "v134", "v135", "v136", "v137", "v138", "v139", "v140", "v141", "v142", "v143", "v144", "v145", "v146", "v147", "v148", "v149", "v150", "v151", "v152", "v153", "v154", "v155", "v156", "v157", "v158", "v159", "v160", "v161", "v162", "v163", "v164", "v165", "v166", "v167", "v168", "v169", "v170", "v171", "v172", "v173", "v174", "v175", "v176", "v177", "v178", "v179", "v180", "v181", "v182", "v183", "v184", "v185", "v186", "v187", "v188", "v189", "v190", "v191", "v192", "v193", "v194", "v195", "v196", "v197", "v198", "v199", "v200", "v201", "v202", "v203", "v204", "v205", "v206", "v207", "v208", "v209", "v210", "v211", "v212", "v213", "v214", "v215", "v216", "v217", "v218", "v219", "v220", "v221", "v222", "v223", "v224", "v225", "v226", "v227", "v228", "v229", "v230", "v231", "v232", "v233", "v234", "v235", "v236", "v237", "v238", "v239", "v240", "v241", "v242", "v243", "v244", "v245", "v246", "v247", "v248", "v249", "v250", "v251", "v252", "v253", "v254", "v255"
#define RLX_AGENT __ATOMIC_RELAXED, __HIP_MEMORY_SCOPE_AGENT

constexpr int NB = 8, SQ = 64, DM = 576, HQ = 32, HKV = 16, SK = 2048, DH = 128;
constexpr float QSCALE = 0.08838834764831845f * 1.4426950408889634f;
constexpr float THR = 8.f;
#define TOUCH 2
#define TPOL " nt"
#define CPRIO 2
#define LPRIO 0
#define LSLEEP 1
#define QW 10
#define CTOUCH 1
#define HPRE0 0
#define HPRE 2
#define NBUF 13
#define PENDB 10
#define REGMAX GATEA
#define PEND 9
#define FSLEEP 2
#define PACE 2
#define PA0 125
#define PA1 5
#define PB0 100
#define PB1 0
#define PMIN 100
#define WARM 0
#define WT0 200
#define WT1 600
#define WMAX 10
#define LATESIGN 1
#define WTA 10
#define WTB 6
#define TBPOL " nt"
#define TB64
#define TBN 0
#define TOUCHB
#define CSLEEP "s_sleep 1\n\t"
#define NOWO 0
#define NQROT 1
#define LWIN 3
#define GATEA 11
#define NTA 17
#define TPOFF 2
constexpr int NT_A = NTA, NT_B = 32 - NTA, TP = NT_B - TPOFF;
constexpr int USLOT = 16384, NSL = 9, MISC = NSL * USLOT, LDS1 = 163840;
constexpr int L_LANDED = MISC, L_DONE = MISC + 16, L_CNT = MISC + 32, L_TAG = MISC + 64, L_LSE = MISC + 256;
constexpr int NQU = 23;
constexpr int SPIN_CAP = 1 << 17;
constexpr size_t WS_CT = 0;
constexpr size_t WS_EP = 128 * 1024, WS_FS = WS_EP + 8 * 256;
constexpr size_t WS_QX = 1 << 20;
constexpr size_t WS_PX = WS_QX + ((size_t)4 << 20);
constexpr size_t WS_PL = WS_PX + ((size_t)4 << 20);
constexpr size_t WS_OX = WS_PL + ((size_t)1 << 20);
constexpr size_t WS_SLAB = WS_OX + ((size_t)4 << 20);
constexpr int WS_USED = 32 << 20;

#define STAMP3(i)
#define STAMP2(i)
#define STAMP_DECL
#define STAMP(i)
#define STAMP_OUT(k, w)
#define LSTAMP
#define LSTAMP_OP
DI unsigned pk2(float a, float b) { bf2_t r = __builtin_convertvector((f32x2){a, b}, bf2_t); return __builtin_bit_cast(unsigned, r); }
DI s16x4 trrd(lcptr p) { typedef short v4i16_t __attribute__((ext_vector_type(4))); return __builtin_bit_cast(s16x4, __builtin_amdgcn_ds_read_tr16_b64_v4i16((LAS v4i16_t*)p)); }
DI bf16x8 trrd2(lcptr p0, lcptr p1) { s16x4 a = trrd(p0), b = trrd(p1); return __builtin_shufflevector(a, b, 0, 1, 2, 3, 4, 5, 6, 7); }
DI bf16x8 ldsrd16(lcptr p) { return *(LAS const bf16x8*)p; }
DI float swap_max(float m) { auto rr = __builtin_amdgcn_permlane32_swap(__float_as_uint(m), __float_as_uint(m), false, false); return __builtin_fmaxf(__uint_as_float(rr[0]), __uint_as_float(rr[1])); }
DI float swap_add(float m) { auto rr = __builtin_amdgcn_permlane32_swap(__float_as_uint(m), __float_as_uint(m), false, false); return __uint_as_float(rr[0]) + __uint_as_float(rr[1]); }
DI u64 next_tag(u64 e) { return e * 6364136223846793005ull + 1442695040888963407ull; }
DI u64 rfl64(u64 v) { return ((u64)(unsigned)__builtin_amdgcn_readfirstlane((unsigned)(v >> 32)) << 32) | (unsigned)__builtin_amdgcn_readfirstlane((unsigned)v); }
DI int lds_min4(unsigned a) { u32x4 v; asm volatile("ds_read_b128 %0, %1\n\ts_waitcnt lgkmcnt(0)" : "=v"(v) : "v"(a) : "memory");
    unsigned m = v[0] < v[1] ? v[0] : v[1], n = v[2] < v[3] ? v[2] : v[3]; return __builtin_amdgcn_readfirstlane(m < n ? m : n); }
DI void lds_put(unsigned a, unsigned val) { asm volatile("ds_write_b32 %0, %1" :: "v"(a), "v"(val) : "memory"); }
DI void poll_flag(const char* p, u64 tag, int& budget) {
#pragma nounroll
    while (rfl64(__hip_atomic_load((GAS const u64*)p, RLX_AGENT)) != tag && --budget > 0) __builtin_amdgcn_s_sleep(1);
    asm volatile("" ::: "memory"); }

#define DMA4(POL, sbase, v0, v1, v2, v3, ldst) do { unsigned sv_; asm volatile("s_mov_b32 %0, m0\n\ts_mov_b32 m0, %6\n\ts_nop 0\n\t" \
    "global_load_lds_dwordx4 %1, %5" POL "\n\tglobal_load_lds_dwordx4 %2, %5 offset:1024" POL "\n\tglobal_load_lds_dwordx4 %3, %5 offset:2048" POL "\n\tglobal_load_lds_dwordx4 %4, %5 offset:3072" POL "\n\t" \
    "s_mov_b32 m0, %0" : "=&s"(sv_) : "v"(v0), "v"(v1), "v"(v2), "v"(v3), "s"(sbase), "s"(__builtin_amdgcn_readfirstlane(ldst)) : "memory"); } while (0)
#define DMA2(sbase, v0, v1, ldst) do { unsigned sv_; asm volatile("s_mov_b32 %0, m0\n\ts_mov_b32 m0, %4\n\ts_nop 0\n\t" \
    "global_load_lds_dwordx4 %1, %3\n\tglobal_load_lds_dwordx4 %2, %3 offset:1024\n\t" \
    "s_mov_b32 m0, %0" : "=&s"(sv_) : "v"(v0), "v"(v1), "s"(sbase), "s"(__builtin_amdgcn_readfirstlane(ldst)) : "memory"); } while (0)
DI void dma_def(const void* sbase, unsigned voff, unsigned ldst) {
    unsigned sv; asm volatile("s_mov_b32 %0, m0\n\ts_mov_b32 m0, %3\n\ts_nop 0\n\tglobal_load_lds_dwordx4 %1, %2\n\ts_mov_b32 m0, %0" : "=&s"(sv) : "v"(voff), "s"(sbase), "s"(__builtin_amdgcn_readfirstlane(ldst)) : "memory"); }

__global__ __launch_bounds__(512, 2) void attn_k(const char* __restrict__ X, const char* __restrict__ Kc, const char* __restrict__ Vc, const char* __restrict__ Wq, const char* __restrict__ Wo, char* __restrict__ out, char* __restrict__ ws) {
    extern __shared__ __attribute__((aligned(16))) char lds_[];
    const int tid = threadIdx.x, lane = tid & 63, r32 = lane & 31, hi = lane >> 5;
    const int wid = __builtin_amdgcn_readfirstlane(tid >> 6);
    const int bid = blockIdx.x, xcd = bid & 7, idx = bid >> 3;
    const int kvh = xcd * 2 + (idx & 1), b = (idx >> 1) & 7;
    const bool roleB = idx < 16;
    const int pair = b * HKV + kvh;
    const lptr lds = (lptr)lds_;
    const unsigned lds0 = (unsigned)(uintptr_t)lds;
    const int NT = roleB ? NT_B : NT_A;
    const int rot = b;
    char* const ctl = ws + WS_CT + (size_t)pair * 1024;
    char* const epo = ws + WS_EP + b * 256;
    const int ks = xcd, nq = ((idx & 1) + (roleB ? 0 : 2) + (NQROT ? b : 0)) & 3;
    char* const fsl = ws + WS_FS + (size_t)(b * 4 + nq) * 8 * 256;
    const __amdgpu_buffer_rsrc_t wsr = __builtin_amdgcn_make_buffer_rsrc(ws, 0, WS_USED, 0x00020000);
    STAMP_DECL; STAMP(0);
    u64 pc0; unsigned code_len;
    asm volatile("s_getpc_b64 %0\n.Lcs%=:\n\ts_mov_b32 %1, .Lce%=-.Lcs%=\n\t.subsection 2\n.Lce%=:\n\t.fill 128, 4, 0xBF800000\n\t.subsection 0" : "=s"(pc0), "=s"(code_len));
    if (tid < 16) *(LAS unsigned*)(lds + MISC + tid * 4) = 0u;
    WAITBAR(63);
    int budget = SPIN_CAP;
    u64 tagf;

    if (wid >= 4) {
        const int j = wid - 4, lr = lane >> 4, lc = lane & 15;
        __builtin_amdgcn_s_setprio(LPRIO);
        const size_t koff = ((size_t)pair * SK + (roleB ? NT_A * 64 : 0)) * 256;
        const char* kp = Kc + koff; const char* vp = Vc + koff;
        unsigned vK[4];
#pragma unroll
        for (int i = 0; i < 4; ++i) vK[i] = (unsigned)((16 * j + lr) * 256 + 16 * (lc ^ (4 * i + lr)));
        const unsigned vV = (unsigned)((16 * j + lr) * 256 + 16 * (lc ^ (lr << 2)));
        int u = 0, mind = 0;
        unsigned so = lds0;
        unsigned a_landed = lds0 + L_LANDED + 4 * j, a_done = lds0 + L_DONE; PIN(a_landed); PIN(a_done);
        if (roleB) {
            unsigned vX[2], vW[4];
#pragma unroll
            for (int i = 0; i < 2; ++i) { const int row = 16 * j + 8 * i + (lane >> 3); vX[i] = (unsigned)(row * 1152 + 16 * ((lane & 7) ^ ((row >> 1) & 7)) - 1024 * i); }
#pragma unroll
            for (int i = 0; i < 4; ++i) vW[i] = (unsigned)((8 * j + 2 * i + hi) * 8192 + 16 * (r32 ^ (((2 * i + hi) & 3) << 2)) - 1024 * i);
            const char* xb = X + (size_t)b * SQ * DM * 2; const char* wb = Wq + kvh * 512;
            int xo = rot * 128, sb = 0, cnt = 7;
#define QADV "s_add_i32 %[xo], %[xo], 128\n\ts_cmp_eq_u32 %[xo], 1152\n\ts_cselect_b32 %[xo], 0, %[xo]\n\ts_add_i32 %[sb], %[sb], 0xc000\n\ts_cmp_eq_u32 %[sb], 0x24000\n\ts_cselect_b32 %[sb], 0, %[sb]\n\t"
#define QW4 "global_load_lds_dwordx4 %[w0], s[60:61]\n\tglobal_load_lds_dwordx4 %[w1], s[60:61] offset:1024\n\tglobal_load_lds_dwordx4 %[w2], s[60:61] offset:2048\n\tglobal_load_lds_dwordx4 %[w3], s[60:61] offset:3072\n\t"
#define QEVEN0 "s_add_u32 s62, %[xl], %[xo]\n\ts_addc_u32 s63, %[xh], 0\n\ts_lshl_b32 s66, %[xo], 12\n\ts_add_u32 s60, %[wl], s66\n\ts_addc_u32 s61, %[wh], 0\n\ts_add_i32 m0, %[sb], %[dx]\n\ts_add_i32 s66, %[sb], %[dw]\n\t"
#define QEVEN1 "global_load_lds_dwordx4 %[x0], s[62:63]\n\tglobal_load_lds_dwordx4 %[x1], s[62:63] offset:1024\n\ts_mov_b32 m0, s66\n\ts_nop 0\n\t" QW4
#define QODD0 "s_add_u32 s60, s60, 0x40000\n\ts_addc_u32 s61, s61, 0\n\ts_add_i32 m0, m0, 0x4000\n\t"
            asm volatile(
                "s_mov_b32 s70, m0\n\t"
                QEVEN0 QEVEN1 QODD0 "s_nop 0\n\t" QW4 QADV QEVEN0 QEVEN1 QODD0
                "s_waitcnt vmcnt(10)\n"
                ".Lqr%=:\n\t"
                "s_barrier\n\t" QW4 QADV QEVEN0 "s_waitcnt vmcnt(10)\n\t"
                "s_barrier\n\t" QEVEN1 QODD0 "s_sub_i32 %[cnt], %[cnt], 1\n\ts_cmp_gt_i32 %[cnt], 0\n\ts_waitcnt vmcnt(10)\n\ts_cbranch_scc1 .Lqr%=\n\t"
                "s_barrier\n\t" QW4 "s_waitcnt vmcnt(10)\n\t"
                "s_barrier\n\ts_waitcnt vmcnt(4)\n\t"
                "s_barrier\n\ts_waitcnt vmcnt(0)\n\t"
                "s_barrier\n\ts_mov_b32 m0, s70"
                : [xo] "+&s"(xo), [sb] "+&s"(sb), [cnt] "+&s"(cnt)
                : [xl] "s"((unsigned)(uintptr_t)xb), [xh] "s"((unsigned)((uintptr_t)xb >> 32)), [wl] "s"((unsigned)(uintptr_t)wb), [wh] "s"((unsigned)((uintptr_t)wb >> 32)),
                  [dx] "s"(lds0 + j * 2048), [dw] "s"(lds0 + USLOT + j * 4096),
                  [x0] "v"(vX[0]), [x1] "v"(vX[1]), [w0] "v"(vW[0]), [w1] "v"(vW[1]), [w2] "v"(vW[2]), [w3] "v"(vW[3])
                : "memory", "scc", "s60", "s61", "s62", "s63", "s66", "s70");
        }
        STAMP(1);
        {
            const char* wb = Wo + (size_t)ks * 512 * 1152;
            unsigned oF[4];
#pragma unroll
            for (int i = 0; i < 4; ++i) oF[i] = (unsigned)(((4 * j + i) * 4 + ((lane >> 2) & 3)) * 1152 + nq * 256 + lr * 64 + (lane & 3) * 16 - 1024 * i);
            const unsigned oH = (unsigned)((j * 128 + (lane >> 1)) * 1152 + 1024 + nq * 32 + (lane & 1) * 16);
            const unsigned wl = (unsigned)(lds0 + j * 4096 + lane * 16), a_gate = lds0 + L_CNT + 12;
            const unsigned oT = (unsigned)(((idx * 4 + j) * 36 + (lane < 36 ? lane : 0)) * 128), oB = (unsigned)((j * 64 + lane) * 128);
            const int nkv = 2 * NT, ntot = nkv + (NOWO ? 0 : NSL);
            int ni = 0, nl = 0, nw = 0, bi = 0, bw = 0, nb = 0, gate = roleB ? HPRE0 : GATEA, si = (int)(so - lds0), sw = si, lim = mind + NSL - u;
            asm volatile(
                "s_mov_b32 s70, m0\n\ts_mov_b64 s[72:73], 0\n\ts_getpc_b64 s[64:65]\n"
                ".Lp%=:\n\t"
                "s_cmp_eq_u32 %[tb], 0\n\ts_cbranch_scc1 .Lprep%=\n\t"
                "s_add_u32 s60, %[kpl], %[tb]\n\ts_addc_u32 s61, %[kph], 0\n\ts_add_u32 s62, %[vpl], %[tb]\n\ts_addc_u32 s63, %[vph], 0\n\t"
                TOUCHB
                "s_branch .Lprep%=\n"
                ".Ltw%=:\n\ts_mov_b32 s62, %[wpl]\n\ts_mov_b32 s63, %[wph]\n\tglobal_load_dword v37, %[tw], s[62:63] nt\n\ts_branch .Ltwb%=\n"
                ".Ltop%=:\n\t"
                "s_sub_i32 s66, %[ni], %[nl]\n\ts_cmp_lt_i32 s66, 3\n\ts_cbranch_scc1 .Lc1%=\n\t"
                "s_cmp_lt_i32 %[nw], %[nl]\n\ts_cbranch_scc1 .Lalt%=\n"
                ".Lblk%=:\n\ts_waitcnt vmcnt(8)\n\ts_add_i32 %[nl], %[nl], 1\n\t" LSTAMP
                ".Lc1%=:\n\t"
                "s_waitcnt lgkmcnt(0)\n\tv_min_u32 v42, v42, v43\n\tv_min3_u32 v40, v40, v41, v42\n\ts_mov_b32 s71, 0\n\ts_cmp_lt_i32 %[nw], %[nl]\n\tv_readfirstlane_b32 s67, v40\n\t"
                "s_cbranch_scc0 .Lc2%=\n\t"
                "s_bitcmp1_b64 s[72:73], %[nw]\n\ts_cbranch_scc1 .Lwr%=\n"
                ".Lpb%=:\n\t"
                "s_add_i32 %[nw], %[nw], 1\n\ts_add_i32 s66, %[nw], %[u0]\n\tv_mov_b32 v38, s66\n\tds_write_b32 %[aland], v38\n\t"
                "s_add_i32 %[sw], %[sw], 0x4000\n\ts_cmp_eq_u32 %[sw], 0x24000\n\ts_cselect_b32 %[sw], 0, %[sw]\n\ts_mov_b32 s71, 1\n"
                ".Lc2%=:\n\t"
                "s_add_i32 s67, s67, %[lofs]\n\ts_max_i32 %[lim], %[lim], s67\n\t"
                "s_cmp_ge_i32 %[ni], %[gate]\n\ts_cbranch_scc1 .Lno%=\n\t"
                "s_min_i32 s67, %[lim], %[nkv]\n\ts_cmp_lt_i32 %[ni], s67\n\ts_cbranch_scc0 .Lis%=\n\t"
                "global_load_lds_dwordx4 v44, s[60:61] nt\n\tglobal_load_lds_dwordx4 v45, s[60:61] offset:1024 nt\n\t"
                "global_load_lds_dwordx4 v46, s[60:61] offset:2048 nt\n\tglobal_load_lds_dwordx4 v47, s[60:61] offset:3072 nt\n"
                ".Lbk%=:\n\t"
                "s_add_i32 %[ni], %[ni], 1\n\t"
                "s_add_i32 %[si], %[si], 0x4000\n\ts_cmp_eq_u32 %[si], 0x24000\n\ts_cselect_b32 %[si], 0, %[si]\n"
                ".Lprep%=:\n\t"
                "ds_read_b128 v[40:43], %[adone]\n\t"
                "s_add_i32 m0, %[si], %[dbase]\n\t"
                "s_cmp_eq_u32 %[ni], %[ntw]\n\ts_cbranch_scc1 .Ltw%=\n"
                ".Ltwb%=:\n\t"
                "s_cmp_ge_i32 %[ni], %[nkv]\n\ts_cbranch_scc1 .Lpw1%=\n\t"
                "s_lshr_b32 s66, %[ni], 1\n\ts_lshl_b32 s66, s66, 14\n\t"
                "s_bitcmp1_b32 %[ni], 0\n\ts_cselect_b32 s60, %[vpl], %[kpl]\n\ts_cselect_b32 s61, %[vph], %[kph]\n\ts_cselect_b64 vcc, -1, 0\n\t"
                "s_add_u32 s60, s60, s66\n\ts_addc_u32 s61, s61, 0\n\t"
                "v_cndmask_b32 v44, %[k0], %[vv], vcc\n\tv_cndmask_b32 v45, %[k1], %[vv], vcc\n\tv_cndmask_b32 v46, %[k2], %[vv], vcc\n\tv_cndmask_b32 v47, %[k3], %[vv], vcc\n\t"
                "s_branch .Ltop%=\n"
                ".Lpw1%=:\n\t"
                "s_cmp_eq_u32 %[si], 0x20000\n\ts_cbranch_scc1 .Lpw2%=\n\t"
                "s_lshr_b32 s66, %[si], 10\n\ts_mul_i32 s66, s66, 4608\n\t"
                "s_add_u32 s60, %[wpl], s66\n\ts_addc_u32 s61, %[wph], 0\n\t"
                "v_mov_b32 v44, %[f0]\n\tv_mov_b32 v45, %[f1]\n\tv_mov_b32 v46, %[f2]\n\tv_mov_b32 v47, %[f3]\n\ts_branch .Ltop%=\n"
                ".Lpw2%=:\n\t"
                "s_mov_b32 s60, %[wpl]\n\ts_mov_b32 s61, %[wph]\n\t"
                "v_mov_b32 v44, %[hh]\n\tv_add_u32 v45, 35840, %[hh]\n\tv_add_u32 v46, 71680, %[hh]\n\tv_add_u32 v47, 107520, %[hh]\n\ts_branch .Ltop%=\n"
                ".Lno%=:\n\t"
                "s_cmp_ge_i32 %[gate], %[ntot]\n\ts_cbranch_scc1 .Lno2%=\n\t"
                "ds_read_b32 v40, %[agate]\n\ts_waitcnt lgkmcnt(0)\n\tv_readfirstlane_b32 s67, v40\n\ts_nop 3\n\t"
                "s_cmp_ge_u32 s67, 4\n\ts_cselect_b32 %[gate], %[g1], %[gate]\n\ts_cmp_ge_u32 s67, 8\n\ts_cselect_b32 %[gate], %[nkv], %[gate]\n\ts_cmp_ge_u32 s67, 12\n\ts_cselect_b32 %[gate], %[ntot], %[gate]\n\t"
                "s_cmp_lt_i32 %[ni], %[gate]\n\ts_cselect_b32 s71, 1, s71\n"
                ".Lno2%=:\n\t"
                "ds_read_b128 v[40:43], %[adone]\n\t"
                "s_cmp_lg_u32 s71, 0\n\ts_cbranch_scc1 .Ltop%=\n\t"
                "s_cmp_lt_i32 %[nw], %[nl]\n\ts_cbranch_scc1 .Lidle%=\n\t"
                "s_sub_i32 s66, %[ni], %[nl]\n\ts_cmp_eq_u32 s66, 0\n\ts_cbranch_scc1 .Lno3%=\n\t"
                "s_cmp_eq_u32 s66, 2\n\ts_cbranch_scc1 .Lw4%=\n\ts_waitcnt vmcnt(0)\n"
                ".Lw4%=:\n\ts_waitcnt vmcnt(4)\n\ts_add_i32 %[nl], %[nl], 1\n\t" LSTAMP "s_branch .Lc1%=\n"
                ".Lno3%=:\n\ts_cmp_ge_i32 %[nw], %[ntot]\n\ts_cbranch_scc1 .Lend%=\n"
                ".Lidle%=:\n\t"
                "s_sleep " STR(LSLEEP) "\n\ts_sub_i32 %[bud], %[bud], 1\n\ts_cmp_gt_i32 %[bud], 0\n\ts_cbranch_scc1 .Lc1%=\n\t"
                "s_mov_b32 %[lim], 0x3fffffff\n\ts_mov_b32 %[gate], %[ntot]\n\ts_branch .Lc1%=\n"
                ".Lis%=:\n\t"
                "s_cmp_lt_i32 %[ni], %[lim]\n\ts_cbranch_scc1 .Lit2%=\n\t"
                "s_cmp_ge_i32 %[ni], " STR(REGMAX) "\n\ts_cbranch_scc1 .Lno2%=\n\t"
                "s_cmp_ge_i32 %[nb], " STR(NBUF) "\n\ts_cbranch_scc1 .Lno2%=\n\t"
                "s_cmp_lt_i32 %[ni], %[nkv]\n\ts_cselect_b32 s67, 0, .Lit2%=-.Lit%=+36\n\ts_add_u32 s67, s67, %[bi]\n\ts_add_u32 s67, s67, .Lit%=-.Lp%=\n\t"
                "s_add_u32 s62, s64, s67\n\ts_addc_u32 s63, s65, 0\n\t"
                "s_bitset1_b64 s[72:73], %[ni]\n\ts_add_i32 %[nb], %[nb], 1\n\t"
                "s_add_i32 %[bi], %[bi], 36\n\ts_cmp_eq_u32 %[bi], 36*" STR(NBUF) "\n\ts_cselect_b32 %[bi], 0, %[bi]\n\t"
                "s_setpc_b64 s[62:63]\n"
#define LTAB(POL) \
                ".set r_, 48\n\t.rept " STR(NBUF) "\n\t" \
                "global_load_dwordx4 v[r_:r_+3], v44, s[60:61]" POL "\n\tglobal_load_dwordx4 v[r_+4:r_+7], v45, s[60:61] offset:1024" POL "\n\t" \
                "global_load_dwordx4 v[r_+8:r_+11], v46, s[60:61] offset:2048" POL "\n\tglobal_load_dwordx4 v[r_+12:r_+15], v47, s[60:61] offset:3072" POL "\n\ts_branch .Lbk%=\n\t" \
                ".set r_, r_+16\n\t.endr\n"
                ".Lit%=:\n\t" LTAB(" nt")
                ".Lit2%=:\n\t"
                "global_load_lds_dwordx4 v44, s[60:61]\n\tglobal_load_lds_dwordx4 v45, s[60:61] offset:1024\n\t"
                "global_load_lds_dwordx4 v46, s[60:61] offset:2048\n\tglobal_load_lds_dwordx4 v47, s[60:61] offset:3072\n\ts_branch .Lbk%=\n\t"
                LTAB("")
                ".Lalt%=:\n\t"
                "s_bitcmp1_b64 s[72:73], %[nw]\n\ts_cbranch_scc0 .Lpw%=\n\t"
                "s_cmp_lt_i32 %[nw], %[lim]\n\ts_cbranch_scc1 .Lwr2%=\n\t"
                "s_waitcnt lgkmcnt(0)\n\tv_min_u32 v42, v42, v43\n\tv_min3_u32 v40, v40, v41, v42\n\ts_nop 1\n\tv_readfirstlane_b32 s67, v40\n\ts_nop 0\n\t"
                "s_add_i32 s66, s67, %[lofs]\n\ts_max_i32 %[lim], %[lim], s66\n\t"
                "s_cmp_lt_i32 %[nw], %[lim]\n\ts_cbranch_scc0 .Lblk%=\n"
                ".Lwr%=:\n\t"
                "s_add_i32 s66, s67, %[lofs]\n\ts_max_i32 %[lim], %[lim], s66\n\t"
                "s_cmp_lt_i32 %[nw], %[lim]\n\ts_cbranch_scc0 .Lc2%=\n"
                ".Lwr2%=:\n\t"
                "v_add_u32 v39, %[sw], %[wl]\n\t"
                "s_add_u32 s66, %[bw], .Lwt%=-.Lp%=\n\ts_add_u32 s62, s64, s66\n\ts_addc_u32 s63, s65, 0\n\t"
                "s_add_i32 %[bw], %[bw], 36\n\ts_cmp_eq_u32 %[bw], 36*" STR(NBUF) "\n\ts_cselect_b32 %[bw], 0, %[bw]\n\ts_sub_i32 %[nb], %[nb], 1\n\t"
                "s_setpc_b64 s[62:63]\n"
                ".Lwt%=:\n\t"
                ".set r_, 48\n\t.rept " STR(NBUF) "\n\t"
                "ds_write_b128 v39, v[r_:r_+3]\n\tds_write_b128 v39, v[r_+4:r_+7] offset:1024\n\tds_write_b128 v39, v[r_+8:r_+11] offset:2048\n\tds_write_b128 v39, v[r_+12:r_+15] offset:3072\n\ts_branch .Lpw%=\n\t"
                ".set r_, r_+16\n\t.endr\n"
                ".Lpw%=:\n\t"
                "s_sub_i32 s66, %[ni], %[nl]\n\ts_cmp_lt_i32 s66, 3\n\ts_cbranch_scc1 .Lpb%=\n\t"
                "s_add_i32 %[nw], %[nw], 1\n\ts_add_i32 s66, %[nw], %[u0]\n\tv_mov_b32 v38, s66\n\tds_write_b32 %[aland], v38\n\tds_read_b128 v[40:43], %[adone]\n\t"
                "s_add_i32 %[sw], %[sw], 0x4000\n\ts_cmp_eq_u32 %[sw], 0x24000\n\ts_cselect_b32 %[sw], 0, %[sw]\n\ts_branch .Ltop%=\n"
                ".Lend%=:\n\ts_mov_b32 m0, s70\n\ts_waitcnt lgkmcnt(0)"
                : [ni] "+&s"(ni), [nl] "+&s"(nl), [nw] "+&s"(nw), [bi] "+&s"(bi), [bw] "+&s"(bw), [nb] "+&s"(nb), [gate] "+&s"(gate), [si] "+&s"(si), [sw] "+&s"(sw), [lim] "+&s"(lim), [bud] "+&s"(budget) LSTAMP_OP
                : [nkv] "s"(nkv), [ntot] "s"(ntot), [ntw] "s"(nkv - (roleB ? WTB : WTA)), [tb] "s"(roleB ? 0 : NT_A * 16384), [tw] "v"(oT), [tk] "v"(oB), [g1] "s"(roleB ? HPRE : GATEA), [u0] "s"(u), [lofs] "s"(NSL - u), [dbase] "s"(lds0 + j * 4096),
                  [kpl] "s"((unsigned)(uintptr_t)kp), [kph] "s"((unsigned)((uintptr_t)kp >> 32)), [vpl] "s"((unsigned)(uintptr_t)vp), [vph] "s"((unsigned)((uintptr_t)vp >> 32)),
                  [wpl] "s"((unsigned)(uintptr_t)wb), [wph] "s"((unsigned)((uintptr_t)wb >> 32)),
                  [k0] "v"(vK[0]), [k1] "v"(vK[1]), [k2] "v"(vK[2]), [k3] "v"(vK[3]), [vv] "v"(vV), [f0] "v"(oF[0]), [f1] "v"(oF[1]), [f2] "v"(oF[2]), [f3] "v"(oF[3]), [hh] "v"(oH),
                  [wl] "v"(wl), [adone] "v"(a_done), [agate] "v"(a_gate), [aland] "v"(a_landed)
                : "memory", "scc", "vcc", "s60", "s61", "s62", "s63", "s64", "s65", "s66", "s67", "s68", "s69", "s70", "s71", "s72", "s73", "s74", "v37", "v38", "v39", CLOB_40_255);
        }
        STAMP(4);
        STAMP_OUT(1, 4);
        if (j != 0) return;
        while (__builtin_amdgcn_readfirstlane(*(volatile LAS unsigned*)(lds + L_CNT + 8)) == 0 && --budget > 0) __builtin_amdgcn_s_sleep(1);
        tagf = rfl64(*(volatile LAS const u64*)(lds + L_TAG));
    } else {

    const int rg = wid;
    __builtin_amdgcn_s_setprio(CPRIO);
    if (!roleB) {
        unsigned d[4];
        const unsigned i0 = (idx - 16) * 288 + rg * 72 + lane;
#pragma unroll
        for (int k = 0; k < 2; ++k) { const unsigned i = k < 1 ? i0 : i0 - lane + 64 + (lane & 7);
            asm volatile("global_load_dword %0, %2, %3" TPOL "\n\tglobal_load_dword %1, %4, %5" : "=&v"(d[2 * k]), "=&v"(d[2 * k + 1]) : "v"((i >> 3) * 8192 + xcd * 1024 + (i & 7) * 128), "s"(Wq), "v"(i * 128), "s"(X) : "memory"); }
        asm volatile("s_waitcnt vmcnt(0)" :: "v"(d[0]), "v"(d[1]), "v"(d[2]), "v"(d[3]) : "memory");
    }
    if (!roleB && (idx & 3) == 0) {
        unsigned d; unsigned o = (rg * 64 + lane) * 128; o = o < code_len ? o : 0;
        asm volatile("global_load_dword %0, %1, %2\n\ts_waitcnt vmcnt(0)" : "=&v"(d) : "v"(o), "s"(pc0) : "memory");
    }
    const u64 epoch = __hip_atomic_load((GAS const u64*)epo, RLX_AGENT);
    u64 tag;
    const int q4 = (lane & 15) >> 2, p4 = lane & 3, blk = (lane >> 4) & 1;
    int u = 0, avail = 0;
    unsigned a_landed = lds0 + L_LANDED, a_done = lds0 + L_DONE + 4 * rg; PIN(a_landed); PIN(a_done);
    unsigned so = lds0;
    auto acquire = [&]() __attribute__((always_inline)) -> unsigned {
        avail = __builtin_amdgcn_readfirstlane(avail); budget = __builtin_amdgcn_readfirstlane(budget);
        asm volatile("s_cmp_gt_i32 %0, %2\n\ts_cbranch_scc0 .Las%=\n"
                     ".Lab%=:\n\t.subsection 1\n"
                     ".Las%=:\n\tds_read_b128 v[140:143], %3\n\ts_waitcnt lgkmcnt(0)\n\tv_min_u32 v142, v142, v143\n\tv_min3_u32 v140, v140, v141, v142\n\ts_nop 1\n\tv_readfirstlane_b32 %0, v140\n\ts_nop 3\n\t"
                     "s_cmp_gt_i32 %0, %2\n\ts_cbranch_scc1 .Lab%=\n\ts_sub_i32 %1, %1, 1\n\ts_cmp_lt_i32 %1, 1\n\ts_cbranch_scc1 .Lab%=\n\t" CSLEEP "s_branch .Las%=\n\t.subsection 0"
                     : "+&s"(avail), "+&s"(budget) : "s"(__builtin_amdgcn_readfirstlane(u)), "v"(a_landed) : "memory", "scc", "v140", "v141", "v142", "v143");
        const unsigned o = so; ++u; so = so == lds0 + (NSL - 1) * USLOT ? lds0 : so + USLOT; return o; };
    auto release = [&](int c) { lds_put(a_done, (unsigned)c); };
    u32x4 pk_;
#define PEEK() do { pk_ = *(volatile LAS const u32x4*)LP(a_landed); } while (0)
#define SEEN() do { unsigned m_ = pk_[0] < pk_[1] ? pk_[0] : pk_[1], n_ = pk_[2] < pk_[3] ? pk_[2] : pk_[3]; const int a_ = __builtin_amdgcn_readfirstlane(m_ < n_ ? m_ : n_); avail = a_ > avail ? a_ : avail; } while (0)
    auto signoff = [&](int cnt_idx, char* flag) __attribute__((always_inline)) { WAITV(0);
        unsigned old = 0; if (lane == 0) old = __hip_atomic_fetch_add((LAS unsigned*)(lds + L_CNT + 4 * cnt_idx), 1u, __ATOMIC_RELAXED, __HIP_MEMORY_SCOPE_WORKGROUP);
        if (__builtin_amdgcn_readfirstlane(old) == 3 && lane == 0) __hip_atomic_store((GAS u64*)flag, tag, RLX_AGENT); };
    auto opengate = [&](unsigned n) __attribute__((always_inline)) { if (lane == 0) __hip_atomic_fetch_add((LAS unsigned*)(lds + L_CNT + 12), n, __ATOMIC_RELAXED, __HIP_MEMORY_SCOPE_WORKGROUP); };
    const unsigned krd = (unsigned)(256 * r32 + 16 * (hi ^ (r32 & 15)));
    bf16x8 qf[8];
    const unsigned fx = (unsigned)((pair * 4 + rg) * 8192 + lane * 16);
    if (roleB) {
        const int p4s = ((p4 & 1) << 1) | (p4 >> 1);
        unsigned wrd[4];
#pragma unroll
        for (int dt = 0; dt < 4; ++dt) wrd[dt] = (unsigned)((rg >> 1) * 256 + 512 * (8 * hi + q4) + 16 * ((4 * dt + 2 * blk + (p4s >> 1)) ^ (q4 << 2)) + 8 * (p4s & 1));
        f32x16 acc[4] = {};
        bf16x8 xf[4], xn[4] = {}, wf[8];
        unsigned xa[4];
        { const int row = (rg & 1) * 32 + r32;
#pragma unroll
          for (int s = 0; s < 4; ++s) xa[s] = (unsigned)(row * 128 + 16 * ((2 * s + hi) ^ ((row >> 1) & 7))); }
#define WRD(i, wo) wf[i] = trrd2(LP((wo) + wrd[(i) & 3]) + ((i) >> 2) * 8192, LP((wo) + wrd[(i) & 3]) + ((i) >> 2) * 8192 + 2048)
#define QG(i, wi, wo, X) do { acc[(i) & 3] = MFMA32(wf[i], xf[2 * (wi) + ((i) >> 2)], acc[(i) & 3]); PIN(acc[(i) & 3]); SBAR(); WRD(i, wo); X; SBAR(); } while (0)
#define XRD(s, xo) ldsrd16(LP((xo) + xa[s]))
#define BAR() asm volatile("s_barrier" ::: "memory")
        unsigned sb = lds0;
        BAR();
#pragma unroll
        for (int s = 0; s < 4; ++s) xf[s] = XRD(s, sb);
#pragma unroll
        for (int i = 0; i < 8; ++i) WRD(i, sb + USLOT);
#pragma nounroll
        for (int gi = 0; gi < 9; ++gi) {
            const unsigned wo = sb + 2 * USLOT;
            BAR(); SBAR();
            QG(0, 0, wo, (void)0); QG(1, 0, wo, (void)0); QG(2, 0, wo, (void)0); QG(3, 0, wo, (void)0); QG(4, 0, wo, (void)0); QG(5, 0, wo, (void)0); QG(6, 0, wo, (void)0); QG(7, 0, wo, (void)0);
            sb = sb == lds0 + 6 * USLOT ? lds0 : sb + 3 * USLOT;
            if (gi < 8) BAR();
            SBAR();
            QG(0, 1, sb + USLOT, xn[0] = XRD(0, sb)); QG(1, 1, sb + USLOT, xn[1] = XRD(1, sb)); QG(2, 1, sb + USLOT, (void)0); QG(3, 1, sb + USLOT, (void)0);
            QG(4, 1, sb + USLOT, xn[2] = XRD(2, sb)); QG(5, 1, sb + USLOT, (void)0); QG(6, 1, sb + USLOT, (void)0); QG(7, 1, sb + USLOT, xn[3] = XRD(3, sb));
            xf[0] = xn[0]; xf[1] = xn[1]; xf[2] = xn[2]; xf[3] = xn[3];
        }
        STAMP(1);
        tag = next_tag(rfl64(epoch));
#pragma unroll
        for (int dt = 0; dt < 4; ++dt)
#pragma unroll
            for (int s = 0; s < 2; ++s) { const f32x16& a = acc[dt];
                u32x4 w = {pk2(a[8 * s] * QSCALE, a[8 * s + 1] * QSCALE), pk2(a[8 * s + 2] * QSCALE, a[8 * s + 3] * QSCALE), pk2(a[8 * s + 4] * QSCALE, a[8 * s + 5] * QSCALE), pk2(a[8 * s + 6] * QSCALE, a[8 * s + 7] * QSCALE)};
                qf[2 * dt + s] = __builtin_bit_cast(bf16x8, w);
                __builtin_amdgcn_raw_buffer_store_b128(w, wsr, (unsigned)WS_QX + fx + (2 * dt + s) * 1024, 0, 16); }
        asm volatile("" ::: "memory");
        opengate(1);
        STAMP(46);
        signoff(0, ctl + 256); opengate(1);
    } else {
        tag = next_tag(rfl64(epoch));
#pragma nounroll
        while (rfl64(__hip_atomic_load((GAS const u64*)(ctl + 256), RLX_AGENT)) != tag && --budget > 0) {}
        asm volatile("" ::: "memory");
        STAMP(1);
#pragma unroll
        for (int s = 0; s < 8; ++s) qf[s] = __builtin_bit_cast(bf16x8, __builtin_amdgcn_raw_buffer_load_b128(wsr, (unsigned)WS_QX + fx + s * 1024, 0, 16));
        asm volatile("" ::: "memory"); opengate(3);
        __builtin_amdgcn_s_waitcnt(0x0F70);
    }
    STAMP(2);
    unsigned vrd[4];
#pragma unroll
    for (int c = 0; c < 4; ++c) vrd[c] = (unsigned)(256 * (4 * hi + q4) + 16 * ((4 * c + 2 * blk + (p4 >> 1)) ^ (q4 << 2)) + 8 * (p4 & 1));
    f32x16 O0 = {}, O1 = {}, O2 = {}, O3 = {}; float lsum = 0.f, mrow; f32x16 negm, SA, SB; bf16x8 kf[8]; u32x4 pw0, pw1;
#define R_O0 "{v[192:207]}"
#define R_O1 "{v[208:223]}"
#define R_O2 "{v[224:239]}"
#define R_O3 "{v[240:255]}"
#define R_SA "{v[176:191]}"
#define R_SB "{v[160:175]}"
#define N_SA "176"
#define N_SB "160"
#define R_NM "{v[144:159]}"
#define PINR(x) asm volatile("" : "+" R_##x(x))
#define EXP2(x) __builtin_amdgcn_exp2f(x)
#define MX3(a, b, c) __builtin_fmaxf(__builtin_fmaxf((a), (b)), (c))
#define KRD(s, off) kf[s] = ldsrd16(LP(ka[s]) + (off))
#define VRD(g, off) vf[g] = trrd2(lds + va[(g) & 3] + (off) + ((g) >> 2) * 4096, lds + va[(g) & 3] + (off) + ((g) >> 2) * 4096 + 2048)
#define VRDH(g, t, off) vh[2 * (g) + (t)] = trrd(LP(va[(g) & 3]) + (off) + ((g) >> 2) * 4096 + (t) * 2048)
#define VF(g) __builtin_shufflevector(vh[2 * (g)], vh[2 * (g) + 1], 0, 1, 2, 3, 4, 5, 6, 7)

#define KADDR(s, o) do { ka[s] = (krd ^ ((s) << 5)) + (o); PIN(ka[s]); } while (0)
#define VADDR(c, o) do { va[c] = vrd[c] + (o); PIN(va[c]); } while (0)
#define KSTEP(s, d) do { ka[s] += (d); PIN(ka[s]); } while (0)
#define VSTEP(c, d) do { va[c] += (d); PIN(va[c]); } while (0)
#define RESCALE(S, mx, mxb, thr) do { float t0_; asm volatile( \
        "v_max_f32 %8, %8, %11\n\tv_cmp_lt_f32 vcc, %10, %8\n\ts_cbranch_vccnz .Lrs%=\n" \
        ".Lrb%=:\n\t.subsection 1\n" \
        ".Lrs%=:\n\ts_nop 15\n\tv_mov_b32 %9, %8\n\ts_nop 1\n\tv_permlane32_swap_b32 %8, %9\n\ts_nop 1\n\tv_max3_f32 %8, %8, %9, 0\n\t" \
        "v_add_f32 %6, %6, %8\n\tv_exp_f32 %9, -%8\n\t" \
        ".set r_, 0\n\t.rept 16\n\tv_sub_f32 v[" N_##S "+r_], v[" N_##S "+r_], %8\n\tv_sub_f32 v[144+r_], 0, %6\n\t.set r_, r_+1\n\t.endr\n\t" \
        "v_mul_f32 %7, %7, %9\n\t" \
        ".set r_, 192\n\t.rept 64\n\tv_mul_f32 v[r_], v[r_], %9\n\t.set r_, r_+1\n\t.endr\n\ts_nop 1\n\ts_branch .Lrb%=\n\t.subsection 0" \
        : "+" R_O0(O0), "+" R_O1(O1), "+" R_O2(O2), "+" R_O3(O3), "+" R_##S(S), "+" R_NM(negm), "+v"(mrow), "+v"(lsum), "+v"(mx), "=&v"(t0_) : "s"(thr), "v"(mxb) : "vcc"); } while (0)
#define G1(SN, SO, s, CIN, PK, KL) do { SN = MFMA32(kf[s], qf[s], CIN); PINR(SN); SBAR(); PINR(SO); KL; SO[2 * (s)] = EXP2(SO[2 * (s)]); SO[2 * (s) + 1] = EXP2(SO[2 * (s) + 1]); PK; PINR(SO); SBAR(); } while (0)
#define PKW(SO, W, i, r) do { W[i] = pk2(SO[r], SO[(r) + 1]); PIN(W); } while (0)
#define PH1(SN, SO, KL) do { SBAR(); \
    G1(SN, SO, 0, negm, (void)0, KL(0));            G1(SN, SO, 1, SN, PKW(SO, pw0, 0, 0), KL(1)); G1(SN, SO, 2, SN, PKW(SO, pw0, 1, 2), KL(2));  G1(SN, SO, 3, SN, PKW(SO, pw0, 2, 4), KL(3)); \
    G1(SN, SO, 4, SN, PKW(SO, pw0, 3, 6), KL(4));   G1(SN, SO, 5, SN, PKW(SO, pw1, 0, 8), KL(5)); G1(SN, SO, 6, SN, PKW(SO, pw1, 1, 10), KL(6)); G1(SN, SO, 7, SN, PKW(SO, pw1, 2, 12), KL(7)); } while (0)
#define G2(g, OC, W, X) do { OC = MFMA32(VF(g), __builtin_bit_cast(bf16x8, W), OC); PINR(OC); SBAR(); X; SBAR(); } while (0)
#define ADD4(SO, r) do { ls0 += SO[r]; ls1 += SO[(r) + 1]; ls0 += SO[(r) + 2]; ls1 += SO[(r) + 3]; PIN(ls0); PIN(ls1); } while (0)
#define V2(g, vo) do { VRDH(g, 0, vo); VRDH(g, 1, vo); } while (0)
#define PH2(SN, SO, vo, A0, A1, A2, A3) do { SBAR(); \
    G2(0, O0, pw0, { V2(0, vo); PKW(SO, pw1, 3, 14); ls0 = lsum + SO[0]; ls1 = SO[1] + SO[3]; ls0 += SO[2]; PIN(ls0); PIN(ls1); }); G2(1, O1, pw0, { V2(1, vo); ADD4(SO, 4); }); \
    G2(2, O2, pw0, { V2(2, vo); ADD4(SO, 8); }); G2(3, O3, pw0, { V2(3, vo); ADD4(SO, 12); }); \
    G2(4, O0, pw1, { V2(4, vo); mx0 = MX3(SN[0], SN[1], SN[2]); mx1 = MX3(SN[3], SN[4], SN[5]); lsum = ls0 + ls1; PIN(mx0); PIN(mx1); PIN(lsum); A0; }); \
    G2(5, O1, pw1, { V2(5, vo); mx0 = MX3(mx0, SN[6], SN[7]); mx1 = MX3(mx1, SN[8], SN[9]); PIN(mx0); PIN(mx1); A1; }); \
    G2(6, O2, pw1, { V2(6, vo); mx0 = MX3(mx0, SN[10], SN[11]); mx1 = MX3(mx1, SN[12], SN[13]); PIN(mx0); PIN(mx1); A2; }); \
    G2(7, O3, pw1, { V2(7, vo); mx0 = MX3(mx0, SN[14], SN[15]); A3; }); \
    RESCALE(SN, mx0, mx1, THR); } while (0)
#define KL0(s) KRD(s, 0)
#define KL1(s) KRD(s, 8192)
#define KLN(s) (void)0
#define NOP_ (void)0
    unsigned ka[8], va[4]; s16x4 vh[16]; float ls0, ls1, mx0, mx1;
    {
        const unsigned ko = acquire();
#pragma unroll
        for (int s = 0; s < 8; ++s) { KADDR(s, ko); KRD(s, 0); }
        SA = MFMA32(kf[0], qf[0], (f32x16){});
#pragma unroll
        for (int s = 1; s < 8; ++s) SA = MFMA32(kf[s], qf[s], SA);
#pragma unroll
        for (int s = 0; s < 8; ++s) KRD(s, 8192);
        float mx = MX3(SA[0], SA[1], SA[2]);
#pragma unroll
        for (int r = 3; r < 15; r += 2) mx = MX3(mx, SA[r], SA[r + 1]);
        mx = swap_max(__builtin_fmaxf(mx, SA[15])); mrow = mx;
#pragma unroll
        for (int r = 0; r < 16; ++r) { SA[r] -= mx; negm[r] = -mx; }
        asm volatile("" : "+" R_NM(negm));
    }

    STAMP(6);
    unsigned vcur, kcur;
    { const unsigned vo = acquire(); VADDR(0, vo); VADDR(1, vo); VADDR(2, vo); VADDR(3, vo); vcur = vo; kcur = so;
#pragma unroll
      for (int g = 0; g < 8; ++g) V2(g, 0);
#pragma unroll
      for (int s = 0; s < 8; ++s) KADDR(s, so); }
    STAMP(8);
    int tnext = (int)(unsigned)__builtin_amdgcn_s_memrealtime(), per = roleB ? PB0 : PA0;
#pragma nounroll
    for (int t = 0; t < NT - 1; ++t) {
        (void)acquire();
        {
            int now = (int)(unsigned)__builtin_amdgcn_s_memrealtime();
#pragma nounroll
            while (now - tnext < 0) { __builtin_amdgcn_s_sleep(1); now = (int)(unsigned)__builtin_amdgcn_s_memrealtime(); }
            tnext = now + (t >= (roleB ? PENDB : PEND) ? 0 : per); per = per - (roleB ? PB1 : PA1) > PMIN ? per - (roleB ? PB1 : PA1) : PMIN;
        }
        STAMP2(8 + t);
        LGKM(15);
        PH1(SB, SA, KL0);
        LGKM(8);
        PH2(SB, SA, 8192, NOP_, NOP_, NOP_, NOP_);
        LGKM(15);
        PH1(SA, SB, KL1);
        release(u - 1);
        const unsigned vo = acquire();
        const unsigned dv = vo - vcur, dk = so - kcur; vcur = vo; kcur = so;
        VSTEP(0, dv); VSTEP(1, dv); VSTEP(2, dv); VSTEP(3, dv);
        LGKM(8);
        PH2(SA, SB, 0, { KSTEP(0, dk); KSTEP(1, dk); }, { KSTEP(2, dk); KSTEP(3, dk); }, { KSTEP(4, dk); KSTEP(5, dk); }, { KSTEP(6, dk); KSTEP(7, dk); });
    }
    u64 fpv = 0;
    if (!roleB) fpv = __hip_atomic_load((GAS const u64*)(ctl + 512), RLX_AGENT);
    STAMP(30);
    PH1(SB, SA, KLN);
    release(u - 1);
    u32x4 pf[8]; float plse = 0.f; bool early = false;
#define PLOAD() do { _Pragma("unroll") for (int i = 0; i < 8; ++i) pf[i] = __builtin_amdgcn_raw_buffer_load_b128(wsr, (unsigned)WS_PX + fx + i * 1024, 0, 16); \
        plse = __uint_as_float(__builtin_amdgcn_raw_buffer_load_b32(wsr, (unsigned)WS_PL + (pair * 4 + rg) * 256 + lane * 4, 0, 16)); } while (0)
    if (!roleB && rfl64(fpv) == tag) { early = true; asm volatile("" ::: "memory"); PLOAD(); }
    PH2(SB, SA, 8192, NOP_, NOP_, NOP_, NOP_);
    {
#pragma unroll
        for (int r = 0; r < 16; ++r) { SB[r] = EXP2(SB[r]); lsum += SB[r]; }
        pw0 = (u32x4){pk2(SB[0], SB[1]), pk2(SB[2], SB[3]), pk2(SB[4], SB[5]), pk2(SB[6], SB[7])};
        pw1 = (u32x4){pk2(SB[8], SB[9]), pk2(SB[10], SB[11]), pk2(SB[12], SB[13]), pk2(SB[14], SB[15])};
        const bf16x8 p0 = __builtin_bit_cast(bf16x8, pw0), p1 = __builtin_bit_cast(bf16x8, pw1);
        O0 = MFMA32(VF(0), p0, O0); O1 = MFMA32(VF(1), p0, O1); O2 = MFMA32(VF(2), p0, O2); O3 = MFMA32(VF(3), p0, O3);
        O0 = MFMA32(VF(4), p1, O0); O1 = MFMA32(VF(5), p1, O1); O2 = MFMA32(VF(6), p1, O2); O3 = MFMA32(VF(7), p1, O3);
    }
    release(u);
    STAMP(3);
    lsum = swap_add(lsum);
    const float lse = mrow + __builtin_amdgcn_logf(lsum);
    if (!roleB) {
        if (!early) { poll_flag(ctl + 512, tag, budget); PLOAD(); }
        STAMP(5);
        const float M = __builtin_fmaxf(lse, plse), eb = EXP2(lse - M), ea = EXP2(plse - M), r = __builtin_amdgcn_rcpf(ea + eb);
        const float fb = eb * r * __builtin_amdgcn_rcpf(lsum), fa = ea * r;
#pragma unroll
        for (int f = 0; f < 8; ++f) { const f32x16& a = f < 2 ? O0 : f < 4 ? O1 : f < 6 ? O2 : O3; const int q = 8 * (f & 1); u32x4 w;
#pragma unroll
            for (int e = 0; e < 4; ++e) w[e] = pk2(__builtin_fmaf(a[q + 2 * e], fb, fa * __uint_as_float(pf[f][e] << 16)), __builtin_fmaf(a[q + 2 * e + 1], fb, fa * __uint_as_float(pf[f][e] & 0xffff0000u)));
            __builtin_amdgcn_raw_buffer_store_b128(w, wsr, (unsigned)WS_OX + fx + f * 1024, 0, 16); }
    } else {
        const float inv = __builtin_amdgcn_rcpf(lsum);
#pragma unroll
        for (int f = 0; f < 8; ++f) { const f32x16& a = f < 2 ? O0 : f < 4 ? O1 : f < 6 ? O2 : O3; const int r = 8 * (f & 1);
            u32x4 w = {pk2(a[r] * inv, a[r + 1] * inv), pk2(a[r + 2] * inv, a[r + 3] * inv), pk2(a[r + 4] * inv, a[r + 5] * inv), pk2(a[r + 6] * inv, a[r + 7] * inv)};
            __builtin_amdgcn_raw_buffer_store_b128(w, wsr, (unsigned)WS_PX + fx + f * 1024, 0, 16); }
        __builtin_amdgcn_raw_buffer_store_b32(__float_as_uint(lse), wsr, (unsigned)WS_PL + (pair * 4 + rg) * 256 + lane * 4, 0, 16);
    }
    signoff(1, roleB ? ctl + 512 : ctl + 768);
    if (roleB) opengate(1);
    STAMP(4);
    {
        const int qh = rg & 1, nh = rg >> 1;
        *(volatile LAS u64*)(lds + L_TAG) = tag;
        if (!NOWO) { u += NSL - 1; (void)acquire(); }
        STAMP(7);
        unsigned wa0 = lds0 + (nh * 64 + hi) * 1024 + nh * 512 + 64 * q4 + 32 * blk + 8 * p4, wa1 = wa0 ^ 65536, wh = lds0 + 131072 + (nh * 64 + hi) * 128 + 32 * q4 + 128 * blk + 8 * p4; PIN(wa0); PIN(wa1); PIN(wh);
#define AKS(i) ((i) < 48 ? (i) / 3 : 16 + ((i) - 48) / 2)
#define ATL(i) ((i) < 48 ? (i) % 3 : ((i) - 48) % 2)
#define ARD(i) (ATL(i) == 2 ? trrd2(LP(wh) + AKS(i) * 512, LP(wh) + AKS(i) * 512 + 256) \
                            : trrd2(LP((i) < 48 ? wa0 : wa1) + (AKS(i) & 15) * 4096 + ATL(i) * 256, LP((i) < 48 ? wa0 : wa1) + (AKS(i) & 15) * 4096 + 2048 + ATL(i) * 256))
        constexpr int AD = 7;
        bf16x8 af[AD];
#pragma unroll
        for (int i = 0; i < AD; ++i) af[i] = ARD(i);
        const int pr0 = b * HKV + ks * 2 + nh, pr1 = pr0 ^ 1;
        bf16x8 of[32];
#pragma nounroll
        while (!__all(__hip_atomic_load((GAS const u64*)(ws + WS_CT + (size_t)(pr0 ^ (lane & 1)) * 1024 + 768), RLX_AGENT) == tag) && --budget > 0) {}
        asm volatile("" ::: "memory");
        STAMP(40);
#pragma unroll
        for (int f = 0; f < 16; ++f) of[f] = __builtin_bit_cast(bf16x8, __builtin_amdgcn_raw_buffer_load_b128(wsr, (unsigned)WS_OX + (pr0 * 4 + (f >> 3) * 2 + qh) * 8192 + (f & 7) * 1024 + lane * 16, 0, 16));
#pragma unroll
        for (int f = 0; f < 16; ++f) of[16 + f] = __builtin_bit_cast(bf16x8, __builtin_amdgcn_raw_buffer_load_b128(wsr, (unsigned)WS_OX + (pr1 * 4 + (f >> 3) * 2 + qh) * 8192 + (f & 7) * 1024 + lane * 16, 0, 16));
        f32x16 acc[3] = {};
        SBAR();
#pragma unroll
        for (int i = 0; i < 80; ++i) { acc[ATL(i)] = MFMA32(af[i % AD], of[AKS(i)], acc[ATL(i)]); PIN(acc[ATL(i)]); SBAR(); if (i + AD < 80) af[i % AD] = ARD(i + AD); SBAR(); }
        STAMP(41);
        const unsigned sb = (unsigned)WS_SLAB + ((b * 4 + nq) * 8 + (r32 >> 2)) * 20480 + ks * 2560 + ((lane & 3) * 2 + hi) * 16;
#pragma unroll
        for (int k = 0; k < 5; ++k) { const int kk = k == 0 ? 4 : k - 1; const f32x16& a = acc[kk >> 1]; const int r = 8 * (kk & 1);
            const u32x4 v = {pk2(a[r], a[r + 1]), pk2(a[r + 2], a[r + 3]), pk2(a[r + 4], a[r + 5]), pk2(a[r + 6], a[r + 7])};
            __builtin_amdgcn_raw_buffer_store_b128(v, wsr, sb + (kk < 4 ? (qh * 8 + nh * 4 + kk) * 128 : 2048 + (nh * 2 + qh) * 128), 0, 16); }
        STAMP(42);
        signoff(2, fsl + ks * 256);
        STAMP(43);
        tagf = tag;
    }
    }
    {
#pragma nounroll
        while (!__all(__hip_atomic_load((GAS const u64*)(fsl + (lane & 7) * 256), RLX_AGENT) == tagf) && --budget > 0) {}
        asm volatile("" ::: "memory");
        STAMP(44);
        const int e = wid * 32 + r32;
        const unsigned sb = (unsigned)WS_SLAB + ((b * 4 + nq) * 8 + ks) * 20480 + hi * 10240 + e * 16;
        u32x4 v[4];
#pragma unroll
        for (int k = 0; k < 4; ++k) v[k] = __builtin_amdgcn_raw_buffer_load_b128(wsr, sb + k * 2560, 0, 16);
        if (ks == 0 && nq == 0 && tid == 0) __hip_atomic_store((GAS u64*)epo, tagf, RLX_AGENT);
        float s[8];
#pragma unroll
        for (int c = 0; c < 4; ++c) { s[2 * c] = (__uint_as_float(v[0][c] << 16) + __uint_as_float(v[1][c] << 16)) + (__uint_as_float(v[2][c] << 16) + __uint_as_float(v[3][c] << 16));
            s[2 * c + 1] = (__uint_as_float(v[0][c] & 0xffff0000u) + __uint_as_float(v[1][c] & 0xffff0000u)) + (__uint_as_float(v[2][c] & 0xffff0000u) + __uint_as_float(v[3][c] & 0xffff0000u)); }
        float t[4];
#pragma unroll
        for (int c = 0; c < 4; ++c) { auto rr = __builtin_amdgcn_permlane32_swap(__float_as_uint(s[c]), __float_as_uint(s[4 + c]), false, false); t[c] = __uint_as_float(rr[0]) + __uint_as_float(rr[1]); }
        int qh = e >> 6, col = nq * 128 + ((e >> 3) & 7) * 16;
        if (wid == 4) {
#pragma unroll
            for (int c = 0; c < 4; ++c) t[c] += __shfl_xor(t[c], 16);
            qh = (e >> 3) & 1; col = 512 + nq * 16; }
        if (e < 144) *(u32x2*)(out + ((size_t)(b * SQ + qh * 32 + ks * 4 + ((e >> 1) & 3)) * DM + col + hi * 8 + (e & 1) * 4) * 2) = (u32x2){pk2(t[0], t[1]), pk2(t[2], t[3])};
    }
    STAMP(45);
    STAMP_OUT(0, 0);
}

extern "C" void kernel_launch(void* const* d_in, const int* in_sizes, int n_in, void* d_out, int out_size, void* d_ws, size_t ws_size, hipStream_t stream) {
    (void)hipFuncSetAttribute((const void*)attn_k, hipFuncAttributeMaxDynamicSharedMemorySize, LDS1);
    hipLaunchKernelGGL(attn_k, dim3(256), dim3(512), LDS1, stream, (const char*)d_in[0], (const char*)d_in[1], (const char*)d_in[2], (const char*)d_in[3], (const char*)d_in[4], (char*)d_out, (char*)d_ws);
}
```

```cpp
#include <hip/hip_runtime.h>
#include <hip/hip_bf16.h>
#include <stdint.h>

typedef short bf16x8 __attribute__((ext_vector_type(8)));
typedef short s16x4 __attribute__((ext_vector_type(4)));
typedef float f32x16 __attribute__((ext_vector_type(16)));
typedef float f32x4 __attribute__((ext_vector_type(4)));
typedef float f32x2 __attribute__((ext_vector_type(2)));
typedef unsigned u32x4 __attribute__((ext_vector_type(4)));
typedef unsigned u32x2 __attribute__((ext_vector_type(2)));
typedef __bf16 bf2_t __attribute__((ext_vector_type(2)));
typedef unsigned long long u64;
#define LAS __attribute__((address_space(3)))
#define GAS __attribute__((address_space(1)))
typedef LAS const char* lcptr;
typedef LAS char* lptr;
#define DI __device__ __forceinline__
#define MFMA32(a, b, c) __builtin_amdgcn_mfma_f32_32x32x16_bf16((a), (b), (c), 0, 0, 0)
#define MFMA16(a, b, c) __builtin_amdgcn_mfma_f32_16x16x32_bf16((a), (b), (c), 0, 0, 0)
#define WAITBAR(N) asm volatile("s_waitcnt vmcnt(" #N ") lgkmcnt(0)\n\ts_barrier" ::: "memory")
#define WAITV(N) asm volatile("s_waitcnt vmcnt(" #N ")" ::: "memory")
#define PIN(x) asm volatile("" : "+v"(x))
#define SBAR() __builtin_amdgcn_sched_barrier(0)
#define LGKM(n) do { __builtin_amdgcn_s_waitcnt(0xC07F | ((n) << 8)); SBAR(); } while (0)
#define LP(a) ((lcptr)(uintptr_t)(a))
#define STR2(x) #x
#define STR(x) STR2(x)
#define CLOB_40_255 "v40", "v41", "v42", "v43", "v44", "v45", "v46", "v47", "v48", "v49", "v50", "v51", "v52", "v53", "v54", "v55", "v56", "v57", "v58", "v59", "v60", "v61", "v62", "v63", "v64", "v65", "v66", "v67", "v68", "v69", "v70", "v71", "v72", "v73", "v74", "v75", "v76", "v77", "v78", "v79", "v80", "v81", "v82", "v83", "v84", "v85", "v86", "v87", "v88", "v89", "v90", "v91", "v92", "v93", "v94", "v95", "v96", "v97", "v98", "v99", "v100", "v101", "v102", "v103", "v104", "v105", "v106", "v107", "v108", "v109", "v110", "v111", "v112", "v113", "v114", "v115", "v116", "v117", "v118", "v119", "v120", "v121", "v122", "v123", "v124", "v125", "v126", "v127", "v128", "v129", "v130", "v131", "v132", "v133", "v134", "v135", "v136", "v137", "v138", "v139", "v140", "v141", "v142", "v143", "v144", "v145", "v146", "v147", "v148", "v149", "v150", "v151", "v152", "v153", "v154", "v155", "v156", "v157", "v158", "v159", "v160", "v161", "v162", "v163", "v164", "v165", "v166", "v167", "v168", "v169", "v170", "v171", "v172", "v173", "v174", "v175", "v176", "v177", "v178", "v179", "v180", "v181", "v182", "v183", "v184", "v185", "v186", "v187", "v188", "v189", "v190", "v191", "v192", "v193", "v194", "v195", "v196", "v197", "v198", "v199", "v200", "v201", "v202", "v203", "v204", "v205", "v206", "v207", "v208", "v209", "v210", "v211", "v212", "v213", "v214", "v215", "v216", "v217", "v218", "v219", "v220", "v221", "v222", "v223", "v224", "v225", "v226", "v227", "v228", "v229", "v230", "v231", "v232", "v233", "v234", "v235", "v236", "v237", "v238", "v239", "v240", "v241", "v242", "v243", "v244", "v245", "v246", "v247", "v248", "v249", "v250", "v251", "v252", "v253", "v254", "v255"
#define RLX_AGENT __ATOMIC_RELAXED, __HIP_MEMORY_SCOPE_AGENT

constexpr int NB = 8, SQ = 64, DM = 576, HQ = 32, HKV = 16, SK = 2048, DH = 128;
constexpr float QSCALE = 0.08838834764831845f * 1.4426950408889634f;
constexpr float THR = 8.f;
#define TOUCH 2
#define TPOL " nt"
#define CPRIO 2
#define LPRIO 0
#define LSLEEP 1
#define QW 10
#define CTOUCH 1
#define HPRE0 0
#define HPRE 2
#define NBUF 13
#define OPRE 10
#define WTCH 0
#define HPOS 0
#define BWPOL ""
#define WROT 0
#define WOWIN 8
#define WOPOL " nt"
#define SROT 0
#define WTPOL " nt"
#define RG2A -99
#define RG2B 0
#define BWO 0
#define PENDB 99
#define REGMAX GATEA
#define PEND 12
#define FSLEEP 2
#define PACE 2
#define PA0 125
#define PA1 5
#define PB0 100
#define PB1 0
#define PMIN 100
#define WARM 0
#define WT0 200
#define WT1 600
#define WMAX 10
#define LATESIGN 1
#define WTA 99
#define WTB 99
#define TBPOL " nt"
#define TB64
#define TBN 0
#define TOUCHB
#define CSLEEP "s_sleep 1\n\t"
#define NOWO 0
#define NQROT 0
#define LWIN 3
#define GATEA 10
#define NTA 17
#define TPOFF 2
constexpr int NT_A = NTA, NT_B = 32 - NTA, TP = NT_B - TPOFF;
constexpr int USLOT = 16384, NSL = 9, MISC = NSL * USLOT, LDS1 = 163840;
constexpr int L_LANDED = MISC, L_DONE = MISC + 16, L_CNT = MISC + 32, L_TAG = MISC + 64, L_LSE = MISC + 256;
constexpr int NQU = 23;
constexpr int SPIN_CAP = 1 << 17;
constexpr size_t WS_CT = 0;
constexpr size_t WS_EP = 128 * 1024, WS_FS = WS_EP + 8 * 256;
constexpr size_t WS_QX = 1 << 20;
constexpr size_t WS_PX = WS_QX + ((size_t)4 << 20);
constexpr size_t WS_PL = WS_PX + ((size_t)4 << 20);
constexpr size_t WS_OX = WS_PL + ((size_t)1 << 20);
constexpr size_t WS_SLAB = WS_OX + ((size_t)4 << 20);
constexpr int WS_USED = 32 << 20;

#define STAMP3(i)
#define STAMP2(i)
#define STAMP_DECL
#define STAMP(i)
#define STAMP_OUT(k, w)
#define LSTAMP
#define LSTAMP_OP
DI unsigned pk2(float a, float b) { bf2_t r = __builtin_convertvector((f32x2){a, b}, bf2_t); return __builtin_bit_cast(unsigned, r); }
DI s16x4 trrd(lcptr p) { typedef short v4i16_t __attribute__((ext_vector_type(4))); return __builtin_bit_cast(s16x4, __builtin_amdgcn_ds_read_tr16_b64_v4i16((LAS v4i16_t*)p)); }
DI bf16x8 trrd2(lcptr p0, lcptr p1) { s16x4 a = trrd(p0), b = trrd(p1); return __builtin_shufflevector(a, b, 0, 1, 2, 3, 4, 5, 6, 7); }
DI bf16x8 ldsrd16(lcptr p) { return *(LAS const bf16x8*)p; }
DI float swap_max(float m) { auto rr = __builtin_amdgcn_permlane32_swap(__float_as_uint(m), __float_as_uint(m), false, false); return __builtin_fmaxf(__uint_as_float(rr[0]), __uint_as_float(rr[1])); }
DI float swap_add(float m) { auto rr = __builtin_amdgcn_permlane32_swap(__float_as_uint(m), __float_as_uint(m), false, false); return __uint_as_float(rr[0]) + __uint_as_float(rr[1]); }
DI u64 next_tag(u64 e) { return e * 6364136223846793005ull + 1442695040888963407ull; }
DI u64 rfl64(u64 v) { return ((u64)(unsigned)__builtin_amdgcn_readfirstlane((unsigned)(v >> 32)) << 32) | (unsigned)__builtin_amdgcn_readfirstlane((unsigned)v); }
DI int lds_min4(unsigned a) { u32x4 v; asm volatile("ds_read_b128 %0, %1\n\ts_waitcnt lgkmcnt(0)" : "=v"(v) : "v"(a) : "memory");
    unsigned m = v[0] < v[1] ? v[0] : v[1], n = v[2] < v[3] ? v[2] : v[3]; return __builtin_amdgcn_readfirstlane(m < n ? m : n); }
DI void lds_put(unsigned a, unsigned val) { asm volatile("ds_write_b32 %0, %1" :: "v"(a), "v"(val) : "memory"); }
DI void poll_flag(const char* p, u64 tag, int& budget) {
#pragma nounroll
    while (rfl64(__hip_atomic_load((GAS const u64*)p, RLX_AGENT)) != tag && --budget > 0) __builtin_amdgcn_s_sleep(1);
    asm volatile("" ::: "memory"); }

#define DMA4(POL, sbase, v0, v1, v2, v3, ldst) do { unsigned sv_; asm volatile("s_mov_b32 %0, m0\n\ts_mov_b32 m0, %6\n\ts_nop 0\n\t" \
    "global_load_lds_dwordx4 %1, %5" POL "\n\tglobal_load_lds_dwordx4 %2, %5 offset:1024" POL "\n\tglobal_load_lds_dwordx4 %3, %5 offset:2048" POL "\n\tglobal_load_lds_dwordx4 %4, %5 offset:3072" POL "\n\t" \
    "s_mov_b32 m0, %0" : "=&s"(sv_) : "v"(v0), "v"(v1), "v"(v2), "v"(v3), "s"(sbase), "s"(__builtin_amdgcn_readfirstlane(ldst)) : "memory"); } while (0)
#define DMA2(sbase, v0, v1, ldst) do { unsigned sv_; asm volatile("s_mov_b32 %0, m0\n\ts_mov_b32 m0, %4\n\ts_nop 0\n\t" \
    "global_load_lds_dwordx4 %1, %3\n\tglobal_load_lds_dwordx4 %2, %3 offset:1024\n\t" \
    "s_mov_b32 m0, %0" : "=&s"(sv_) : "v"(v0), "v"(v1), "s"(sbase), "s"(__builtin_amdgcn_readfirstlane(ldst)) : "memory"); } while (0)
DI void dma_def(const void* sbase, unsigned voff, unsigned ldst) {
    unsigned sv; asm volatile("s_mov_b32 %0, m0\n\ts_mov_b32 m0, %3\n\ts_nop 0\n\tglobal_load_lds_dwordx4 %1, %2\n\ts_mov_b32 m0, %0" : "=&s"(sv) : "v"(voff), "s"(sbase), "s"(__builtin_amdgcn_readfirstlane(ldst)) : "memory"); }

__global__ __launch_bounds__(512, 2) void attn_k(const char* __restrict__ X, const char* __restrict__ Kc, const char* __restrict__ Vc, const char* __restrict__ Wq, const char* __restrict__ Wo, char* __restrict__ out, char* __restrict__ ws) {
    extern __shared__ __attribute__((aligned(16))) char lds_[];
    const int tid = threadIdx.x, lane = tid & 63, r32 = lane & 31, hi = lane >> 5;
    const int wid = __builtin_amdgcn_readfirstlane(tid >> 6);
    const int bid = blockIdx.x, xcd = bid & 7, idx = bid >> 3;
    const int kvh = xcd * 2 + (idx & 1), b = (idx >> 1) & 7;
    const bool roleB = idx < 16;
    const int pair = b * HKV + kvh;
    const lptr lds = (lptr)lds_;
    const unsigned lds0 = (unsigned)(uintptr_t)lds;
    const int NT = roleB ? NT_B : NT_A;
    const int rot = b;
    char* const ctl = ws + WS_CT + (size_t)pair * 1024;
    const int slot0 = SROT ? b : 0;
    char* const epo = ws + WS_EP + b * 256;
    const int ks = xcd, nq = ((idx & 1) + (roleB ? 0 : 2) + (NQROT ? b : 0)) & 3;
    char* const fsl = ws + WS_FS + (size_t)(b * 4 + nq) * 8 * 256;
    const __amdgpu_buffer_rsrc_t wsr = __builtin_amdgcn_make_buffer_rsrc(ws, 0, WS_USED, 0x00020000);
    STAMP_DECL; STAMP(0);
    u64 pc0; unsigned code_len;
    asm volatile("s_getpc_b64 %0\n.Lcs%=:\n\ts_mov_b32 %1, .Lce%=-.Lcs%=\n\t.subsection 2\n.Lce%=:\n\t.fill 128, 4, 0xBF800000\n\t.subsection 0" : "=s"(pc0), "=s"(code_len));
    if (tid < 16) *(LAS unsigned*)(lds + MISC + tid * 4) = 0u;
    WAITBAR(63);
    int budget = SPIN_CAP;
    u64 tagf;

    if (wid >= 4) {
        const int j = wid - 4, lr = lane >> 4, lc = lane & 15;
        __builtin_amdgcn_s_setprio(LPRIO);
        const size_t koff = ((size_t)pair * SK + (roleB ? NT_A * 64 : 0)) * 256;
        const char* kp = Kc + koff; const char* vp = Vc + koff;
        unsigned vK[4];
#pragma unroll
        for (int i = 0; i < 4; ++i) vK[i] = (unsigned)((16 * j + lr) * 256 + 16 * (lc ^ (4 * i + lr)));
        const unsigned vV = (unsigned)((16 * j + lr) * 256 + 16 * (lc ^ (lr << 2)));
        int u = 0, mind = 0;
        unsigned so = lds0 + slot0 * USLOT;
        unsigned a_landed = lds0 + L_LANDED + 4 * j, a_done = lds0 + L_DONE; PIN(a_landed); PIN(a_done);
        if (roleB) {
            unsigned vX[2], vW[4];
#pragma unroll
            for (int i = 0; i < 2; ++i) { const int row = 16 * j + 8 * i + (lane >> 3); vX[i] = (unsigned)(row * 1152 + 16 * ((lane & 7) ^ ((row >> 1) & 7)) - 1024 * i); }
#pragma unroll
            for (int i = 0; i < 4; ++i) vW[i] = (unsigned)((8 * j + 2 * i + hi) * 8192 + 16 * (r32 ^ (((2 * i + hi) & 3) << 2)) - 1024 * i);
            const char* xb = X + (size_t)b * SQ * DM * 2; const char* wb = Wq + kvh * 512;
            int xo = rot * 128, sb = 0, cnt = 7;
#define QADV "s_add_i32 %[xo], %[xo], 128\n\ts_cmp_eq_u32 %[xo], 1152\n\ts_cselect_b32 %[xo], 0, %[xo]\n\ts_add_i32 %[sb], %[sb], 0xc000\n\ts_cmp_eq_u32 %[sb], 0x24000\n\ts_cselect_b32 %[sb], 0, %[sb]\n\t"
#define QW4 "global_load_lds_dwordx4 %[w0], s[60:61]\n\tglobal_load_lds_dwordx4 %[w1], s[60:61] offset:1024\n\tglobal_load_lds_dwordx4 %[w2], s[60:61] offset:2048\n\tglobal_load_lds_dwordx4 %[w3], s[60:61] offset:3072\n\t"
#define QEVEN0 "s_add_u32 s62, %[xl], %[xo]\n\ts_addc_u32 s63, %[xh], 0\n\ts_lshl_b32 s66, %[xo], 12\n\ts_add_u32 s60, %[wl], s66\n\ts_addc_u32 s61, %[wh], 0\n\ts_add_i32 m0, %[sb], %[dx]\n\ts_add_i32 s66, %[sb], %[dw]\n\t"
#define QEVEN1 "global_load_lds_dwordx4 %[x0], s[62:63]\n\tglobal_load_lds_dwordx4 %[x1], s[62:63] offset:1024\n\ts_mov_b32 m0, s66\n\ts_nop 0\n\t" QW4
#define QODD0 "s_add_u32 s60, s60, 0x40000\n\ts_addc_u32 s61, s61, 0\n\ts_add_i32 m0, m0, 0x4000\n\t"
            asm volatile(
                "s_mov_b32 s70, m0\n\t"
                QEVEN0 QEVEN1 QODD0 "s_nop 0\n\t" QW4 QADV QEVEN0 QEVEN1 QODD0
                "s_waitcnt vmcnt(10)\n"
                ".Lqr%=:\n\t"
                "s_barrier\n\t" QW4 QADV QEVEN0 "s_waitcnt vmcnt(10)\n\t"
                "s_barrier\n\t" QEVEN1 QODD0 "s_sub_i32 %[cnt], %[cnt], 1\n\ts_cmp_gt_i32 %[cnt], 0\n\ts_waitcnt vmcnt(10)\n\ts_cbranch_scc1 .Lqr%=\n\t"
                "s_barrier\n\t" QW4 "s_waitcnt vmcnt(10)\n\t"
                "s_barrier\n\ts_waitcnt vmcnt(4)\n\t"
                "s_barrier\n\ts_waitcnt vmcnt(0)\n\t"
                "s_barrier\n\ts_mov_b32 m0, s70"
                : [xo] "+&s"(xo), [sb] "+&s"(sb), [cnt] "+&s"(cnt)
                : [xl] "s"((unsigned)(uintptr_t)xb), [xh] "s"((unsigned)((uintptr_t)xb >> 32)), [wl] "s"((unsigned)(uintptr_t)wb), [wh] "s"((unsigned)((uintptr_t)wb >> 32)),
                  [dx] "s"(lds0 + j * 2048), [dw] "s"(lds0 + USLOT + j * 4096),
                  [x0] "v"(vX[0]), [x1] "v"(vX[1]), [w0] "v"(vW[0]), [w1] "v"(vW[1]), [w2] "v"(vW[2]), [w3] "v"(vW[3])
                : "memory", "scc", "s60", "s61", "s62", "s63", "s66", "s70");
        }
        STAMP(1);
        {
            const char* wb = Wo + (size_t)ks * 512 * 1152;
            unsigned oF[4], oH[4];
#pragma unroll
            for (int i = 0; i < 4; ++i) { oF[i] = (unsigned)(((4 * j + i) * 4 + lr) * 1152 + nq * 256 + 16 * (lc ^ (lr << 2)) - 1024 * i);
                oH[i] = (unsigned)((j * 128 + i * 32 + (lane >> 1)) * 1152 + 1024 + nq * 32 + (lane & 1) * 16 - 1024 * i); }
            const unsigned wl = (unsigned)(lds0 + j * 4096 + lane * 16), a_gate = lds0 + L_CNT + 12;
            const int nkv = 2 * NT, ntot = roleB ? nkv : nkv + NSL;
            int ni = 0, nl = 0, nw = 0, bi = 0, bw = 0, nb = 0, gate = roleB ? 0 : GATEA, si = 0, sw = 0, lim = NSL, wc = roleB ? 9 : 0, wp = 0, hold = 0x7fffffff;
            asm volatile(
                "s_mov_b32 s70, m0\n\ts_mov_b64 s[72:73], 0\n\ts_mov_b32 s75, 0\n\ts_getpc_b64 s[64:65]\n"
                ".Lp%=:\n\t"
                "s_branch .Lprep%=\n"
                ".Ltop%=:\n\t"
                "s_sub_i32 s66, %[ni], %[nl]\n\ts_cmp_lt_i32 s66, 3\n\ts_cbranch_scc1 .Lc1%=\n\t"
                "s_waitcnt vmcnt(8)\n\ts_add_i32 %[nl], %[nl], 1\n\t" LSTAMP
                ".Lc1%=:\n\t"
                "s_waitcnt lgkmcnt(0)\n\tv_min_u32 v42, v42, v43\n\tv_min3_u32 v40, v40, v41, v42\n\ts_mov_b32 s71, 0\n\ts_cmp_lt_i32 %[nw], %[nl]\n\tv_readfirstlane_b32 s67, v40\n\t"
                "s_cbranch_scc0 .Lc2%=\n\t"
                "s_bitcmp1_b64 s[72:73], %[nw]\n\ts_cbranch_scc1 .Lwr%=\n"
                ".Lpb%=:\n\t"
                "s_add_i32 %[nw], %[nw], 1\n\tv_mov_b32 v38, %[nw]\n\tds_write_b32 %[aland], v38\n\t"
                "s_add_i32 %[sw], %[sw], 0x4000\n\ts_cmp_eq_u32 %[sw], 0x24000\n\ts_cselect_b32 %[sw], 0, %[sw]\n\ts_mov_b32 s71, 1\n\t"
                "s_cmp_lt_i32 %[nw], %[nl]\n\ts_cbranch_scc1 .Lmore%=\n"
                ".Lc2%=:\n\t"
                "s_add_i32 s67, s67, 9\n\ts_max_i32 %[lim], %[lim], s67\n\t"
                "s_cmp_ge_i32 %[ni], %[gate]\n\ts_cbranch_scc1 .Lno%=\n\t"
                "s_cmp_lt_i32 %[ni], %[lim]\n\ts_cbranch_scc0 .Lis%=\n\t"
                "global_load_lds_dwordx4 v44, s[60:61] nt\n\tglobal_load_lds_dwordx4 v45, s[60:61] offset:1024 nt\n\t"
                "global_load_lds_dwordx4 v46, s[60:61] offset:2048 nt\n\tglobal_load_lds_dwordx4 v47, s[60:61] offset:3072 nt\n"
                ".Lbk%=:\n\t"
                "s_add_i32 %[ni], %[ni], 1\n\ts_mov_b32 s75, 1\n\t"
                "s_add_i32 %[si], %[si], 0x4000\n\ts_cmp_eq_u32 %[si], 0x24000\n\ts_cselect_b32 %[si], 0, %[si]\n"
                ".Lprep%=:\n\t"
                "ds_read_b128 v[40:43], %[adone]\n\t"
                "s_add_i32 m0, %[si], %[dbase]\n\t"
                "s_lshr_b32 s66, %[ni], 1\n\ts_lshl_b32 s66, s66, 14\n\t"
                "s_bitcmp1_b32 %[ni], 0\n\ts_cselect_b32 s60, %[vpl], %[kpl]\n\ts_cselect_b32 s61, %[vph], %[kph]\n\ts_cselect_b64 vcc, -1, 0\n\t"
                "s_add_u32 s60, s60, s66\n\ts_addc_u32 s61, s61, 0\n\t"
                "v_cndmask_b32 v44, %[k0], %[vv], vcc\n\tv_cndmask_b32 v45, %[k1], %[vv], vcc\n\tv_cndmask_b32 v46, %[k2], %[vv], vcc\n\tv_cndmask_b32 v47, %[k3], %[vv], vcc\n\t"
                "s_branch .Ltop%=\n"
                ".Lmore%=:\n\ts_bitcmp1_b64 s[72:73], %[nw]\n\ts_cbranch_scc1 .Lwr%=\n\ts_branch .Lpb%=\n"
                ".Lno%=:\n\t"
                "s_cmp_ge_i32 %[gate], %[nkv]\n\ts_cbranch_scc1 .Lno2%=\n\t"
                "ds_read_b32 v40, %[agate]\n\ts_waitcnt lgkmcnt(0)\n\tv_readfirstlane_b32 s67, v40\n\ts_nop 3\n\t"
                "s_cmp_ge_u32 s67, 4\n\ts_cselect_b32 %[gate], %[g1], %[gate]\n\ts_cmp_ge_u32 s67, 8\n\ts_cselect_b32 %[gate], %[nkv], %[gate]\n\t"
                "s_cmp_lt_i32 %[ni], %[gate]\n\ts_cselect_b32 s71, 1, s71\n"
                ".Lno2%=:\n\t"
                "ds_read_b128 v[40:43], %[adone]\n\t"
                "s_cmp_lg_u32 s71, 0\n\ts_cbranch_scc1 .Ltop%=\n\t"
                "s_sub_i32 s66, %[ni], %[nl]\n\ts_cmp_eq_u32 s66, 0\n\ts_cbranch_scc1 .Lno3%=\n\t"
                "s_cmp_eq_u32 s66, 2\n\ts_cbranch_scc1 .Lw4%=\n\ts_waitcnt vmcnt(0)\n"
                ".Lw4%=:\n\ts_waitcnt vmcnt(4)\n\ts_add_i32 %[nl], %[nl], 1\n\t" LSTAMP "s_branch .Lc1%=\n"
                ".Lno3%=:\n\ts_cmp_ge_i32 %[nw], %[ntot]\n\ts_cbranch_scc1 .Lend%=\n\t"
                "s_cmp_lt_i32 %[gate], %[nkv]\n\ts_cbranch_scc1 .Lidle%=\n\t"
                "s_cmp_lt_i32 %[wc], 9\n\ts_cbranch_scc1 .Lwo0%=\n\t"
                "s_cmp_eq_u32 %[ni], %[nkv]\n\ts_cbranch_scc0 .Lidle%=\n\ts_cmp_eq_u32 %[ni], %[ntot]\n\ts_cbranch_scc1 .Lidle%=\n\t"
                "s_waitcnt vmcnt(0)\n\ts_lshl_b64 s[62:63], 0x1ff, %[nkv]\n\ts_or_b64 s[72:73], s[72:73], s[62:63]\n\ts_mov_b32 %[ni], %[ntot]\n\ts_mov_b32 %[nl], %[ntot]\n\ts_branch .Lc1%=\n"
                ".Lidle%=:\n\t"
                "s_sleep " STR(LSLEEP) "\n\ts_sub_i32 %[bud], %[bud], 1\n\ts_cmp_gt_i32 %[bud], 0\n\ts_cbranch_scc1 .Lc1%=\n\t"
                "s_mov_b32 %[lim], 0x3fffffff\n\ts_mov_b32 %[gate], %[nkv]\n\ts_mov_b32 %[hold], 0x7fffffff\n\ts_branch .Lc1%=\n"
                ".Lrf%=:\n\t"
                "s_cmp_eq_u32 s75, 0\n\ts_cbranch_scc1 .Lno2%=\n\ts_cmp_ge_i32 %[wc], 9\n\ts_cbranch_scc1 .Lno2%=\n\ts_cmp_lt_i32 %[gate], %[nkv]\n\ts_cbranch_scc1 .Lno2%=\n\t"
                "s_mov_b32 s75, 0\n\tds_read_b128 v[40:43], %[adone]\n\ts_branch .Lwo%=\n"
                ".Lwo0%=:\n\t"
                "s_cmp_lt_i32 %[ni], %[hold]\n\ts_cbranch_scc1 .Lwo1%=\n\t"
                "ds_read_b32 v39, %[agate]\n\ts_waitcnt lgkmcnt(0)\n\tv_readfirstlane_b32 s67, v39\n\ts_nop 3\n\ts_cmp_lt_u32 s67, 12\n\ts_cbranch_scc1 .Lidle%=\n\ts_mov_b32 %[hold], 0x7fffffff\n"
                ".Lwo1%=:\n\ts_waitcnt vmcnt(" STR(WOWIN) ")\n"
                ".Lwo%=:\n\t"
                "s_and_b32 s66, %[wp], 7\n\ts_mul_i32 s66, s66, 73728\n\ts_add_u32 s76, %[wpl], s66\n\ts_addc_u32 s77, %[wph], 0\n\t"
                "s_mul_i32 s66, %[wp], 36\n\ts_add_u32 s66, s66, .Lit2%=-.Lp%=\n\ts_add_u32 s62, s64, s66\n\ts_addc_u32 s63, s65, 0\n\t"
                "s_add_i32 %[wc], %[wc], 1\n\ts_add_i32 %[wp], %[wp], 1\n\ts_cmp_eq_u32 %[wp], 9\n\ts_cselect_b32 %[wp], 0, %[wp]\n\t"
                "s_setpc_b64 s[62:63]\n"
                ".Lit2%=:\n\t"
                ".set r_, 112\n\t.rept 8\n\t"
                "global_load_dwordx4 v[r_:r_+3], %[f0], s[76:77]" WOPOL "\n\tglobal_load_dwordx4 v[r_+4:r_+7], %[f1], s[76:77] offset:1024" WOPOL "\n\t"
                "global_load_dwordx4 v[r_+8:r_+11], %[f2], s[76:77] offset:2048" WOPOL "\n\tglobal_load_dwordx4 v[r_+12:r_+15], %[f3], s[76:77] offset:3072" WOPOL "\n\ts_branch .Lc1%=\n\t"
                ".set r_, r_+16\n\t.endr\n\t"
                "global_load_dwordx4 v[240:243], %[h0], s[76:77]" WOPOL "\n\tglobal_load_dwordx4 v[244:247], %[h1], s[76:77] offset:1024" WOPOL "\n\t"
                "global_load_dwordx4 v[248:251], %[h2], s[76:77] offset:2048" WOPOL "\n\tglobal_load_dwordx4 v[252:255], %[h3], s[76:77] offset:3072" WOPOL "\n\ts_branch .Lc1%=\n"
                ".Lis%=:\n\t"
                "s_cmp_ge_i32 %[ni], " STR(REGMAX) "\n\ts_cbranch_scc1 .Lrf%=\n\t"
                "s_cmp_ge_i32 %[nb], 4\n\ts_cbranch_scc1 .Lno2%=\n\t"
                "s_add_u32 s67, %[bi], .Lit%=-.Lp%=\n\ts_add_u32 s62, s64, s67\n\ts_addc_u32 s63, s65, 0\n\t"
                "s_bitset1_b64 s[72:73], %[ni]\n\ts_add_i32 %[nb], %[nb], 1\n\t"
                "s_add_i32 %[bi], %[bi], 36\n\ts_cmp_eq_u32 %[bi], 144\n\ts_cselect_b32 %[bi], 0, %[bi]\n\t"
                "s_setpc_b64 s[62:63]\n"
                ".Lit%=:\n\t"
                ".set r_, 48\n\t.rept 4\n\t"
                "global_load_dwordx4 v[r_:r_+3], v44, s[60:61] nt\n\tglobal_load_dwordx4 v[r_+4:r_+7], v45, s[60:61] offset:1024 nt\n\t"
                "global_load_dwordx4 v[r_+8:r_+11], v46, s[60:61] offset:2048 nt\n\tglobal_load_dwordx4 v[r_+12:r_+15], v47, s[60:61] offset:3072 nt\n\ts_branch .Lbk%=\n\t"
                ".set r_, r_+16\n\t.endr\n"
                ".Lwr%=:\n\t"
                "s_add_i32 s66, s67, 9\n\ts_max_i32 %[lim], %[lim], s66\n\t"
                "s_cmp_lt_i32 %[nw], %[lim]\n\ts_cbranch_scc0 .Lc2%=\n\t"
                "v_add_u32 v39, %[sw], %[wl]\n\t"
                "s_lshr_b32 s66, %[sw], 14\n\ts_mul_i32 s66, s66, 36\n\ts_add_i32 s66, s66, 144\n\ts_cmp_lt_i32 %[nw], %[nkv]\n\ts_cselect_b32 s66, %[bw], s66\n\t"
                "s_add_u32 s66, s66, .Lwt%=-.Lp%=\n\ts_add_u32 s62, s64, s66\n\ts_addc_u32 s63, s65, 0\n\t"
                "s_add_i32 %[bw], %[bw], 36\n\ts_cmp_eq_u32 %[bw], 144\n\ts_cselect_b32 %[bw], 0, %[bw]\n\ts_sub_i32 %[nb], %[nb], 1\n\t"
                "s_setpc_b64 s[62:63]\n"
                ".Lwt%=:\n\t"
                ".set r_, 48\n\t.rept 13\n\t"
                "ds_write_b128 v39, v[r_:r_+3]\n\tds_write_b128 v39, v[r_+4:r_+7] offset:1024\n\tds_write_b128 v39, v[r_+8:r_+11] offset:2048\n\tds_write_b128 v39, v[r_+12:r_+15] offset:3072\n\ts_branch .Lpb%=\n\t"
                ".set r_, r_+16\n\t.endr\n"
                ".Lend%=:\n\ts_mov_b32 m0, s70\n\ts_waitcnt lgkmcnt(0)"
                : [ni] "+&s"(ni), [nl] "+&s"(nl), [nw] "+&s"(nw), [bi] "+&s"(bi), [bw] "+&s"(bw), [nb] "+&s"(nb), [gate] "+&s"(gate), [si] "+&s"(si), [sw] "+&s"(sw), [lim] "+&s"(lim),
                  [wc] "+&s"(wc), [wp] "+&s"(wp), [hold] "+&s"(hold), [bud] "+&s"(budget) LSTAMP_OP
                : [nkv] "s"(nkv), [ntot] "s"(ntot), [g1] "s"(roleB ? HPRE : GATEA), [dbase] "s"(lds0 + j * 4096),
                  [kpl] "s"((unsigned)(uintptr_t)kp), [kph] "s"((unsigned)((uintptr_t)kp >> 32)), [vpl] "s"((unsigned)(uintptr_t)vp), [vph] "s"((unsigned)((uintptr_t)vp >> 32)),
                  [wpl] "s"((unsigned)(uintptr_t)wb), [wph] "s"((unsigned)((uintptr_t)wb >> 32)),
                  [k0] "v"(vK[0]), [k1] "v"(vK[1]), [k2] "v"(vK[2]), [k3] "v"(vK[3]), [vv] "v"(vV), [f0] "v"(oF[0]), [f1] "v"(oF[1]), [f2] "v"(oF[2]), [f3] "v"(oF[3]),
                  [h0] "v"(oH[0]), [h1] "v"(oH[1]), [h2] "v"(oH[2]), [h3] "v"(oH[3]),
                  [wl] "v"(wl), [adone] "v"(a_done), [agate] "v"(a_gate), [aland] "v"(a_landed)
                : "memory", "scc", "vcc", "s60", "s61", "s62", "s63", "s64", "s65", "s66", "s67", "s68", "s69", "s70", "s71", "s72", "s73", "s74", "s75", "s76", "s77", "v38", "v39", CLOB_40_255);
            if (roleB) {
                while (__builtin_amdgcn_readfirstlane(*(volatile LAS unsigned*)(lds + L_CNT + 12)) < 12 && --budget > 0) __builtin_amdgcn_s_sleep(1);
                int p = b;
                STAMP(2);
#pragma nounroll
                for (int k = 0; k < 8; ++k) { DMA4(BWPOL, wb + (size_t)p * 73728, oF[0], oF[1], oF[2], oF[3], lds0 + p * USLOT + j * 4096); p = (p + 1) & 7;
                    if (k == HPOS) DMA4(BWPOL, wb, oH[0], oH[1], oH[2], oH[3], lds0 + 8 * USLOT + j * 4096);
                    if (k == 2) STAMP(6); if (k == 5) STAMP(8); }
                STAMP(3);
                WAITV(0); lds_put(a_landed, (unsigned)(nkv + NSL));
            }
        }
        STAMP(4);
        STAMP_OUT(1, 4);
        if (j != 0) return;
        while (__builtin_amdgcn_readfirstlane(*(volatile LAS unsigned*)(lds + L_CNT + 8)) == 0 && --budget > 0) __builtin_amdgcn_s_sleep(1);
        tagf = rfl64(*(volatile LAS const u64*)(lds + L_TAG));
    } else {

    const int rg = wid;
    __builtin_amdgcn_s_setprio(CPRIO);
    if (!roleB) {
        unsigned d[4];
        const unsigned i0 = (idx - 16) * 288 + rg * 72 + lane;
#pragma unroll
        for (int k = 0; k < 2; ++k) { const unsigned i = k < 1 ? i0 : i0 - lane + 64 + (lane & 7);
            asm volatile("global_load_dword %0, %2, %3" TPOL "\n\tglobal_load_dword %1, %4, %5" : "=&v"(d[2 * k]), "=&v"(d[2 * k + 1]) : "v"((i >> 3) * 8192 + xcd * 1024 + (i & 7) * 128), "s"(Wq), "v"(i * 128), "s"(X) : "memory"); }
        asm volatile("s_waitcnt vmcnt(0)" :: "v"(d[0]), "v"(d[1]), "v"(d[2]), "v"(d[3]) : "memory");
    }
    if (!roleB && (idx & 3) == 0) {
        unsigned d; unsigned o = (rg * 64 + lane) * 128; o = o < code_len ? o : 0;
        asm volatile("global_load_dword %0, %1, %2\n\ts_waitcnt vmcnt(0)" : "=&v"(d) : "v"(o), "s"(pc0) : "memory");
    }
    const u64 epoch = __hip_atomic_load((GAS const u64*)epo, RLX_AGENT);
    u64 tag;
    const int q4 = (lane & 15) >> 2, p4 = lane & 3, blk = (lane >> 4) & 1;
    int u = 0, avail = 0;
    unsigned a_landed = lds0 + L_LANDED, a_done = lds0 + L_DONE + 4 * rg; PIN(a_landed); PIN(a_done);
    unsigned so = lds0 + slot0 * USLOT;
    auto acquire = [&]() __attribute__((always_inline)) -> unsigned {
        avail = __builtin_amdgcn_readfirstlane(avail); budget = __builtin_amdgcn_readfirstlane(budget);
        asm volatile("s_cmp_gt_i32 %0, %2\n\ts_cbranch_scc0 .Las%=\n"
                     ".Lab%=:\n\t.subsection 1\n"
                     ".Las%=:\n\tds_read_b128 v[140:143], %3\n\ts_waitcnt lgkmcnt(0)\n\tv_min_u32 v142, v142, v143\n\tv_min3_u32 v140, v140, v141, v142\n\ts_nop 1\n\tv_readfirstlane_b32 %0, v140\n\ts_nop 3\n\t"
                     "s_cmp_gt_i32 %0, %2\n\ts_cbranch_scc1 .Lab%=\n\ts_sub_i32 %1, %1, 1\n\ts_cmp_lt_i32 %1, 1\n\ts_cbranch_scc1 .Lab%=\n\t" CSLEEP "s_branch .Las%=\n\t.subsection 0"
                     : "+&s"(avail), "+&s"(budget) : "s"(__builtin_amdgcn_readfirstlane(u)), "v"(a_landed) : "memory", "scc", "v140", "v141", "v142", "v143");
        const unsigned o = so; ++u; so = so == lds0 + (NSL - 1) * USLOT ? lds0 : so + USLOT; return o; };
    auto release = [&](int c) { lds_put(a_done, (unsigned)c); };
    u32x4 pk_;
#define PEEK() do { pk_ = *(volatile LAS const u32x4*)LP(a_landed); } while (0)
#define SEEN() do { unsigned m_ = pk_[0] < pk_[1] ? pk_[0] : pk_[1], n_ = pk_[2] < pk_[3] ? pk_[2] : pk_[3]; const int a_ = __builtin_amdgcn_readfirstlane(m_ < n_ ? m_ : n_); avail = a_ > avail ? a_ : avail; } while (0)
    auto signoff = [&](int cnt_idx, char* flag) __attribute__((always_inline)) { WAITV(0);
        unsigned old = 0; if (lane == 0) old = __hip_atomic_fetch_add((LAS unsigned*)(lds + L_CNT + 4 * cnt_idx), 1u, __ATOMIC_RELAXED, __HIP_MEMORY_SCOPE_WORKGROUP);
        if (__builtin_amdgcn_readfirstlane(old) == 3 && lane == 0) __hip_atomic_store((GAS u64*)flag, tag, RLX_AGENT); };
    auto opengate = [&](unsigned n) __attribute__((always_inline)) { if (lane == 0) __hip_atomic_fetch_add((LAS unsigned*)(lds + L_CNT + 12), n, __ATOMIC_RELAXED, __HIP_MEMORY_SCOPE_WORKGROUP); };
    const unsigned krd = (unsigned)(256 * r32 + 16 * (hi ^ (r32 & 15)));
    bf16x8 qf[8];
    const unsigned fx = (unsigned)((pair * 4 + rg) * 8192 + lane * 16);
    if (roleB) {
        const int p4s = ((p4 & 1) << 1) | (p4 >> 1);
        unsigned wrd[4];
#pragma unroll
        for (int dt = 0; dt < 4; ++dt) wrd[dt] = (unsigned)((rg >> 1) * 256 + 512 * (8 * hi + q4) + 16 * ((4 * dt + 2 * blk + (p4s >> 1)) ^ (q4 << 2)) + 8 * (p4s & 1));
        f32x16 acc[4] = {};
        bf16x8 xf[4], xn[4] = {}, wf[8];
        unsigned xa[4];
        { const int row = (rg & 1) * 32 + r32;
#pragma unroll
          for (int s = 0; s < 4; ++s) xa[s] = (unsigned)(row * 128 + 16 * ((2 * s + hi) ^ ((row >> 1) & 7))); }
#define WRD(i, wo) wf[i] = trrd2(LP((wo) + wrd[(i) & 3]) + ((i) >> 2) * 8192, LP((wo) + wrd[(i) & 3]) + ((i) >> 2) * 8192 + 2048)
#define QG(i, wi, wo, X) do { acc[(i) & 3] = MFMA32(wf[i], xf[2 * (wi) + ((i) >> 2)], acc[(i) & 3]); PIN(acc[(i) & 3]); SBAR(); WRD(i, wo); X; SBAR(); } while (0)
#define XRD(s, xo) ldsrd16(LP((xo) + xa[s]))
#define BAR() asm volatile("s_barrier" ::: "memory")
        unsigned sb = lds0;
        BAR();
#pragma unroll
        for (int s = 0; s < 4; ++s) xf[s] = XRD(s, sb);
#pragma unroll
        for (int i = 0; i < 8; ++i) WRD(i, sb + USLOT);
#pragma nounroll
        for (int gi = 0; gi < 9; ++gi) {
            const unsigned wo = sb + 2 * USLOT;
            BAR(); SBAR();
            QG(0, 0, wo, (void)0); QG(1, 0, wo, (void)0); QG(2, 0, wo, (void)0); QG(3, 0, wo, (void)0); QG(4, 0, wo, (void)0); QG(5, 0, wo, (void)0); QG(6, 0, wo, (void)0); QG(7, 0, wo, (void)0);
            sb = sb == lds0 + 6 * USLOT ? lds0 : sb + 3 * USLOT;
            if (gi < 8) BAR();
            SBAR();
            QG(0, 1, sb + USLOT, xn[0] = XRD(0, sb)); QG(1, 1, sb + USLOT, xn[1] = XRD(1, sb)); QG(2, 1, sb + USLOT, (void)0); QG(3, 1, sb + USLOT, (void)0);
            QG(4, 1, sb + USLOT, xn[2] = XRD(2, sb)); QG(5, 1, sb + USLOT, (void)0); QG(6, 1, sb + USLOT, (void)0); QG(7, 1, sb + USLOT, xn[3] = XRD(3, sb));
            xf[0] = xn[0]; xf[1] = xn[1]; xf[2] = xn[2]; xf[3] = xn[3];
        }
        STAMP(1);
        tag = next_tag(rfl64(epoch));
#pragma unroll
        for (int dt = 0; dt < 4; ++dt)
#pragma unroll
            for (int s = 0; s < 2; ++s) { const f32x16& a = acc[dt];
                u32x4 w = {pk2(a[8 * s] * QSCALE, a[8 * s + 1] * QSCALE), pk2(a[8 * s + 2] * QSCALE, a[8 * s + 3] * QSCALE), pk2(a[8 * s + 4] * QSCALE, a[8 * s + 5] * QSCALE), pk2(a[8 * s + 6] * QSCALE, a[8 * s + 7] * QSCALE)};
                qf[2 * dt + s] = __builtin_bit_cast(bf16x8, w);
                __builtin_amdgcn_raw_buffer_store_b128(w, wsr, (unsigned)WS_QX + fx + (2 * dt + s) * 1024, 0, 16); }
        asm volatile("" ::: "memory");
        opengate(1);
        STAMP(46);
        signoff(0, ctl + 256); opengate(1);
    } else {
        tag = next_tag(rfl64(epoch));
#pragma nounroll
        while (rfl64(__hip_atomic_load((GAS const u64*)(ctl + 256), RLX_AGENT)) != tag && --budget > 0) {}
        asm volatile("" ::: "memory");
        STAMP(1);
#pragma unroll
        for (int s = 0; s < 8; ++s) qf[s] = __builtin_bit_cast(bf16x8, __builtin_amdgcn_raw_buffer_load_b128(wsr, (unsigned)WS_QX + fx + s * 1024, 0, 16));
        asm volatile("" ::: "memory"); opengate(2);
        __builtin_amdgcn_s_waitcnt(0x0F70);
    }
    STAMP(2);
    unsigned vrd[4];
#pragma unroll
    for (int c = 0; c < 4; ++c) vrd[c] = (unsigned)(256 * (4 * hi + q4) + 16 * ((4 * c + 2 * blk + (p4 >> 1)) ^ (q4 << 2)) + 8 * (p4 & 1));
    f32x16 O0 = {}, O1 = {}, O2 = {}, O3 = {}; float lsum = 0.f, mrow; f32x16 negm, SA, SB; bf16x8 kf[8]; u32x4 pw0, pw1;
#define R_O0 "{v[192:207]}"
#define R_O1 "{v[208:223]}"
#define R_O2 "{v[224:239]}"
#define R_O3 "{v[240:255]}"
#define R_SA "{v[176:191]}"
#define R_SB "{v[160:175]}"
#define N_SA "176"
#define N_SB "160"
#define R_NM "{v[144:159]}"
#define PINR(x) asm volatile("" : "+" R_##x(x))
#define EXP2(x) __builtin_amdgcn_exp2f(x)
#define MX3(a, b, c) __builtin_fmaxf(__builtin_fmaxf((a), (b)), (c))
#define KRD(s, off) kf[s] = ldsrd16(LP(ka[s]) + (off))
#define VRD(g, off) vf[g] = trrd2(lds + va[(g) & 3] + (off) + ((g) >> 2) * 4096, lds + va[(g) & 3] + (off) + ((g) >> 2) * 4096 + 2048)
#define VRDH(g, t, off) vh[2 * (g) + (t)] = trrd(LP(va[(g) & 3]) + (off) + ((g) >> 2) * 4096 + (t) * 2048)
#define VF(g) __builtin_shufflevector(vh[2 * (g)], vh[2 * (g) + 1], 0, 1, 2, 3, 4, 5, 6, 7)

#define KADDR(s, o) do { ka[s] = (krd ^ ((s) << 5)) + (o); PIN(ka[s]); } while (0)
#define VADDR(c, o) do { va[c] = vrd[c] + (o); PIN(va[c]); } while (0)
#define KSTEP(s, d) do { ka[s] += (d); PIN(ka[s]); } while (0)
#define VSTEP(c, d) do { va[c] += (d); PIN(va[c]); } while (0)
#define RESCALE(S, mx, mxb, thr) do { float t0_; asm volatile( \
        "v_max_f32 %8, %8, %11\n\tv_cmp_lt_f32 vcc, %10, %8\n\ts_cbranch_vccnz .Lrs%=\n" \
        ".Lrb%=:\n\t.subsection 1\n" \
        ".Lrs%=:\n\ts_nop 15\n\tv_mov_b32 %9, %8\n\ts_nop 1\n\tv_permlane32_swap_b32 %8, %9\n\ts_nop 1\n\tv_max3_f32 %8, %8, %9, 0\n\t" \
        "v_add_f32 %6, %6, %8\n\tv_exp_f32 %9, -%8\n\t" \
        ".set r_, 0\n\t.rept 16\n\tv_sub_f32 v[" N_##S "+r_], v[" N_##S "+r_], %8\n\tv_sub_f32 v[144+r_], 0, %6\n\t.set r_, r_+1\n\t.endr\n\t" \
        "v_mul_f32 %7, %7, %9\n\t" \
        ".set r_, 192\n\t.rept 64\n\tv_mul_f32 v[r_], v[r_], %9\n\t.set r_, r_+1\n\t.endr\n\ts_nop 1\n\ts_branch .Lrb%=\n\t.subsection 0" \
        : "+" R_O0(O0), "+" R_O1(O1), "+" R_O2(O2), "+" R_O3(O3), "+" R_##S(S), "+" R_NM(negm), "+v"(mrow), "+v"(lsum), "+v"(mx), "=&v"(t0_) : "s"(thr), "v"(mxb) : "vcc"); } while (0)
#define G1(SN, SO, s, CIN, PK, KL) do { SN = MFMA32(kf[s], qf[s], CIN); PINR(SN); SBAR(); PINR(SO); KL; SO[2 * (s)] = EXP2(SO[2 * (s)]); SO[2 * (s) + 1] = EXP2(SO[2 * (s) + 1]); PK; PINR(SO); SBAR(); } while (0)
#define PKW(SO, W, i, r) do { W[i] = pk2(SO[r], SO[(r) + 1]); PIN(W); } while (0)
#define PH1(SN, SO, KL) do { SBAR(); \
    G1(SN, SO, 0, negm, (void)0, KL(0));            G1(SN, SO, 1, SN, PKW(SO, pw0, 0, 0), KL(1)); G1(SN, SO, 2, SN, PKW(SO, pw0, 1, 2), KL(2));  G1(SN, SO, 3, SN, PKW(SO, pw0, 2, 4), KL(3)); \
    G1(SN, SO, 4, SN, PKW(SO, pw0, 3, 6), KL(4));   G1(SN, SO, 5, SN, PKW(SO, pw1, 0, 8), KL(5)); G1(SN, SO, 6, SN, PKW(SO, pw1, 1, 10), KL(6)); G1(SN, SO, 7, SN, PKW(SO, pw1, 2, 12), KL(7)); } while (0)
#define G2(g, OC, W, X) do { OC = MFMA32(VF(g), __builtin_bit_cast(bf16x8, W), OC); PINR(OC); SBAR(); X; SBAR(); } while (0)
#define ADD4(SO, r) do { ls0 += SO[r]; ls1 += SO[(r) + 1]; ls0 += SO[(r) + 2]; ls1 += SO[(r) + 3]; PIN(ls0); PIN(ls1); } while (0)
#define V2(g, vo) do { VRDH(g, 0, vo); VRDH(g, 1, vo); } while (0)
#define PH2(SN, SO, vo, A0, A1, A2, A3) do { SBAR(); \
    G2(0, O0, pw0, { V2(0, vo); PKW(SO, pw1, 3, 14); ls0 = lsum + SO[0]; ls1 = SO[1] + SO[3]; ls0 += SO[2]; PIN(ls0); PIN(ls1); }); G2(1, O1, pw0, { V2(1, vo); ADD4(SO, 4); }); \
    G2(2, O2, pw0, { V2(2, vo); ADD4(SO, 8); }); G2(3, O3, pw0, { V2(3, vo); ADD4(SO, 12); }); \
    G2(4, O0, pw1, { V2(4, vo); mx0 = MX3(SN[0], SN[1], SN[2]); mx1 = MX3(SN[3], SN[4], SN[5]); lsum = ls0 + ls1; PIN(mx0); PIN(mx1); PIN(lsum); A0; }); \
    G2(5, O1, pw1, { V2(5, vo); mx0 = MX3(mx0, SN[6], SN[7]); mx1 = MX3(mx1, SN[8], SN[9]); PIN(mx0); PIN(mx1); A1; }); \
    G2(6, O2, pw1, { V2(6, vo); mx0 = MX3(mx0, SN[10], SN[11]); mx1 = MX3(mx1, SN[12], SN[13]); PIN(mx0); PIN(mx1); A2; }); \
    G2(7, O3, pw1, { V2(7, vo); mx0 = MX3(mx0, SN[14], SN[15]); A3; }); \
    RESCALE(SN, mx0, mx1, THR); } while (0)
#define KL0(s) KRD(s, 0)
#define KL1(s) KRD(s, 8192)
#define KLN(s) (void)0
#define NOP_ (void)0
    unsigned ka[8], va[4]; s16x4 vh[16]; float ls0, ls1, mx0, mx1;
    {
        const unsigned ko = acquire();
#pragma unroll
        for (int s = 0; s < 8; ++s) { KADDR(s, ko); KRD(s, 0); }
        SA = MFMA32(kf[0], qf[0], (f32x16){});
#pragma unroll
        for (int s = 1; s < 8; ++s) SA = MFMA32(kf[s], qf[s], SA);
#pragma unroll
        for (int s = 0; s < 8; ++s) KRD(s, 8192);
        float mx = MX3(SA[0], SA[1], SA[2]);
#pragma unroll
        for (int r = 3; r < 15; r += 2) mx = MX3(mx, SA[r], SA[r + 1]);
        mx = swap_max(__builtin_fmaxf(mx, SA[15])); mrow = mx;
#pragma unroll
        for (int r = 0; r < 16; ++r) { SA[r] -= mx; negm[r] = -mx; }
        asm volatile("" : "+" R_NM(negm));
    }

    STAMP(6);
    unsigned vcur, kcur;
    { const unsigned vo = acquire(); VADDR(0, vo); VADDR(1, vo); VADDR(2, vo); VADDR(3, vo); vcur = vo; kcur = so;
#pragma unroll
      for (int g = 0; g < 8; ++g) V2(g, 0);
#pragma unroll
      for (int s = 0; s < 8; ++s) KADDR(s, so); }
    STAMP(8);
    int tnext = (int)(unsigned)__builtin_amdgcn_s_memrealtime(), per = roleB ? PB0 : PA0;
#pragma nounroll
    for (int t = 0; t < NT - 1; ++t) {
        (void)acquire();
        {
            int now = (int)(unsigned)__builtin_amdgcn_s_memrealtime();
#pragma nounroll
            while (now - tnext < 0) { __builtin_amdgcn_s_sleep(1); now = (int)(unsigned)__builtin_amdgcn_s_memrealtime(); }
            tnext = now + (t >= (roleB ? PENDB : PEND) ? 0 : per); if (!roleB) per = per - PA1 > PMIN ? per - PA1 : PMIN;
        }
        STAMP2(8 + t);
        LGKM(15);
        PH1(SB, SA, KL0);
        LGKM(8);
        PH2(SB, SA, 8192, NOP_, NOP_, NOP_, NOP_);
        LGKM(15);
        PH1(SA, SB, KL1);
        release(u - 1);
        const unsigned vo = acquire();
        const unsigned dv = vo - vcur, dk = so - kcur; vcur = vo; kcur = so;
        VSTEP(0, dv); VSTEP(1, dv); VSTEP(2, dv); VSTEP(3, dv);
        LGKM(8);
        PH2(SA, SB, 0, { KSTEP(0, dk); KSTEP(1, dk); }, { KSTEP(2, dk); KSTEP(3, dk); }, { KSTEP(4, dk); KSTEP(5, dk); }, { KSTEP(6, dk); KSTEP(7, dk); });
    }
    u64 fpv = 0;
    if (!roleB) fpv = __hip_atomic_load((GAS const u64*)(ctl + 512), RLX_AGENT);
    STAMP(30);
    PH1(SB, SA, KLN);
    release(u - 1);
    u32x4 pf[8]; float plse = 0.f; bool early = false;
#define PLOAD() do { _Pragma("unroll") for (int i = 0; i < 8; ++i) pf[i] = __builtin_amdgcn_raw_buffer_load_b128(wsr, (unsigned)WS_PX + fx + i * 1024, 0, 16); \
        plse = __uint_as_float(__builtin_amdgcn_raw_buffer_load_b32(wsr, (unsigned)WS_PL + (pair * 4 + rg) * 256 + lane * 4, 0, 16)); } while (0)
    if (!roleB && rfl64(fpv) == tag) { early = true; asm volatile("" ::: "memory"); PLOAD(); }
    PH2(SB, SA, 8192, NOP_, NOP_, NOP_, NOP_);
    {
#pragma unroll
        for (int r = 0; r < 16; ++r) { SB[r] = EXP2(SB[r]); lsum += SB[r]; }
        pw0 = (u32x4){pk2(SB[0], SB[1]), pk2(SB[2], SB[3]), pk2(SB[4], SB[5]), pk2(SB[6], SB[7])};
        pw1 = (u32x4){pk2(SB[8], SB[9]), pk2(SB[10], SB[11]), pk2(SB[12], SB[13]), pk2(SB[14], SB[15])};
        const bf16x8 p0 = __builtin_bit_cast(bf16x8, pw0), p1 = __builtin_bit_cast(bf16x8, pw1);
        O0 = MFMA32(VF(0), p0, O0); O1 = MFMA32(VF(1), p0, O1); O2 = MFMA32(VF(2), p0, O2); O3 = MFMA32(VF(3), p0, O3);
        O0 = MFMA32(VF(4), p1, O0); O1 = MFMA32(VF(5), p1, O1); O2 = MFMA32(VF(6), p1, O2); O3 = MFMA32(VF(7), p1, O3);
    }
    release(u);
    STAMP(3);
    lsum = swap_add(lsum);
    const float lse = mrow + __builtin_amdgcn_logf(lsum);
    if (!roleB) {
        if (!early) { poll_flag(ctl + 512, tag, budget); PLOAD(); }
        STAMP(5);
        const float M = __builtin_fmaxf(lse, plse), eb = EXP2(lse - M), ea = EXP2(plse - M), r = __builtin_amdgcn_rcpf(ea + eb);
        const float fb = eb * r * __builtin_amdgcn_rcpf(lsum), fa = ea * r;
#pragma unroll
        for (int f = 0; f < 8; ++f) { const f32x16& a = f < 2 ? O0 : f < 4 ? O1 : f < 6 ? O2 : O3; const int q = 8 * (f & 1); u32x4 w;
#pragma unroll
            for (int e = 0; e < 4; ++e) w[e] = pk2(__builtin_fmaf(a[q + 2 * e], fb, fa * __uint_as_float(pf[f][e] << 16)), __builtin_fmaf(a[q + 2 * e + 1], fb, fa * __uint_as_float(pf[f][e] & 0xffff0000u)));
            __builtin_amdgcn_raw_buffer_store_b128(w, wsr, (unsigned)WS_OX + fx + f * 1024, 0, 16); }
    } else {
        const float inv = __builtin_amdgcn_rcpf(lsum);
#pragma unroll
        for (int f = 0; f < 8; ++f) { const f32x16& a = f < 2 ? O0 : f < 4 ? O1 : f < 6 ? O2 : O3; const int r = 8 * (f & 1);
            u32x4 w = {pk2(a[r] * inv, a[r + 1] * inv), pk2(a[r + 2] * inv, a[r + 3] * inv), pk2(a[r + 4] * inv, a[r + 5] * inv), pk2(a[r + 6] * inv, a[r + 7] * inv)};
            __builtin_amdgcn_raw_buffer_store_b128(w, wsr, (unsigned)WS_PX + fx + f * 1024, 0, 16); }
        __builtin_amdgcn_raw_buffer_store_b32(__float_as_uint(lse), wsr, (unsigned)WS_PL + (pair * 4 + rg) * 256 + lane * 4, 0, 16);
    }
    signoff(1, roleB ? ctl + 512 : ctl + 768);
    if (roleB) opengate(1);
    STAMP(4);
    {
        const int qh = rg & 1, nh = rg >> 1;
        *(volatile LAS u64*)(lds + L_TAG) = tag;
        if (!NOWO) { u += NSL - 1; (void)acquire(); }
        STAMP(7);
        unsigned wa[4], wh = lds0 + 131072 + (nh * 64 + hi) * 128 + 32 * q4 + 128 * blk + 8 * p4; PIN(wh);
#pragma unroll
        for (int c = 0; c < 4; ++c) { wa[c] = lds0 + ((((nh ^ (c >> 1)) * 64 + hi) * 4 + q4) * 256 + 64 * ((2 * nh + (c & 1)) ^ q4) + 32 * blk + 8 * p4); PIN(wa[c]); }
#define AKS(i) ((i) < 48 ? (i) / 3 : 16 + ((i) - 48) / 2)
#define ATL(i) ((i) < 48 ? (i) % 3 : ((i) - 48) % 2)
#define AWA(i) wa[((i) < 48 ? 0 : 2) + (ATL(i) & 1)]
#define ARD(i) (ATL(i) == 2 ? trrd2(LP(wh) + AKS(i) * 512, LP(wh) + AKS(i) * 512 + 256) : trrd2(LP(AWA(i)) + (AKS(i) & 15) * 4096, LP(AWA(i)) + (AKS(i) & 15) * 4096 + 2048))
        constexpr int AD = 7;
        bf16x8 af[AD];
#pragma unroll
        for (int i = 0; i < AD; ++i) af[i] = ARD(i);
        const int pr0 = b * HKV + ks * 2 + nh, pr1 = pr0 ^ 1;
        bf16x8 of[32];
#pragma nounroll
        while (!__all(__hip_atomic_load((GAS const u64*)(ws + WS_CT + (size_t)(pr0 ^ (lane & 1)) * 1024 + 768), RLX_AGENT) == tag) && --budget > 0) {}
        asm volatile("" ::: "memory");
        STAMP(40);
        const unsigned ob0 = (unsigned)WS_OX + (pr0 * 4 + qh) * 8192 + lane * 16, ob1 = (unsigned)WS_OX + (pr1 * 4 + qh) * 8192 + lane * 16;
#define OLD(f) of[f] = __builtin_bit_cast(bf16x8, __builtin_amdgcn_raw_buffer_load_b128(wsr, ((f) < 16 ? ob0 : ob1) + ((f) & 3) * 1024, (((f) >> 3) & 1) * 16384 + (((f) >> 2) & 1) * 4096, 16))
#pragma unroll
        for (int f = 0; f < OPRE; ++f) OLD(f);
        f32x16 acc[3] = {};
        SBAR();
#pragma unroll
        for (int i = 0; i < 80; ++i) { acc[ATL(i)] = MFMA32(af[i % AD], of[AKS(i)], acc[ATL(i)]); PIN(acc[ATL(i)]); SBAR(); if (i + AD < 80) af[i % AD] = ARD(i + AD);
            if ((i & 1) == 0 && OPRE + i / 2 < 32) OLD(OPRE + i / 2); SBAR(); }
        STAMP(41);
        const unsigned sb = (unsigned)WS_SLAB + ((b * 4 + nq) * 8 + (r32 >> 2)) * 20480 + ks * 2560 + ((lane & 3) * 2 + hi) * 16;
#pragma unroll
        for (int k = 0; k < 5; ++k) { const int kk = k == 0 ? 4 : k - 1; const f32x16& a = acc[kk >> 1]; const int r = 8 * (kk & 1);
            const u32x4 v = {pk2(a[r], a[r + 1]), pk2(a[r + 2], a[r + 3]), pk2(a[r + 4], a[r + 5]), pk2(a[r + 6], a[r + 7])};
            __builtin_amdgcn_raw_buffer_store_b128(v, wsr, sb + (kk < 4 ? (qh * 8 + nh * 4 + kk) * 128 : 2048 + (nh * 2 + qh) * 128), 0, 16); }
        STAMP(42);
        signoff(2, fsl + ks * 256);
        STAMP(43);
        tagf = tag;
    }
    }
    {
#pragma nounroll
        while (!__all(__hip_atomic_load((GAS const u64*)(fsl + (lane & 7) * 256), RLX_AGENT) == tagf) && --budget > 0) {}
        asm volatile("" ::: "memory");
        STAMP(44);
        const int e = wid * 32 + r32;
        const unsigned sb = (unsigned)WS_SLAB + ((b * 4 + nq) * 8 + ks) * 20480 + hi * 10240 + e * 16;
        u32x4 v[4];
#pragma unroll
        for (int k = 0; k < 4; ++k) v[k] = __builtin_amdgcn_raw_buffer_load_b128(wsr, sb + k * 2560, 0, 16);
        if (ks == 0 && nq == 0 && tid == 0) __hip_atomic_store((GAS u64*)epo, tagf, RLX_AGENT);
        float s[8];
#pragma unroll
        for (int c = 0; c < 4; ++c) { s[2 * c] = (__uint_as_float(v[0][c] << 16) + __uint_as_float(v[1][c] << 16)) + (__uint_as_float(v[2][c] << 16) + __uint_as_float(v[3][c] << 16));
            s[2 * c + 1] = (__uint_as_float(v[0][c] & 0xffff0000u) + __uint_as_float(v[1][c] & 0xffff0000u)) + (__uint_as_float(v[2][c] & 0xffff0000u) + __uint_as_float(v[3][c] & 0xffff0000u)); }
        float t[4];
#pragma unroll
        for (int c = 0; c < 4; ++c) { auto rr = __builtin_amdgcn_permlane32_swap(__float_as_uint(s[c]), __float_as_uint(s[4 + c]), false, false); t[c] = __uint_as_float(rr[0]) + __uint_as_float(rr[1]); }
        int qh = e >> 6, col = nq * 128 + ((e >> 3) & 7) * 16;
        if (wid == 4) {
#pragma unroll
            for (int c = 0; c < 4; ++c) t[c] += __shfl_xor(t[c], 16);
            qh = (e >> 3) & 1; col = 512 + nq * 16; }
        if (e < 144) *(u32x2*)(out + ((size_t)(b * SQ + qh * 32 + ks * 4 + ((e >> 1) & 3)) * DM + col + hi * 8 + (e & 1) * 4) * 2) = (u32x2){pk2(t[0], t[1]), pk2(t[2], t[3])};
    }
    STAMP(45);
    STAMP_OUT(0, 0);
}

extern "C" void kernel_launch(void* const* d_in, const int* in_sizes, int n_in, void* d_out, int out_size, void* d_ws, size_t ws_size, hipStream_t stream) {
    (void)hipFuncSetAttribute((const void*)attn_k, hipFuncAttributeMaxDynamicSharedMemorySize, LDS1);
    hipLaunchKernelGGL(attn_k, dim3(256), dim3(512), LDS1, stream, (const char*)d_in[0], (const char*)d_in[1], (const char*)d_in[2], (const char*)d_in[3], (const char*)d_in[4], (char*)d_out, (char*)d_ws);
}
```

```cpp
#include <hip/hip_runtime.h>
#include <hip/hip_bf16.h>
#include <stdint.h>

typedef short bf16x8 __attribute__((ext_vector_type(8)));
typedef short s16x4 __attribute__((ext_vector_type(4)));
typedef float f32x16 __attribute__((ext_vector_type(16)));
typedef float f32x2 __attribute__((ext_vector_type(2)));
typedef unsigned u32x4 __attribute__((ext_vector_type(4)));
typedef unsigned u32x2 __attribute__((ext_vector_type(2)));
typedef __bf16 bf2_t __attribute__((ext_vector_type(2)));
typedef unsigned long long u64;
#define LAS __attribute__((address_space(3)))
#define GAS __attribute__((address_space(1)))
typedef LAS const char* lcptr;
typedef LAS char* lptr;
#define DI __device__ __forceinline__
#define AINL __attribute__((always_inline))
#define MFMA32(a, b, c) __builtin_amdgcn_mfma_f32_32x32x16_bf16((a), (b), (c), 0, 0, 0)
#define WAITV(N) asm volatile("s_waitcnt vmcnt(" #N ")" ::: "memory")
#define BAR() asm volatile("s_barrier" ::: "memory")
#define PIN(x) asm volatile("" : "+v"(x))
#define SBAR() __builtin_amdgcn_sched_barrier(0)
#define LGKM(n) do { __builtin_amdgcn_s_waitcnt(0xC07F | ((n) << 8)); SBAR(); } while (0)
#define LP(a) ((lcptr)(uintptr_t)(a))
#define STR2(x) #x
#define STR(x) STR2(x)
#define CLOB_V38_255 "v38", "v39", "v40", "v41", "v42", "v43", "v44", "v45", "v46", "v47", "v48", "v49", "v50", "v51", "v52", "v53", "v54", "v55", "v56", "v57", "v58", "v59", "v60", "v61", "v62", "v63", "v64", "v65", "v66", "v67", "v68", "v69", "v70", "v71", "v72", "v73", "v74", "v75", "v76", "v77", "v78", "v79", "v80", "v81", "v82", "v83", "v84", "v85", "v86", "v87", "v88", "v89", "v90", "v91", "v92", "v93", "v94", "v95", "v96", "v97", "v98", "v99", "v100", "v101", "v102", "v103", "v104", "v105", "v106", "v107", "v108", "v109", "v110", "v111", "v112", "v113", "v114", "v115", "v116", "v117", "v118", "v119", "v120", "v121", "v122", "v123", "v124", "v125", "v126", "v127", "v128", "v129", "v130", "v131", "v132", "v133", "v134", "v135", "v136", "v137", "v138", "v139", "v140", "v141", "v142", "v143", "v144", "v145", "v146", "v147", "v148", "v149", "v150", "v151", "v152", "v153", "v154", "v155", "v156", "v157", "v158", "v159", "v160", "v161", "v162", "v163", "v164", "v165", "v166", "v167", "v168", "v169", "v170", "v171", "v172", "v173", "v174", "v175", "v176", "v177", "v178", "v179", "v180", "v181", "v182", "v183", "v184", "v185", "v186", "v187", "v188", "v189", "v190", "v191", "v192", "v193", "v194", "v195", "v196", "v197", "v198", "v199", "v200", "v201", "v202", "v203", "v204", "v205", "v206", "v207", "v208", "v209", "v210", "v211", "v212", "v213", "v214", "v215", "v216", "v217", "v218", "v219", "v220", "v221", "v222", "v223", "v224", "v225", "v226", "v227", "v228", "v229", "v230", "v231", "v232", "v233", "v234", "v235", "v236", "v237", "v238", "v239", "v240", "v241", "v242", "v243", "v244", "v245", "v246", "v247", "v248", "v249", "v250", "v251", "v252", "v253", "v254", "v255"
#define RLX_AGENT __ATOMIC_RELAXED, __HIP_MEMORY_SCOPE_AGENT
#define RLX_WG __ATOMIC_RELAXED, __HIP_MEMORY_SCOPE_WORKGROUP
#define AUX_SC1 16

#define NTA 18
#define GATEA 10
#define HPRE 2
#define PA0 110
#define PA1 2
#define PMINA 103
#define PB0 150
#define PB1 9
#define PMINB 103
#define PEND 13

constexpr int SQ = 64, DM = 576, HKV = 16, SK = 2048;
constexpr float QSCALE = 0.08838834764831845f * 1.4426950408889634f;
constexpr float THR = 8.f;
constexpr int NT_A = NTA, NT_B = 32 - NTA;
constexpr int USLOT = 16384, NSL = 9, MISC = NSL * USLOT, LDS_BYTES = 163840;
constexpr int L_LANDED = MISC, L_DONE = MISC + 16, L_CNT = MISC + 32, L_GATE = MISC + 44, L_TAG = MISC + 64;
constexpr int SPIN_CAP = 1 << 20;
constexpr size_t WS_CT = 0;
constexpr size_t WS_EP = 128 * 1024, WS_FS = WS_EP + 8 * 256;
constexpr size_t WS_QX = 1 << 20;
constexpr size_t WS_PX = WS_QX + ((size_t)4 << 20);
constexpr size_t WS_PL = WS_PX + ((size_t)4 << 20);
constexpr size_t WS_OX = WS_PL + ((size_t)1 << 20);
constexpr size_t WS_SLAB = WS_OX + ((size_t)4 << 20);
constexpr int WS_USED = 32 << 20;

DI unsigned pk2(float a, float b) { bf2_t r = __builtin_convertvector((f32x2){a, b}, bf2_t); return __builtin_bit_cast(unsigned, r); }
DI float bflo(unsigned v) { return __uint_as_float(v << 16); }
DI float bfhi(unsigned v) { return __uint_as_float(v & 0xffff0000u); }
DI s16x4 trrd(lcptr p) { typedef short v4i16_t __attribute__((ext_vector_type(4))); return __builtin_bit_cast(s16x4, __builtin_amdgcn_ds_read_tr16_b64_v4i16((LAS v4i16_t*)p)); }
DI bf16x8 trrd2(lcptr p0, lcptr p1) { s16x4 a = trrd(p0), b = trrd(p1); return __builtin_shufflevector(a, b, 0, 1, 2, 3, 4, 5, 6, 7); }
DI bf16x8 ldsrd16(lcptr p) { return *(LAS const bf16x8*)p; }
DI float swap_max(float m) { auto rr = __builtin_amdgcn_permlane32_swap(__float_as_uint(m), __float_as_uint(m), false, false); return __builtin_fmaxf(__uint_as_float(rr[0]), __uint_as_float(rr[1])); }
DI float swap_add(float m) { auto rr = __builtin_amdgcn_permlane32_swap(__float_as_uint(m), __float_as_uint(m), false, false); return __uint_as_float(rr[0]) + __uint_as_float(rr[1]); }
DI u64 next_tag(u64 e) { return e * 6364136223846793005ull + 1442695040888963407ull; }
DI u64 rfl64(u64 v) { return ((u64)(unsigned)__builtin_amdgcn_readfirstlane((unsigned)(v >> 32)) << 32) | (unsigned)__builtin_amdgcn_readfirstlane((unsigned)v); }
DI void lds_put(unsigned a, unsigned val) { asm volatile("ds_write_b32 %0, %1" :: "v"(a), "v"(val) : "memory"); }
DI void poll_flags(const char* p, u64 tag, int& budget) {
#pragma nounroll
    while (!__all(__hip_atomic_load((GAS const u64*)p, RLX_AGENT) == tag) && --budget > 0) {}
    asm volatile("" ::: "memory"); }
DI void wait_lds(lcptr p, unsigned need, int& budget) {
#pragma nounroll
    while ((unsigned)__builtin_amdgcn_readfirstlane(*(volatile LAS const unsigned*)p) < need && --budget > 0) __builtin_amdgcn_s_sleep(1); }

#define DMA4(POL, sbase, v0, v1, v2, v3, ldst) do { unsigned sv_; asm volatile("s_mov_b32 %0, m0\n\ts_mov_b32 m0, %6\n\ts_nop 0\n\t" \
    "global_load_lds_dwordx4 %1, %5" POL "\n\tglobal_load_lds_dwordx4 %2, %5 offset:1024" POL "\n\tglobal_load_lds_dwordx4 %3, %5 offset:2048" POL "\n\tglobal_load_lds_dwordx4 %4, %5 offset:3072" POL "\n\t" \
    "s_mov_b32 m0, %0" : "=&s"(sv_) : "v"(v0), "v"(v1), "v"(v2), "v"(v3), "s"(sbase), "s"(__builtin_amdgcn_readfirstlane(ldst)) : "memory"); } while (0)

__global__ __launch_bounds__(512, 2) void gqa_block_k(const char* __restrict__ X, const char* __restrict__ Kc, const char* __restrict__ Vc, const char* __restrict__ Wq,
                                                      const char* __restrict__ Wo, char* __restrict__ out, char* __restrict__ ws) {
    extern __shared__ __attribute__((aligned(16))) char lds_[];
    const int tid = threadIdx.x, lane = tid & 63, r32 = lane & 31, hi = lane >> 5;
    const int wid = __builtin_amdgcn_readfirstlane(tid >> 6);
    const int bid = blockIdx.x, ks = bid & 7, idx = bid >> 3;
    const int kvh = ks * 2 + (idx & 1), b = (idx >> 1) & 7;
    const bool roleB = idx < 16;
    const int pair = b * HKV + kvh;
    const int nq = (idx & 1) + (roleB ? 0 : 2);
    const lptr lds = (lptr)lds_;
    const unsigned lds0 = (unsigned)(uintptr_t)lds;
    const int NT = roleB ? NT_B : NT_A;
    char* const ctl = ws + WS_CT + (size_t)pair * 1024;
    char* const epo = ws + WS_EP + b * 256;
    char* const fsl = ws + WS_FS + (size_t)(b * 4 + nq) * 8 * 256;
    const __amdgpu_buffer_rsrc_t wsr = __builtin_amdgcn_make_buffer_rsrc(ws, 0, WS_USED, 0x00020000);
    u64 pc0; unsigned code_len;
    asm volatile("s_getpc_b64 %0\n.Lcs%=:\n\ts_mov_b32 %1, .Lce%=-.Lcs%=\n\t.subsection 2\n.Lce%=:\n\t.fill 128, 4, 0xBF800000\n\t.subsection 0" : "=s"(pc0), "=s"(code_len));
    if (tid < 16) *(LAS unsigned*)(lds + MISC + tid * 4) = 0u;
    int budget = SPIN_CAP;
    u64 tagf;

    if (wid >= 4) {
        const int j = wid - 4, lr = lane >> 4, lc = lane & 15;
        const size_t koff = ((size_t)pair * SK + (roleB ? NT_A * 64 : 0)) * 256;
        const char* kp = Kc + koff; const char* vp = Vc + koff;
        unsigned vK[4];
#pragma unroll
        for (int i = 0; i < 4; ++i) vK[i] = (unsigned)((16 * j + lr) * 256 + 16 * (lc ^ (4 * i + lr)));
        const unsigned vV = (unsigned)((16 * j + lr) * 256 + 16 * (lc ^ (lr << 2)));
        unsigned a_landed = lds0 + L_LANDED + 4 * j, a_done = lds0 + L_DONE; PIN(a_landed); PIN(a_done);
        if (roleB) {
            unsigned vX[2], vW[4];
#pragma unroll
            for (int i = 0; i < 2; ++i) { const int row = 16 * j + 8 * i + (lane >> 3); vX[i] = (unsigned)(row * 1152 + 16 * ((lane & 7) ^ ((row >> 1) & 7)) - 1024 * i); }
#pragma unroll
            for (int i = 0; i < 4; ++i) vW[i] = (unsigned)((8 * j + 2 * i + hi) * 8192 + 16 * (r32 ^ (((2 * i + hi) & 3) << 2)) - 1024 * i);
            const char* xb = X + (size_t)b * SQ * DM * 2; const char* wb = Wq + kvh * 512;
            int xo = b * 128, sb = 0, cnt = 7;
#define QADV "s_add_i32 %[xo], %[xo], 128\n\ts_cmp_eq_u32 %[xo], 1152\n\ts_cselect_b32 %[xo], 0, %[xo]\n\ts_add_i32 %[sb], %[sb], 0xc000\n\ts_cmp_eq_u32 %[sb], 0x24000\n\ts_cselect_b32 %[sb], 0, %[sb]\n\t"
#define QW4 "global_load_lds_dwordx4 %[w0], s[60:61]\n\tglobal_load_lds_dwordx4 %[w1], s[60:61] offset:1024\n\tglobal_load_lds_dwordx4 %[w2], s[60:61] offset:2048\n\tglobal_load_lds_dwordx4 %[w3], s[60:61] offset:3072\n\t"
#define QEVEN0 "s_add_u32 s62, %[xl], %[xo]\n\ts_addc_u32 s63, %[xh], 0\n\ts_lshl_b32 s66, %[xo], 12\n\ts_add_u32 s60, %[wl], s66\n\ts_addc_u32 s61, %[wh], 0\n\ts_add_i32 m0, %[sb], %[dx]\n\ts_add_i32 s66, %[sb], %[dw]\n\t"
#define QEVEN1 "global_load_lds_dwordx4 %[x0], s[62:63]\n\tglobal_load_lds_dwordx4 %[x1], s[62:63] offset:1024\n\ts_mov_b32 m0, s66\n\ts_nop 0\n\t" QW4
#define QODD0 "s_add_u32 s60, s60, 0x40000\n\ts_addc_u32 s61, s61, 0\n\ts_add_i32 m0, m0, 0x4000\n\t"
            asm volatile(
                "s_mov_b32 s70, m0\n\t"
                QEVEN0 QEVEN1 QODD0 "s_nop 0\n\t" QW4 QADV QEVEN0 QEVEN1 QODD0
                "s_waitcnt vmcnt(10)\n"
                ".Lqr%=:\n\t"
                "s_barrier\n\t" QW4 QADV QEVEN0 "s_waitcnt vmcnt(10)\n\t"
                "s_barrier\n\t" QEVEN1 QODD0 "s_sub_i32 %[cnt], %[cnt], 1\n\ts_cmp_gt_i32 %[cnt], 0\n\ts_waitcnt vmcnt(10)\n\ts_cbranch_scc1 .Lqr%=\n\t"
                "s_barrier\n\t" QW4 "s_waitcnt vmcnt(10)\n\t"
                "s_barrier\n\ts_waitcnt vmcnt(4)\n\t"
                "s_barrier\n\ts_waitcnt vmcnt(0)\n\t"
                "s_barrier\n\ts_mov_b32 m0, s70"
                : [xo] "+&s"(xo), [sb] "+&s"(sb), [cnt] "+&s"(cnt)
                : [xl] "s"((unsigned)(uintptr_t)xb), [xh] "s"((unsigned)((uintptr_t)xb >> 32)), [wl] "s"((unsigned)(uintptr_t)wb), [wh] "s"((unsigned)((uintptr_t)wb >> 32)),
                  [dx] "s"(lds0 + j * 2048), [dw] "s"(lds0 + USLOT + j * 4096),
                  [x0] "v"(vX[0]), [x1] "v"(vX[1]), [w0] "v"(vW[0]), [w1] "v"(vW[1]), [w2] "v"(vW[2]), [w3] "v"(vW[3])
                : "memory", "scc", "s60", "s61", "s62", "s63", "s66", "s70");
        } else {
            DMA4(" nt", kp, vK[0], vK[1], vK[2], vK[3], lds0 + j * 4096); DMA4(" nt", vp, vV, vV, vV, vV, lds0 + USLOT + j * 4096);
            BAR();
        }
        const char* wb = Wo + (size_t)ks * 512 * 1152;
        unsigned oF[4], oH[4];
#pragma unroll
        for (int i = 0; i < 4; ++i) { oF[i] = (unsigned)(((4 * j + i) * 4 + lr) * 1152 + nq * 256 + 16 * (lc ^ (lr << 2)) - 1024 * i);
            oH[i] = (unsigned)((j * 128 + i * 32 + (lane >> 1)) * 1152 + 1024 + nq * 32 + (lane & 1) * 16 - 1024 * i); }
        const unsigned wl = (unsigned)(lds0 + j * 4096 + lane * 16), a_gate = lds0 + L_GATE;
        const int nkv = 2 * NT, ntot = roleB ? nkv : nkv + NSL, n0 = roleB ? 0 : 2;
        int ni = n0, nl = 0, nw = 0, bi = 0, bw = 0, gate = roleB ? 0 : GATEA, si = n0 * USLOT, sw = 0, lim = NSL, wc = roleB ? 9 : 0;
        static_assert(GATEA >= NSL && GATEA <= NSL + 4, "four register buffers");
#define WQUOTA(L) "s_sub_i32 s67, %[ni], " STR(GATEA) "\n\ts_lshr_b32 s67, s67, 1\n\ts_add_i32 s67, s67, 3\n\ts_cmp_ge_i32 %[wc], s67\n\ts_cbranch_scc1 " L "\n\t"
        asm volatile(
            "s_mov_b32 s70, m0\n\ts_mov_b64 s[72:73], 0\n\ts_mov_b32 s75, 0\n\ts_getpc_b64 s[64:65]\n"
            ".Lp%=:\n\t"
            "s_branch .Lprep%=\n"
            ".Ltop%=:\n\t"
            "s_sub_i32 s66, %[ni], %[nl]\n\ts_cmp_lt_i32 s66, 3\n\ts_cbranch_scc1 .Lc1%=\n\t"
            "s_waitcnt vmcnt(8)\n\ts_add_i32 %[nl], %[nl], 1\n\t"
            ".Lc1%=:\n\t"
            "s_waitcnt lgkmcnt(0)\n\tv_min_u32 v42, v42, v43\n\tv_min3_u32 v40, v40, v41, v42\n\ts_mov_b32 s71, 0\n\ts_cmp_lt_i32 %[nw], %[nl]\n\tv_readfirstlane_b32 s67, v40\n\t"
            "s_cbranch_scc0 .Lc2%=\n\t"
            "s_bitcmp1_b64 s[72:73], %[nw]\n\ts_cbranch_scc1 .Lwr%=\n"
            ".Lpb%=:\n\t"
            "s_add_i32 %[nw], %[nw], 1\n\tv_mov_b32 v38, %[nw]\n\tds_write_b32 %[aland], v38\n\t"
            "s_add_i32 %[sw], %[sw], 0x4000\n\ts_cmp_eq_u32 %[sw], 0x24000\n\ts_cselect_b32 %[sw], 0, %[sw]\n\ts_mov_b32 s71, 1\n\t"
            "s_cmp_lt_i32 %[nw], %[nl]\n\ts_cbranch_scc1 .Lmore%=\n"
            ".Lc2%=:\n\t"
            "s_add_i32 s67, s67, 9\n\ts_max_i32 %[lim], %[lim], s67\n\t"
            "s_cmp_ge_i32 %[ni], %[gate]\n\ts_cbranch_scc1 .Lno%=\n\t"
            "s_cmp_lt_i32 %[ni], %[lim]\n\ts_cbranch_scc0 .Lis%=\n\t"
            "global_load_lds_dwordx4 v44, s[60:61] nt\n\tglobal_load_lds_dwordx4 v45, s[60:61] offset:1024 nt\n\t"
            "global_load_lds_dwordx4 v46, s[60:61] offset:2048 nt\n\tglobal_load_lds_dwordx4 v47, s[60:61] offset:3072 nt\n"
            ".Lbk%=:\n\t"
            "s_add_i32 %[ni], %[ni], 1\n\ts_mov_b32 s75, 1\n\t"
            "s_add_i32 %[si], %[si], 0x4000\n\ts_cmp_eq_u32 %[si], 0x24000\n\ts_cselect_b32 %[si], 0, %[si]\n"
            ".Lprep%=:\n\t"
            "ds_read_b128 v[40:43], %[adone]\n\t"
            "s_add_i32 m0, %[si], %[dbase]\n\t"
            "s_lshr_b32 s66, %[ni], 1\n\ts_lshl_b32 s66, s66, 14\n\t"
            "s_bitcmp1_b32 %[ni], 0\n\ts_cselect_b32 s60, %[vpl], %[kpl]\n\ts_cselect_b32 s61, %[vph], %[kph]\n\ts_cselect_b64 vcc, -1, 0\n\t"
            "s_add_u32 s60, s60, s66\n\ts_addc_u32 s61, s61, 0\n\t"
            "v_cndmask_b32 v44, %[k0], %[vv], vcc\n\tv_cndmask_b32 v45, %[k1], %[vv], vcc\n\tv_cndmask_b32 v46, %[k2], %[vv], vcc\n\tv_cndmask_b32 v47, %[k3], %[vv], vcc\n\t"
            "s_branch .Ltop%=\n"
            ".Lmore%=:\n\ts_bitcmp1_b64 s[72:73], %[nw]\n\ts_cbranch_scc1 .Lwr%=\n\ts_branch .Lpb%=\n"
            ".Lno%=:\n\t"
            "s_cmp_ge_i32 %[gate], %[nkv]\n\ts_cbranch_scc1 .Lno2%=\n\t"
            "ds_read_b32 v40, %[agate]\n\ts_waitcnt lgkmcnt(0)\n\tv_readfirstlane_b32 s67, v40\n\ts_nop 3\n\t"
            "s_cmp_ge_u32 s67, 4\n\ts_cselect_b32 %[gate], %[g1], %[gate]\n\ts_cmp_ge_u32 s67, 8\n\ts_cselect_b32 %[gate], %[nkv], %[gate]\n\t"
            "s_cmp_lt_i32 %[ni], %[gate]\n\ts_cselect_b32 s71, 1, s71\n"
            ".Lno2%=:\n\t"
            "ds_read_b128 v[40:43], %[adone]\n\t"
            "s_cmp_lg_u32 s71, 0\n\ts_cbranch_scc1 .Ltop%=\n\t"
            "s_sub_i32 s66, %[ni], %[nl]\n\ts_cmp_eq_u32 s66, 0\n\ts_cbranch_scc1 .Lno3%=\n\t"
            "s_cmp_eq_u32 s66, 2\n\ts_cbranch_scc1 .Lw4%=\n\ts_waitcnt vmcnt(0)\n"
            ".Lw4%=:\n\ts_waitcnt vmcnt(4)\n\ts_add_i32 %[nl], %[nl], 1\n\ts_branch .Lc1%=\n"
            ".Lno3%=:\n\ts_cmp_ge_i32 %[nw], %[ntot]\n\ts_cbranch_scc1 .Lend%=\n\t"
            "s_cmp_lt_i32 %[gate], %[nkv]\n\ts_cbranch_scc1 .Lidle%=\n\t"
            "s_cmp_lt_i32 %[wc], 9\n\ts_cbranch_scc1 .Lwo0%=\n\t"
            "s_cmp_eq_u32 %[ni], %[nkv]\n\ts_cbranch_scc0 .Lidle%=\n\ts_cmp_eq_u32 %[ni], %[ntot]\n\ts_cbranch_scc1 .Lidle%=\n\t"
            "s_waitcnt vmcnt(0)\n\ts_lshl_b64 s[62:63], 0x1ff, %[nkv]\n\ts_or_b64 s[72:73], s[72:73], s[62:63]\n\ts_mov_b32 %[ni], %[ntot]\n\ts_mov_b32 %[nl], %[ntot]\n\ts_branch .Lc1%=\n"
            ".Lidle%=:\n\t"
            "s_sleep 1\n\ts_sub_i32 %[bud], %[bud], 1\n\ts_cmp_gt_i32 %[bud], 0\n\ts_cbranch_scc1 .Lc1%=\n\t"
            "s_mov_b32 %[lim], 0x3fffffff\n\ts_mov_b32 %[gate], %[nkv]\n\ts_branch .Lc1%=\n"
            ".Lrf%=:\n\t"
            "s_cmp_eq_u32 s75, 0\n\ts_cbranch_scc1 .Lno2%=\n\ts_cmp_ge_i32 %[wc], 9\n\ts_cbranch_scc1 .Lno2%=\n\t"
            WQUOTA(".Lno2%=")
            "s_mov_b32 s75, 0\n\tds_read_b128 v[40:43], %[adone]\n\ts_branch .Lwo%=\n"
            ".Lwo0%=:\n\t"
            WQUOTA(".Lidle%=")
            "s_waitcnt vmcnt(8)\n"
            ".Lwo%=:\n\t"
            "s_and_b32 s66, %[wc], 7\n\ts_mul_i32 s66, s66, 73728\n\ts_add_u32 s76, %[wpl], s66\n\ts_addc_u32 s77, %[wph], 0\n\t"
            "s_mul_i32 s66, %[wc], 36\n\ts_add_u32 s66, s66, .Lit2%=-.Lp%=\n\ts_add_u32 s62, s64, s66\n\ts_addc_u32 s63, s65, 0\n\t"
            "s_add_i32 %[wc], %[wc], 1\n\t"
            "s_setpc_b64 s[62:63]\n"
            ".Lit2%=:\n\t"
            ".set r_, 112\n\t.rept 8\n\t"
            "global_load_dwordx4 v[r_:r_+3], %[f0], s[76:77] nt\n\tglobal_load_dwordx4 v[r_+4:r_+7], %[f1], s[76:77] offset:1024 nt\n\t"
            "global_load_dwordx4 v[r_+8:r_+11], %[f2], s[76:77] offset:2048 nt\n\tglobal_load_dwordx4 v[r_+12:r_+15], %[f3], s[76:77] offset:3072 nt\n\ts_branch .Lc1%=\n\t"
            ".set r_, r_+16\n\t.endr\n\t"
            "global_load_dwordx4 v[240:243], %[h0], s[76:77] nt\n\tglobal_load_dwordx4 v[244:247], %[h1], s[76:77] offset:1024 nt\n\t"
            "global_load_dwordx4 v[248:251], %[h2], s[76:77] offset:2048 nt\n\tglobal_load_dwordx4 v[252:255], %[h3], s[76:77] offset:3072 nt\n\ts_branch .Lc1%=\n"
            ".Lis%=:\n\t"
            "s_cmp_ge_i32 %[ni], " STR(GATEA) "\n\ts_cbranch_scc1 .Lrf%=\n\t"
            "s_add_u32 s67, %[bi], .Lit%=-.Lp%=\n\ts_add_u32 s62, s64, s67\n\ts_addc_u32 s63, s65, 0\n\t"
            "s_bitset1_b64 s[72:73], %[ni]\n\ts_add_i32 %[bi], %[bi], 36\n\t"
            "s_setpc_b64 s[62:63]\n"
            ".Lit%=:\n\t"
            ".set r_, 48\n\t.rept 4\n\t"
            "global_load_dwordx4 v[r_:r_+3], v44, s[60:61] nt\n\tglobal_load_dwordx4 v[r_+4:r_+7], v45, s[60:61] offset:1024 nt\n\t"
            "global_load_dwordx4 v[r_+8:r_+11], v46, s[60:61] offset:2048 nt\n\tglobal_load_dwordx4 v[r_+12:r_+15], v47, s[60:61] offset:3072 nt\n\ts_branch .Lbk%=\n\t"
            ".set r_, r_+16\n\t.endr\n"
            ".Lwr%=:\n\t"
            "s_add_i32 s66, s67, 9\n\ts_max_i32 %[lim], %[lim], s66\n\t"
            "s_cmp_lt_i32 %[nw], %[lim]\n\ts_cbranch_scc0 .Lc2%=\n\t"
            "v_add_u32 v39, %[sw], %[wl]\n\t"
            "s_lshr_b32 s66, %[sw], 14\n\ts_mul_i32 s66, s66, 36\n\ts_add_i32 s66, s66, 144\n\ts_cmp_lt_i32 %[nw], %[nkv]\n\ts_cselect_b32 s66, %[bw], s66\n\t"
            "s_add_u32 s66, s66, .Lwt%=-.Lp%=\n\ts_add_u32 s62, s64, s66\n\ts_addc_u32 s63, s65, 0\n\t"
            "s_add_i32 %[bw], %[bw], 36\n\t"
            "s_setpc_b64 s[62:63]\n"
            ".Lwt%=:\n\t"
            ".set r_, 48\n\t.rept 13\n\t"
            "ds_write_b128 v39, v[r_:r_+3]\n\tds_write_b128 v39, v[r_+4:r_+7] offset:1024\n\tds_write_b128 v39, v[r_+8:r_+11] offset:2048\n\tds_write_b128 v39, v[r_+12:r_+15] offset:3072\n\ts_branch .Lpb%=\n\t"
            ".set r_, r_+16\n\t.endr\n"
            ".Lend%=:\n\ts_mov_b32 m0, s70\n\ts_waitcnt lgkmcnt(0)"
            : [ni] "+&s"(ni), [nl] "+&s"(nl), [nw] "+&s"(nw), [bi] "+&s"(bi), [bw] "+&s"(bw), [gate] "+&s"(gate), [si] "+&s"(si), [sw] "+&s"(sw), [lim] "+&s"(lim),
              [wc] "+&s"(wc), [bud] "+&s"(budget)
            : [nkv] "s"(nkv), [ntot] "s"(ntot), [g1] "s"(roleB ? HPRE : GATEA), [dbase] "s"(lds0 + j * 4096),
              [kpl] "s"((unsigned)(uintptr_t)kp), [kph] "s"((unsigned)((uintptr_t)kp >> 32)), [vpl] "s"((unsigned)(uintptr_t)vp), [vph] "s"((unsigned)((uintptr_t)vp >> 32)),
              [wpl] "s"((unsigned)(uintptr_t)wb), [wph] "s"((unsigned)((uintptr_t)wb >> 32)),
              [k0] "v"(vK[0]), [k1] "v"(vK[1]), [k2] "v"(vK[2]), [k3] "v"(vK[3]), [vv] "v"(vV), [f0] "v"(oF[0]), [f1] "v"(oF[1]), [f2] "v"(oF[2]), [f3] "v"(oF[3]),
              [h0] "v"(oH[0]), [h1] "v"(oH[1]), [h2] "v"(oH[2]), [h3] "v"(oH[3]),
              [wl] "v"(wl), [adone] "v"(a_done), [agate] "v"(a_gate), [aland] "v"(a_landed)
            : "memory", "scc", "vcc", "s60", "s61", "s62", "s63", "s64", "s65", "s66", "s67", "s70", "s71", "s72", "s73", "s75", "s76", "s77", CLOB_V38_255);
        if (roleB) {
            const unsigned li = (unsigned)((idx * 4 + j) * 40 + (lane < 40 ? lane : 0)), row = li / 5, c = li - row * 5; unsigned d;
            asm volatile("global_load_dword %0, %1, %2\n\ts_waitcnt vmcnt(0)" : "=&v"(d) : "v"(row * 1152 + (c < 4 ? c * 128 : 1024)), "s"(wb) : "memory");
            wait_lds(lds + L_GATE, 12, budget);
            DMA4("", wb, oH[0], oH[1], oH[2], oH[3], lds0 + 8 * USLOT + j * 4096);
#pragma nounroll
            for (int k = 0; k < 8; ++k) { const int p = (b + k) & 7; DMA4("", wb + (size_t)p * 73728, oF[0], oF[1], oF[2], oF[3], lds0 + p * USLOT + j * 4096); }
            WAITV(0); lds_put(a_landed, (unsigned)(nkv + NSL));
        }
        if (j != 0) return;
        wait_lds(lds + L_CNT + 8, 1, budget);
        tagf = rfl64(*(volatile LAS const u64*)(lds + L_TAG));
    } else {
        const int rg = wid;
        __builtin_amdgcn_s_setprio(2);
        if (!roleB) {
            unsigned d[5];
            const unsigned i0 = (idx - 16) * 288 + rg * 72 + lane, i1 = i0 - lane + 64 + (lane & 7);
            unsigned oc = (rg * 64 + lane) * 128; oc = oc < code_len && (idx & 3) == 0 ? oc : 0;
            asm volatile("global_load_dword %0, %5, %10 nt\n\tglobal_load_dword %1, %6, %11\n\tglobal_load_dword %2, %7, %10 nt\n\tglobal_load_dword %3, %8, %11\n\t"
                         "s_waitcnt lgkmcnt(0)\n\ts_barrier\n\tglobal_load_dword %4, %9, %12\n\ts_waitcnt vmcnt(0)"
                         : "=&v"(d[0]), "=&v"(d[1]), "=&v"(d[2]), "=&v"(d[3]), "=&v"(d[4])
                         : "v"((i0 >> 3) * 8192 + ks * 1024 + (i0 & 7) * 128), "v"(i0 * 128), "v"((i1 >> 3) * 8192 + ks * 1024 + (i1 & 7) * 128), "v"(i1 * 128), "v"(oc),
                           "s"(Wq), "s"(X), "s"(pc0) : "memory");
        }
        const u64 epoch = __hip_atomic_load((GAS const u64*)epo, RLX_AGENT);
        u64 tag;
        const int q4 = (lane & 15) >> 2, p4 = lane & 3, blk = (lane >> 4) & 1;
        int u = 0, avail = 0;
        unsigned so = lds0;
        unsigned a_landed = lds0 + L_LANDED, a_done = lds0 + L_DONE + 4 * rg; PIN(a_landed); PIN(a_done);
        auto acquire = [&]() AINL -> unsigned {
            avail = __builtin_amdgcn_readfirstlane(avail); budget = __builtin_amdgcn_readfirstlane(budget);
            asm volatile("s_cmp_gt_i32 %0, %2\n\ts_cbranch_scc0 .Las%=\n"
                         ".Lab%=:\n\t.subsection 1\n"
                         ".Las%=:\n\tds_read_b128 v[140:143], %3\n\ts_waitcnt lgkmcnt(0)\n\tv_min_u32 v142, v142, v143\n\tv_min3_u32 v140, v140, v141, v142\n\ts_nop 1\n\tv_readfirstlane_b32 %0, v140\n\ts_nop 3\n\t"
                         "s_cmp_gt_i32 %0, %2\n\ts_cbranch_scc1 .Lab%=\n\ts_sub_i32 %1, %1, 1\n\ts_cmp_lt_i32 %1, 1\n\ts_cbranch_scc1 .Lab%=\n\ts_sleep 1\n\ts_branch .Las%=\n\t.subsection 0"
                         : "+&s"(avail), "+&s"(budget) : "s"(__builtin_amdgcn_readfirstlane(u)), "v"(a_landed) : "memory", "scc", "v140", "v141", "v142", "v143");
            const unsigned o = so; ++u; so = so == lds0 + (NSL - 1) * USLOT ? lds0 : so + USLOT; return o; };
        auto release = [&](int c) AINL { lds_put(a_done, (unsigned)c); };
        auto signoff = [&](int cnt_idx, char* flag) AINL { WAITV(0);
            unsigned old = 0; if (lane == 0) old = __hip_atomic_fetch_add((LAS unsigned*)(lds + L_CNT + 4 * cnt_idx), 1u, RLX_WG);
            if (__builtin_amdgcn_readfirstlane(old) == 3 && lane == 0) __hip_atomic_store((GAS u64*)flag, tag, RLX_AGENT); };
        auto opengate = [&](unsigned n) AINL { asm volatile("" ::: "memory"); if (lane == 0) __hip_atomic_fetch_add((LAS unsigned*)(lds + L_GATE), n, RLX_WG); };
        const unsigned krd = (unsigned)(256 * r32 + 16 * (hi ^ (r32 & 15)));
        bf16x8 qf[8], kf[8]; unsigned ka[8];
#define KRD(s, off) kf[s] = ldsrd16(LP(ka[s]) + (off))
#define KADDR(s, o) do { ka[s] = (krd ^ ((s) << 5)) + (o); PIN(ka[s]); } while (0)
#define KFIRST() do { const unsigned ko_ = acquire(); _Pragma("unroll") for (int s_ = 0; s_ < 8; ++s_) { KADDR(s_, ko_); KRD(s_, 0); } } while (0)
        const unsigned fx = (unsigned)((pair * 4 + rg) * 8192 + lane * 16);
        if (roleB) {
            const int p4s = ((p4 & 1) << 1) | (p4 >> 1);
            unsigned wrd[4], xa[4];
#pragma unroll
            for (int dt = 0; dt < 4; ++dt) wrd[dt] = (unsigned)((rg >> 1) * 256 + 512 * (8 * hi + q4) + 16 * ((4 * dt + 2 * blk + (p4s >> 1)) ^ (q4 << 2)) + 8 * (p4s & 1));
            { const int row = (rg & 1) * 32 + r32;
#pragma unroll
              for (int s = 0; s < 4; ++s) xa[s] = (unsigned)(row * 128 + 16 * ((2 * s + hi) ^ ((row >> 1) & 7))); }
            f32x16 acc[4] = {};
            bf16x8 xf[4], xn[4], wf[8];
#define WRD(i, wo) wf[i] = trrd2(LP((wo) + wrd[(i) & 3]) + ((i) >> 2) * 8192, LP((wo) + wrd[(i) & 3]) + ((i) >> 2) * 8192 + 2048)
#define XRD(s, xo) ldsrd16(LP((xo) + xa[s]))
#define QG(i, wi, wo, X_) do { acc[(i) & 3] = MFMA32(wf[i], xf[2 * (wi) + ((i) >> 2)], acc[(i) & 3]); PIN(acc[(i) & 3]); SBAR(); if ((i) == 0) BAR(); WRD(i, wo); X_; SBAR(); } while (0)
            unsigned sb = lds0;
            asm volatile("s_waitcnt lgkmcnt(0)\n\ts_barrier" ::: "memory");
#pragma unroll
            for (int s = 0; s < 4; ++s) xf[s] = XRD(s, sb);
#pragma unroll
            for (int i = 0; i < 8; ++i) WRD(i, sb + USLOT);
            LGKM(0);
#define QODDPH() do { const unsigned wo = sb + 2 * USLOT; SBAR(); \
                QG(0, 0, wo, (void)0); QG(1, 0, wo, (void)0); QG(2, 0, wo, (void)0); QG(3, 0, wo, (void)0); QG(4, 0, wo, (void)0); QG(5, 0, wo, (void)0); QG(6, 0, wo, (void)0); QG(7, 0, wo, (void)0); } while (0)
#pragma nounroll
            for (int gi = 0; gi < 8; ++gi) {
                QODDPH();
                sb = sb == lds0 + 6 * USLOT ? lds0 : sb + 3 * USLOT;
                SBAR();
                QG(0, 1, sb + USLOT, xn[0] = XRD(0, sb)); QG(1, 1, sb + USLOT, xn[1] = XRD(1, sb)); QG(2, 1, sb + USLOT, (void)0); QG(3, 1, sb + USLOT, (void)0);
                QG(4, 1, sb + USLOT, xn[2] = XRD(2, sb)); QG(5, 1, sb + USLOT, (void)0); QG(6, 1, sb + USLOT, (void)0); QG(7, 1, sb + USLOT, xn[3] = XRD(3, sb));
                xf[0] = xn[0]; xf[1] = xn[1]; xf[2] = xn[2]; xf[3] = xn[3];
            }
            QODDPH();
#pragma unroll
            for (int dt = 0; dt < 4; ++dt) { acc[dt] = MFMA32(wf[dt], xf[2], acc[dt]); acc[dt] = MFMA32(wf[4 + dt], xf[3], acc[dt]); }
            tag = next_tag(rfl64(epoch));
#pragma unroll
            for (int f = 0; f < 8; ++f) { const f32x16& a = acc[f >> 1]; const int r = 8 * (f & 1);
                const u32x4 w = {pk2(a[r] * QSCALE, a[r + 1] * QSCALE), pk2(a[r + 2] * QSCALE, a[r + 3] * QSCALE), pk2(a[r + 4] * QSCALE, a[r + 5] * QSCALE), pk2(a[r + 6] * QSCALE, a[r + 7] * QSCALE)};
                qf[f] = __builtin_bit_cast(bf16x8, w);
                __builtin_amdgcn_raw_buffer_store_b128(w, wsr, (unsigned)WS_QX + fx + f * 1024, 0, AUX_SC1); }
            opengate(1);
            signoff(0, ctl + 256); opengate(1);
            KFIRST();
        } else {
            tag = next_tag(rfl64(epoch));
            KFIRST();
            poll_flags(ctl + 256, tag, budget);
#pragma unroll
            for (int s = 0; s < 8; ++s) qf[s] = __builtin_bit_cast(bf16x8, __builtin_amdgcn_raw_buffer_load_b128(wsr, (unsigned)WS_QX + fx + s * 1024, 0, AUX_SC1));
            opengate(2);
        }
        f32x16 O0 = {}, O1 = {}, O2 = {}, O3 = {}, negm, SA, SB; float lsum = 0.f, mrow, ls0, ls1, mx0, mx1; u32x4 pw0, pw1, pk_; unsigned va[4]; s16x4 vh[16];
#define R_O0 "{v[192:207]}"
#define R_O1 "{v[208:223]}"
#define R_O2 "{v[224:239]}"
#define R_O3 "{v[240:255]}"
#define R_SA "{v[176:191]}"
#define R_SB "{v[160:175]}"
#define N_SA "176"
#define N_SB "160"
#define R_NM "{v[144:159]}"
#define PINR(x) asm volatile("" : "+" R_##x(x))
#define EXP2(x) __builtin_amdgcn_exp2f(x)
#define MX3(a, b, c) __builtin_fmaxf(__builtin_fmaxf((a), (b)), (c))
#define VRDH(g, t, off) vh[2 * (g) + (t)] = trrd(LP(va[(g) & 3]) + (off) + ((g) >> 2) * 4096 + (t) * 2048)
#define V2(g, vo) do { VRDH(g, 0, vo); VRDH(g, 1, vo); } while (0)
#define VF(g) __builtin_shufflevector(vh[2 * (g)], vh[2 * (g) + 1], 0, 1, 2, 3, 4, 5, 6, 7)
#define KSTEP(s, d) do { ka[s] += (d); PIN(ka[s]); } while (0)
#define VSTEP(c, d) do { va[c] += (d); PIN(va[c]); } while (0)
#define RESCALE(S, mx, mxb) do { float t0_; asm volatile( \
            "v_max_f32 %8, %8, %11\n\tv_cmp_lt_f32 vcc, %10, %8\n\ts_cbranch_vccnz .Lrs%=\n" \
            ".Lrb%=:\n\t.subsection 1\n" \
            ".Lrs%=:\n\ts_nop 15\n\tv_mov_b32 %9, %8\n\ts_nop 1\n\tv_permlane32_swap_b32 %8, %9\n\ts_nop 1\n\tv_max3_f32 %8, %8, %9, 0\n\t" \
            "v_add_f32 %6, %6, %8\n\tv_exp_f32 %9, -%8\n\t" \
            ".set r_, 0\n\t.rept 16\n\tv_sub_f32 v[" N_##S "+r_], v[" N_##S "+r_], %8\n\tv_sub_f32 v[144+r_], 0, %6\n\t.set r_, r_+1\n\t.endr\n\t" \
            "v_mul_f32 %7, %7, %9\n\t" \
            ".set r_, 192\n\t.rept 64\n\tv_mul_f32 v[r_], v[r_], %9\n\t.set r_, r_+1\n\t.endr\n\ts_nop 1\n\ts_branch .Lrb%=\n\t.subsection 0" \
            : "+" R_O0(O0), "+" R_O1(O1), "+" R_O2(O2), "+" R_O3(O3), "+" R_##S(S), "+" R_NM(negm), "+v"(mrow), "+v"(lsum), "+v"(mx), "=&v"(t0_) : "s"(THR), "v"(mxb) : "vcc"); } while (0)
#define G1(SN, SO, s, CIN, PK, KL) do { SN = MFMA32(kf[s], qf[s], CIN); PINR(SN); SBAR(); PINR(SO); KL; SO[2 * (s)] = EXP2(SO[2 * (s)]); SO[2 * (s) + 1] = EXP2(SO[2 * (s) + 1]); PK; PINR(SO); SBAR(); } while (0)
#define PKW(SO, W, i, r) do { W[i] = pk2(SO[r], SO[(r) + 1]); PIN(W); } while (0)
#define PH1(SN, SO, KL) do { SBAR(); \
        G1(SN, SO, 0, negm, (void)0, KL(0));          G1(SN, SO, 1, SN, PKW(SO, pw0, 0, 0), KL(1)); G1(SN, SO, 2, SN, PKW(SO, pw0, 1, 2), KL(2));  G1(SN, SO, 3, SN, PKW(SO, pw0, 2, 4), KL(3)); \
        G1(SN, SO, 4, SN, PKW(SO, pw0, 3, 6), KL(4)); G1(SN, SO, 5, SN, PKW(SO, pw1, 0, 8), KL(5)); G1(SN, SO, 6, SN, PKW(SO, pw1, 1, 10), KL(6)); G1(SN, SO, 7, SN, PKW(SO, pw1, 2, 12), KL(7)); } while (0)
#define G2(g, OC, W, X_) do { OC = MFMA32(VF(g), __builtin_bit_cast(bf16x8, W), OC); PINR(OC); SBAR(); X_; SBAR(); } while (0)
#define ADD4(SO, r) do { ls0 += SO[r]; ls1 += SO[(r) + 1]; ls0 += SO[(r) + 2]; ls1 += SO[(r) + 3]; PIN(ls0); PIN(ls1); } while (0)
#define PH2(SN, SO, vo, A0, A1, A2, A3) do { SBAR(); \
        G2(0, O0, pw0, { V2(0, vo); PKW(SO, pw1, 3, 14); ls0 = lsum + SO[0]; ls1 = SO[1] + SO[3]; ls0 += SO[2]; PIN(ls0); PIN(ls1); }); G2(1, O1, pw0, { V2(1, vo); ADD4(SO, 4); }); \
        G2(2, O2, pw0, { V2(2, vo); ADD4(SO, 8); }); G2(3, O3, pw0, { V2(3, vo); ADD4(SO, 12); }); \
        G2(4, O0, pw1, { V2(4, vo); mx0 = MX3(SN[0], SN[1], SN[2]); mx1 = MX3(SN[3], SN[4], SN[5]); lsum = ls0 + ls1; PIN(mx0); PIN(mx1); PIN(lsum); A0; }); \
        G2(5, O1, pw1, { V2(5, vo); mx0 = MX3(mx0, SN[6], SN[7]); mx1 = MX3(mx1, SN[8], SN[9]); PIN(mx0); PIN(mx1); A1; }); \
        G2(6, O2, pw1, { V2(6, vo); mx0 = MX3(mx0, SN[10], SN[11]); mx1 = MX3(mx1, SN[12], SN[13]); PIN(mx0); PIN(mx1); A2; }); \
        G2(7, O3, pw1, { V2(7, vo); mx0 = MX3(mx0, SN[14], SN[15]); A3; }); \
        RESCALE(SN, mx0, mx1); } while (0)
#define KL0(s) KRD(s, 0)
#define KL1(s) KRD(s, 8192)
#define KLN(s) (void)0
#define NOP_ (void)0
#define PEEK() do { pk_ = *(volatile LAS const u32x4*)LP(a_landed); } while (0)
#define SEEN() do { unsigned m_ = pk_[0] < pk_[1] ? pk_[0] : pk_[1], n_ = pk_[2] < pk_[3] ? pk_[2] : pk_[3]; const int a_ = __builtin_amdgcn_readfirstlane(m_ < n_ ? m_ : n_); avail = a_ > avail ? a_ : avail; } while (0)
        {
            SA = MFMA32(kf[0], qf[0], (f32x16){});
#pragma unroll
            for (int s = 1; s < 8; ++s) SA = MFMA32(kf[s], qf[s], SA);
#pragma unroll
            for (int s = 0; s < 8; ++s) KRD(s, 8192);
            float mx = MX3(SA[0], SA[1], SA[2]);
#pragma unroll
            for (int r = 3; r < 15; r += 2) mx = MX3(mx, SA[r], SA[r + 1]);
            mx = swap_max(__builtin_fmaxf(mx, SA[15])); mrow = mx;
#pragma unroll
            for (int r = 0; r < 16; ++r) { SA[r] -= mx; negm[r] = -mx; }
            asm volatile("" : "+" R_NM(negm));
        }
        unsigned vcur = acquire(), kcur = so;
#pragma unroll
        for (int c = 0; c < 4; ++c) { va[c] = (unsigned)(256 * (4 * hi + q4) + 16 * ((4 * c + 2 * blk + (p4 >> 1)) ^ (q4 << 2)) + 8 * (p4 & 1)) + vcur; PIN(va[c]); }
#pragma unroll
        for (int g = 0; g < 8; ++g) V2(g, 0);
#pragma unroll
        for (int s = 0; s < 8; ++s) KADDR(s, kcur);
        int tnext = (int)(unsigned)__builtin_amdgcn_s_memrealtime(), per = roleB ? PB0 : PA0;
#pragma nounroll
        for (int t = 0; t < NT - 1; ++t) {
            (void)acquire();
            if (roleB || t <= PEND) {
                int now = (int)(unsigned)__builtin_amdgcn_s_memrealtime();
#pragma nounroll
                while (now - tnext < 0) { __builtin_amdgcn_s_sleep(1); now = (int)(unsigned)__builtin_amdgcn_s_memrealtime(); }
                tnext = now + per; const int d_ = roleB ? PB1 : PA1, m_ = roleB ? PMINB : PMINA; per = per - d_ > m_ ? per - d_ : m_;
            }
            LGKM(15);
            PH1(SB, SA, KL0);
            LGKM(8);
            PH2(SB, SA, 8192, PEEK(), NOP_, NOP_, NOP_);
            LGKM(15);
            PH1(SA, SB, KL1);
            SEEN();
            release(u - 1);
            const unsigned vo = acquire();
            const unsigned dv = vo - vcur, dk = so - kcur; vcur = vo; kcur = so;
            VSTEP(0, dv); VSTEP(1, dv); VSTEP(2, dv); VSTEP(3, dv);
            LGKM(8);
            PH2(SA, SB, 0, { KSTEP(0, dk); KSTEP(1, dk); }, { KSTEP(2, dk); KSTEP(3, dk); }, { KSTEP(4, dk); KSTEP(5, dk); }, { KSTEP(6, dk); KSTEP(7, dk); });
        }
        u64 fpv = 0;
        if (!roleB) fpv = __hip_atomic_load((GAS const u64*)(ctl + 512), RLX_AGENT);
        PH1(SB, SA, KLN);
        release(u - 1);
        u32x4 pf[8]; float plse = 0.f; bool early = false;
#define PLOAD() do { _Pragma("unroll") for (int i = 0; i < 8; ++i) pf[i] = __builtin_amdgcn_raw_buffer_load_b128(wsr, (unsigned)WS_PX + fx + i * 1024, 0, AUX_SC1); \
            plse = __uint_as_float(__builtin_amdgcn_raw_buffer_load_b32(wsr, (unsigned)WS_PL + (pair * 4 + rg) * 256 + lane * 4, 0, AUX_SC1)); } while (0)
        if (!roleB && rfl64(fpv) == tag) { early = true; asm volatile("" ::: "memory"); PLOAD(); }
        PH2(SB, SA, 8192, NOP_, NOP_, NOP_, NOP_);
        {
#pragma unroll
            for (int r = 0; r < 16; ++r) { SB[r] = EXP2(SB[r]); lsum += SB[r]; }
            pw0 = (u32x4){pk2(SB[0], SB[1]), pk2(SB[2], SB[3]), pk2(SB[4], SB[5]), pk2(SB[6], SB[7])};
            pw1 = (u32x4){pk2(SB[8], SB[9]), pk2(SB[10], SB[11]), pk2(SB[12], SB[13]), pk2(SB[14], SB[15])};
            const bf16x8 p0 = __builtin_bit_cast(bf16x8, pw0), p1 = __builtin_bit_cast(bf16x8, pw1);
            O0 = MFMA32(VF(0), p0, O0); O1 = MFMA32(VF(1), p0, O1); O2 = MFMA32(VF(2), p0, O2); O3 = MFMA32(VF(3), p0, O3);
            O0 = MFMA32(VF(4), p1, O0); O1 = MFMA32(VF(5), p1, O1); O2 = MFMA32(VF(6), p1, O2); O3 = MFMA32(VF(7), p1, O3);
        }
        release(u);
        lsum = swap_add(lsum);
        const float lse = mrow + __builtin_amdgcn_logf(lsum);
        if (!roleB) {
            if (!early) { poll_flags(ctl + 512, tag, budget); PLOAD(); }
            const float M = __builtin_fmaxf(lse, plse), eo = EXP2(lse - M), ep = EXP2(plse - M), r = __builtin_amdgcn_rcpf(eo + ep);
            const float fo = eo * r * __builtin_amdgcn_rcpf(lsum), fp = ep * r;
#pragma unroll
            for (int f = 0; f < 8; ++f) { const f32x16& a = f < 2 ? O0 : f < 4 ? O1 : f < 6 ? O2 : O3; const int q = 8 * (f & 1); u32x4 w;
#pragma unroll
                for (int e = 0; e < 4; ++e) w[e] = pk2(__builtin_fmaf(a[q + 2 * e], fo, fp * bflo(pf[f][e])), __builtin_fmaf(a[q + 2 * e + 1], fo, fp * bfhi(pf[f][e])));
                __builtin_amdgcn_raw_buffer_store_b128(w, wsr, (unsigned)WS_OX + fx + f * 1024, 0, AUX_SC1); }
        } else {
            const float inv = __builtin_amdgcn_rcpf(lsum);
#pragma unroll
            for (int f = 0; f < 8; ++f) { const f32x16& a = f < 2 ? O0 : f < 4 ? O1 : f < 6 ? O2 : O3; const int r = 8 * (f & 1);
                const u32x4 w = {pk2(a[r] * inv, a[r + 1] * inv), pk2(a[r + 2] * inv, a[r + 3] * inv), pk2(a[r + 4] * inv, a[r + 5] * inv), pk2(a[r + 6] * inv, a[r + 7] * inv)};
                __builtin_amdgcn_raw_buffer_store_b128(w, wsr, (unsigned)WS_PX + fx + f * 1024, 0, AUX_SC1); }
            __builtin_amdgcn_raw_buffer_store_b32(__float_as_uint(lse), wsr, (unsigned)WS_PL + (pair * 4 + rg) * 256 + lane * 4, 0, AUX_SC1);
        }
        signoff(1, roleB ? ctl + 512 : ctl + 768);
        if (roleB) opengate(1);
        const int qh = rg & 1, nh = rg >> 1;
        *(volatile LAS u64*)(lds + L_TAG) = tag;
        u += NSL - 1; (void)acquire();
        unsigned wa[4], wh = lds0 + 131072 + (nh * 64 + hi) * 128 + 32 * q4 + 128 * blk + 8 * p4; PIN(wh);
#pragma unroll
        for (int c = 0; c < 4; ++c) { wa[c] = lds0 + ((((nh ^ (c >> 1)) * 64 + hi) * 4 + q4) * 256 + 64 * ((2 * nh + (c & 1)) ^ q4) + 32 * blk + 8 * p4); PIN(wa[c]); }
#define AKS(i) ((i) < 48 ? (i) / 3 : 16 + ((i) - 48) / 2)
#define ATL(i) ((i) < 48 ? (i) % 3 : ((i) - 48) % 2)
#define AWA(i) wa[((i) < 48 ? 0 : 2) + (ATL(i) & 1)]
#define ARD(i) (ATL(i) == 2 ? trrd2(LP(wh) + AKS(i) * 512, LP(wh) + AKS(i) * 512 + 256) : trrd2(LP(AWA(i)) + (AKS(i) & 15) * 4096, LP(AWA(i)) + (AKS(i) & 15) * 4096 + 2048))
        constexpr int AD = 7, OPRE = 10;
        bf16x8 af[AD], of[32];
#pragma unroll
        for (int i = 0; i < AD; ++i) af[i] = ARD(i);
        const int pr0 = b * HKV + ks * 2 + nh;
        poll_flags(ws + WS_CT + (size_t)(pr0 ^ (lane & 1)) * 1024 + 768, tag, budget);
        const unsigned ob0 = (unsigned)WS_OX + (pr0 * 4 + qh) * 8192 + lane * 16, ob1 = (unsigned)WS_OX + ((pr0 ^ 1) * 4 + qh) * 8192 + lane * 16;
#define OLD(f) of[f] = __builtin_bit_cast(bf16x8, __builtin_amdgcn_raw_buffer_load_b128(wsr, ((f) < 16 ? ob0 : ob1) + ((f) & 3) * 1024, (((f) >> 3) & 1) * 16384 + (((f) >> 2) & 1) * 4096, AUX_SC1))
#pragma unroll
        for (int f = 0; f < OPRE; ++f) OLD(f);
        f32x16 acc[3] = {};
        SBAR();
#pragma unroll
        for (int i = 0; i < 80; ++i) { acc[ATL(i)] = MFMA32(af[i % AD], of[AKS(i)], acc[ATL(i)]); PIN(acc[ATL(i)]); SBAR(); if (i + AD < 80) af[i % AD] = ARD(i + AD);
            if ((i & 1) == 0 && OPRE + i / 2 < 32) OLD(OPRE + i / 2); SBAR(); }
        const unsigned sb = (unsigned)WS_SLAB + ((b * 4 + nq) * 8 + (r32 >> 2)) * 20480 + ks * 2560 + ((lane & 3) * 2 + hi) * 16;
#pragma unroll
        for (int k = 0; k < 5; ++k) { const int kk = k == 0 ? 4 : k - 1; const f32x16& a = acc[kk >> 1]; const int r = 8 * (kk & 1);
            const u32x4 v = {pk2(a[r], a[r + 1]), pk2(a[r + 2], a[r + 3]), pk2(a[r + 4], a[r + 5]), pk2(a[r + 6], a[r + 7])};
            __builtin_amdgcn_raw_buffer_store_b128(v, wsr, sb + (kk < 4 ? (qh * 8 + nh * 4 + kk) * 128 : 2048 + (nh * 2 + qh) * 128), 0, AUX_SC1); }
        signoff(2, fsl + ks * 256);
        tagf = tag;
    }
    {
        poll_flags(fsl + (lane & 7) * 256, tagf, budget);
        const int e = wid * 32 + r32;
        const unsigned sb = (unsigned)WS_SLAB + ((b * 4 + nq) * 8 + ks) * 20480 + hi * 10240 + e * 16;
        u32x4 v[4];
#pragma unroll
        for (int k = 0; k < 4; ++k) v[k] = __builtin_amdgcn_raw_buffer_load_b128(wsr, sb + k * 2560, 0, AUX_SC1);
        if (ks == 0 && nq == 0 && tid == 0) __hip_atomic_store((GAS u64*)epo, tagf, RLX_AGENT);
        float s[8], t[4];
#pragma unroll
        for (int c = 0; c < 4; ++c) { s[2 * c] = (bflo(v[0][c]) + bflo(v[1][c])) + (bflo(v[2][c]) + bflo(v[3][c])); s[2 * c + 1] = (bfhi(v[0][c]) + bfhi(v[1][c])) + (bfhi(v[2][c]) + bfhi(v[3][c])); }
#pragma unroll
        for (int c = 0; c < 4; ++c) { auto rr = __builtin_amdgcn_permlane32_swap(__float_as_uint(s[c]), __float_as_uint(s[4 + c]), false, false); t[c] = __uint_as_float(rr[0]) + __uint_as_float(rr[1]); }
        int qh = e >> 6, col = nq * 128 + ((e >> 3) & 7) * 16;
        if (wid == 4) {
#pragma unroll
            for (int c = 0; c < 4; ++c) t[c] += __shfl_xor(t[c], 16);
            qh = (e >> 3) & 1; col = 512 + nq * 16; }
        if (e < 144) *(u32x2*)(out + ((size_t)(b * SQ + qh * 32 + ks * 4 + ((e >> 1) & 3)) * DM + col + hi * 8 + (e & 1) * 4) * 2) = (u32x2){pk2(t[0], t[1]), pk2(t[2], t[3])};
    }
}

extern "C" void kernel_launch(void* const* d_in, const int* in_sizes, int n_in, void* d_out, int out_size, void* d_ws, size_t ws_size, hipStream_t stream) {
    (void)hipFuncSetAttribute((const void*)gqa_block_k, hipFuncAttributeMaxDynamicSharedMemorySize, LDS_BYTES);
    hipLaunchKernelGGL(gqa_block_k, dim3(256), dim3(512), LDS_BYTES, stream, (const char*)d_in[0], (const char*)d_in[1], (const char*)d_in[2], (const char*)d_in[3],
                       (const char*)d_in[4], (char*)d_out, (char*)d_ws);
}
```

```cpp
#include <hip/hip_runtime.h>
#include <hip/hip_bf16.h>
#include <stdint.h>

typedef short bf16x8 __attribute__((ext_vector_type(8)));
typedef short s16x4 __attribute__((ext_vector_type(4)));
typedef float f32x16 __attribute__((ext_vector_type(16)));
typedef float f32x2 __attribute__((ext_vector_type(2)));
typedef unsigned u32x4 __attribute__((ext_vector_type(4)));
typedef unsigned u32x2 __attribute__((ext_vector_type(2)));
typedef __bf16 bf2_t __attribute__((ext_vector_type(2)));
typedef unsigned long long u64;
#define LAS __attribute__((address_space(3)))
#define GAS __attribute__((address_space(1)))
typedef LAS const char* lcptr;
typedef LAS char* lptr;
#define DI __device__ __forceinline__
#define AINL __attribute__((always_inline))
#define MFMA32(a, b, c) __builtin_amdgcn_mfma_f32_32x32x16_bf16((a), (b), (c), 0, 0, 0)
#define WAITV(N) asm volatile("s_waitcnt vmcnt(" #N ")" ::: "memory")
#define BAR() asm volatile("s_barrier" ::: "memory")
#define PIN(x) asm volatile("" : "+v"(x))
#define SBAR() __builtin_amdgcn_sched_barrier(0)
#define LGKM(n) do { __builtin_amdgcn_s_waitcnt(0xC07F | ((n) << 8)); SBAR(); } while (0)
#define LP(a) ((lcptr)(uintptr_t)(a))
#define STR2(x) #x
#define STR(x) STR2(x)
#define CLOB_V38_255 "v38", "v39", "v40", "v41", "v42", "v43", "v44", "v45", "v46", "v47", "v48", "v49", "v50", "v51", "v52", "v53", "v54", "v55", "v56", "v57", "v58", "v59", "v60", "v61", "v62", "v63", "v64", "v65", "v66", "v67", "v68", "v69", "v70", "v71", "v72", "v73", "v74", "v75", "v76", "v77", "v78", "v79", "v80", "v81", "v82", "v83", "v84", "v85", "v86", "v87", "v88", "v89", "v90", "v91", "v92", "v93", "v94", "v95", "v96", "v97", "v98", "v99", "v100", "v101", "v102", "v103", "v104", "v105", "v106", "v107", "v108", "v109", "v110", "v111", "v112", "v113", "v114", "v115", "v116", "v117", "v118", "v119", "v120", "v121", "v122", "v123", "v124", "v125", "v126", "v127", "v128", "v129", "v130", "v131", "v132", "v133", "v134", "v135", "v136", "v137", "v138", "v139", "v140", "v141", "v142", "v143", "v144", "v145", "v146", "v147", "v148", "v149", "v150", "v151", "v152", "v153", "v154", "v155", "v156", "v157", "v158", "v159", "v160", "v161", "v162", "v163", "v164", "v165", "v166", "v167", "v168", "v169", "v170", "v171", "v172", "v173", "v174", "v175", "v176", "v177", "v178", "v179", "v180", "v181", "v182", "v183", "v184", "v185", "v186", "v187", "v188", "v189", "v190", "v191", "v192", "v193", "v194", "v195", "v196", "v197", "v198", "v199", "v200", "v201", "v202", "v203", "v204", "v205", "v206", "v207", "v208", "v209", "v210", "v211", "v212", "v213", "v214", "v215", "v216", "v217", "v218", "v219", "v220", "v221", "v222", "v223", "v224", "v225", "v226", "v227", "v228", "v229", "v230", "v231", "v232", "v233", "v234", "v235", "v236", "v237", "v238", "v239", "v240", "v241", "v242", "v243", "v244", "v245", "v246", "v247", "v248", "v249", "v250", "v251", "v252", "v253", "v254", "v255"
#define RLX_AGENT __ATOMIC_RELAXED, __HIP_MEMORY_SCOPE_AGENT
#define RLX_WG __ATOMIC_RELAXED, __HIP_MEMORY_SCOPE_WORKGROUP
#define AUX_SC1 16

#define PSL 2
#define ONEPOLL 1
#define NTA 18
#define GATEA 10
#define HPRE 2
#define PA0 110
#define PA1 2
#define PMINA 103
#define PB0 150
#define PB1 9
#define PMINB 103
#define PEND 12

constexpr int SQ = 64, DM = 576, HKV = 16, SK = 2048;
constexpr float QSCALE = 0.08838834764831845f * 1.4426950408889634f;
constexpr float THR = 8.f;
constexpr int NT_A = NTA, NT_B = 32 - NTA;
constexpr int USLOT = 16384, NSL = 9, MISC = NSL * USLOT, LDS_BYTES = 163840;
constexpr int L_LANDED = MISC, L_DONE = MISC + 16, L_CNT = MISC + 32, L_GATE = MISC + 44, L_SEEN = MISC + 48, L_TAG = MISC + 64;
constexpr int SPIN_CAP = 1 << 20;
constexpr size_t WS_CT = 0;
constexpr size_t WS_EP = 128 * 1024, WS_FS = WS_EP + 8 * 256;
constexpr size_t WS_QX = 1 << 20;
constexpr size_t WS_PX = WS_QX + ((size_t)4 << 20);
constexpr size_t WS_PL = WS_PX + ((size_t)4 << 20);
constexpr size_t WS_OX = WS_PL + ((size_t)1 << 20);
constexpr size_t WS_SLAB = WS_OX + ((size_t)4 << 20);
constexpr int WS_USED = 32 << 20;

DI unsigned pk2(float a, float b) { bf2_t r = __builtin_convertvector((f32x2){a, b}, bf2_t); return __builtin_bit_cast(unsigned, r); }
DI float bflo(unsigned v) { return __uint_as_float(v << 16); }
DI float bfhi(unsigned v) { return __uint_as_float(v & 0xffff0000u); }
DI s16x4 trrd(lcptr p) { typedef short v4i16_t __attribute__((ext_vector_type(4))); return __builtin_bit_cast(s16x4, __builtin_amdgcn_ds_read_tr16_b64_v4i16((LAS v4i16_t*)p)); }
DI bf16x8 trrd2(lcptr p0, lcptr p1) { s16x4 a = trrd(p0), b = trrd(p1); return __builtin_shufflevector(a, b, 0, 1, 2, 3, 4, 5, 6, 7); }
DI bf16x8 ldsrd16(lcptr p) { return *(LAS const bf16x8*)p; }
DI float swap_max(float m) { auto rr = __builtin_amdgcn_permlane32_swap(__float_as_uint(m), __float_as_uint(m), false, false); return __builtin_fmaxf(__uint_as_float(rr[0]), __uint_as_float(rr[1])); }
DI float swap_add(float m) { auto rr = __builtin_amdgcn_permlane32_swap(__float_as_uint(m), __float_as_uint(m), false, false); return __uint_as_float(rr[0]) + __uint_as_float(rr[1]); }
DI u64 next_tag(u64 e) { return e * 6364136223846793005ull + 1442695040888963407ull; }
DI u64 rfl64(u64 v) { return ((u64)(unsigned)__builtin_amdgcn_readfirstlane((unsigned)(v >> 32)) << 32) | (unsigned)__builtin_amdgcn_readfirstlane((unsigned)v); }
DI void lds_put(unsigned a, unsigned val) { asm volatile("ds_write_b32 %0, %1" :: "v"(a), "v"(val) : "memory"); }
DI void poll_flags(const char* p, u64 tag, int& budget) {
#pragma nounroll
    while (!__all(__hip_atomic_load((GAS const u64*)p, RLX_AGENT) == tag) && --budget > 0) { if (PSL) __builtin_amdgcn_s_sleep(PSL); }
    asm volatile("" ::: "memory"); }
DI void wait_lds_fast(lcptr p, unsigned need, int& budget) {
#pragma nounroll
    while ((unsigned)__builtin_amdgcn_readfirstlane(*(volatile LAS const unsigned*)p) < need && --budget > 0) {}
    asm volatile("" ::: "memory"); }
#define POLL1(ptr, tg, k) do { if (ONEPOLL) { if (wid == 0) { poll_flags(ptr, tg, budget); *(volatile LAS unsigned*)(lds + L_SEEN) = (k); } else wait_lds_fast(lds + L_SEEN, (k), budget); } else poll_flags(ptr, tg, budget); } while (0)
DI void wait_lds(lcptr p, unsigned need, int& budget) {
#pragma nounroll
    while ((unsigned)__builtin_amdgcn_readfirstlane(*(volatile LAS const unsigned*)p) < need && --budget > 0) __builtin_amdgcn_s_sleep(1); }

#define DMA4(POL, sbase, v0, v1, v2, v3, ldst) do { unsigned sv_; asm volatile("s_mov_b32 %0, m0\n\ts_mov_b32 m0, %6\n\ts_nop 0\n\t" \
    "global_load_lds_dwordx4 %1, %5" POL "\n\tglobal_load_lds_dwordx4 %2, %5 offset:1024" POL "\n\tglobal_load_lds_dwordx4 %3, %5 offset:2048" POL "\n\tglobal_load_lds_dwordx4 %4, %5 offset:3072" POL "\n\t" \
    "s_mov_b32 m0, %0" : "=&s"(sv_) : "v"(v0), "v"(v1), "v"(v2), "v"(v3), "s"(sbase), "s"(__builtin_amdgcn_readfirstlane(ldst)) : "memory"); } while (0)

__global__ __launch_bounds__(512, 2) void gqa_block_k(const char* __restrict__ X, const char* __restrict__ Kc, const char* __restrict__ Vc, const char* __restrict__ Wq,
                                                      const char* __restrict__ Wo, char* __restrict__ out, char* __restrict__ ws) {
    extern __shared__ __attribute__((aligned(16))) char lds_[];
    const int tid = threadIdx.x, lane = tid & 63, r32 = lane & 31, hi = lane >> 5;
    const int wid = __builtin_amdgcn_readfirstlane(tid >> 6);
    const int bid = blockIdx.x, ks = bid & 7, idx = bid >> 3;
    const int kvh = ks * 2 + (idx & 1), b = (idx >> 1) & 7;
    const bool roleB = idx < 16;
    const int pair = b * HKV + kvh;
    const int nq = (idx & 1) + (roleB ? 0 : 2);
    const lptr lds = (lptr)lds_;
    const unsigned lds0 = (unsigned)(uintptr_t)lds;
    const int NT = roleB ? NT_B : NT_A;
    char* const ctl = ws + WS_CT + (size_t)pair * 1024;
    char* const epo = ws + WS_EP + b * 256;
    char* const fsl = ws + WS_FS + (size_t)(b * 4 + nq) * 8 * 256;
    const __amdgpu_buffer_rsrc_t wsr = __builtin_amdgcn_make_buffer_rsrc(ws, 0, WS_USED, 0x00020000);
    u64 pc0; unsigned code_len;
    asm volatile("s_getpc_b64 %0\n.Lcs%=:\n\ts_mov_b32 %1, .Lce%=-.Lcs%=\n\t.subsection 2\n.Lce%=:\n\t.fill 128, 4, 0xBF800000\n\t.subsection 0" : "=s"(pc0), "=s"(code_len));
    if (tid < 16) *(LAS unsigned*)(lds + MISC + tid * 4) = 0u;
    int budget = SPIN_CAP;
    u64 tagf;

    if (wid >= 4) {
        const int j = wid - 4, lr = lane >> 4, lc = lane & 15;
        const size_t koff = ((size_t)pair * SK + (roleB ? NT_A * 64 : 0)) * 256;
        const char* kp = Kc + koff; const char* vp = Vc + koff;
        unsigned vK[4];
#pragma unroll
        for (int i = 0; i < 4; ++i) vK[i] = (unsigned)((16 * j + lr) * 256 + 16 * (lc ^ (4 * i + lr)));
        const unsigned vV = (unsigned)((16 * j + lr) * 256 + 16 * (lc ^ (lr << 2)));
        unsigned a_landed = lds0 + L_LANDED + 4 * j, a_done = lds0 + L_DONE; PIN(a_landed); PIN(a_done);
        if (roleB) {
            unsigned vX[2], vW[4];
#pragma unroll
            for (int i = 0; i < 2; ++i) { const int row = 16 * j + 8 * i + (lane >> 3); vX[i] = (unsigned)(row * 1152 + 16 * ((lane & 7) ^ ((row >> 1) & 7)) - 1024 * i); }
#pragma unroll
            for (int i = 0; i < 4; ++i) vW[i] = (unsigned)((8 * j + 2 * i + hi) * 8192 + 16 * (r32 ^ (((2 * i + hi) & 3) << 2)) - 1024 * i);
            const char* xb = X + (size_t)b * SQ * DM * 2; const char* wb = Wq + kvh * 512;
            int xo = b * 128, sb = 0, cnt = 7;
#define QADV "s_add_i32 %[xo], %[xo], 128\n\ts_cmp_eq_u32 %[xo], 1152\n\ts_cselect_b32 %[xo], 0, %[xo]\n\ts_add_i32 %[sb], %[sb], 0xc000\n\ts_cmp_eq_u32 %[sb], 0x24000\n\ts_cselect_b32 %[sb], 0, %[sb]\n\t"
#define QW4 "global_load_lds_dwordx4 %[w0], s[60:61]\n\tglobal_load_lds_dwordx4 %[w1], s[60:61] offset:1024\n\tglobal_load_lds_dwordx4 %[w2], s[60:61] offset:2048\n\tglobal_load_lds_dwordx4 %[w3], s[60:61] offset:3072\n\t"
#define QEVEN0 "s_add_u32 s62, %[xl], %[xo]\n\ts_addc_u32 s63, %[xh], 0\n\ts_lshl_b32 s66, %[xo], 12\n\ts_add_u32 s60, %[wl], s66\n\ts_addc_u32 s61, %[wh], 0\n\ts_add_i32 m0, %[sb], %[dx]\n\ts_add_i32 s66, %[sb], %[dw]\n\t"
#define QEVEN1 "global_load_lds_dwordx4 %[x0], s[62:63]\n\tglobal_load_lds_dwordx4 %[x1], s[62:63] offset:1024\n\ts_mov_b32 m0, s66\n\ts_nop 0\n\t" QW4
#define QODD0 "s_add_u32 s60, s60, 0x40000\n\ts_addc_u32 s61, s61, 0\n\ts_add_i32 m0, m0, 0x4000\n\t"
            asm volatile(
                "s_mov_b32 s70, m0\n\t"
                QEVEN0 QEVEN1 QODD0 "s_nop 0\n\t" QW4 QADV QEVEN0 QEVEN1 QODD0
                "s_waitcnt vmcnt(10)\n"
                ".Lqr%=:\n\t"
                "s_barrier\n\t" QW4 QADV QEVEN0 "s_waitcnt vmcnt(10)\n\t"
                "s_barrier\n\t" QEVEN1 QODD0 "s_sub_i32 %[cnt], %[cnt], 1\n\ts_cmp_gt_i32 %[cnt], 0\n\ts_waitcnt vmcnt(10)\n\ts_cbranch_scc1 .Lqr%=\n\t"
                "s_barrier\n\t" QW4 "s_waitcnt vmcnt(10)\n\t"
                "s_barrier\n\ts_waitcnt vmcnt(4)\n\t"
                "s_barrier\n\ts_waitcnt vmcnt(0)\n\t"
                "s_barrier\n\ts_mov_b32 m0, s70"
                : [xo] "+&s"(xo), [sb] "+&s"(sb), [cnt] "+&s"(cnt)
                : [xl] "s"((unsigned)(uintptr_t)xb), [xh] "s"((unsigned)((uintptr_t)xb >> 32)), [wl] "s"((unsigned)(uintptr_t)wb), [wh] "s"((unsigned)((uintptr_t)wb >> 32)),
                  [dx] "s"(lds0 + j * 2048), [dw] "s"(lds0 + USLOT + j * 4096),
                  [x0] "v"(vX[0]), [x1] "v"(vX[1]), [w0] "v"(vW[0]), [w1] "v"(vW[1]), [w2] "v"(vW[2]), [w3] "v"(vW[3])
                : "memory", "scc", "s60", "s61", "s62", "s63", "s66", "s70");
        } else {
            DMA4(" nt", kp, vK[0], vK[1], vK[2], vK[3], lds0 + j * 4096); DMA4(" nt", vp, vV, vV, vV, vV, lds0 + USLOT + j * 4096);
            BAR();
        }
        const char* wb = Wo + (size_t)ks * 512 * 1152;
        unsigned oF[4], oH[4];
#pragma unroll
        for (int i = 0; i < 4; ++i) { oF[i] = (unsigned)(((4 * j + i) * 4 + lr) * 1152 + nq * 256 + 16 * (lc ^ (lr << 2)) - 1024 * i);
            oH[i] = (unsigned)((j * 128 + i * 32 + (lane >> 1)) * 1152 + 1024 + nq * 32 + (lane & 1) * 16 - 1024 * i); }
        const unsigned wl = (unsigned)(lds0 + j * 4096 + lane * 16), a_gate = lds0 + L_GATE;
        const int nkv = 2 * NT, ntot = roleB ? nkv : nkv + NSL, n0 = roleB ? 0 : 2;
        int ni = n0, nl = 0, nw = 0, bi = 0, bw = 0, gate = roleB ? 0 : GATEA, si = n0 * USLOT, sw = 0, lim = NSL, wc = roleB ? 9 : 0;
        static_assert(GATEA >= NSL && GATEA <= NSL + 4, "four register buffers");
#define WQUOTA(L) "s_sub_i32 s67, %[ni], " STR(GATEA) "\n\ts_lshr_b32 s67, s67, 1\n\ts_add_i32 s67, s67, 3\n\ts_cmp_ge_i32 %[wc], s67\n\ts_cbranch_scc1 " L "\n\t"
        asm volatile(
            "s_mov_b32 s70, m0\n\ts_mov_b64 s[72:73], 0\n\ts_mov_b32 s75, 0\n\ts_getpc_b64 s[64:65]\n"
            ".Lp%=:\n\t"
            "s_branch .Lprep%=\n"
            ".Ltop%=:\n\t"
            "s_sub_i32 s66, %[ni], %[nl]\n\ts_cmp_lt_i32 s66, 3\n\ts_cbranch_scc1 .Lc1%=\n\t"
            "s_waitcnt vmcnt(8)\n\ts_add_i32 %[nl], %[nl], 1\n\t"
            ".Lc1%=:\n\t"
            "s_waitcnt lgkmcnt(0)\n\tv_min_u32 v42, v42, v43\n\tv_min3_u32 v40, v40, v41, v42\n\ts_mov_b32 s71, 0\n\ts_cmp_lt_i32 %[nw], %[nl]\n\tv_readfirstlane_b32 s67, v40\n\t"
            "s_cbranch_scc0 .Lc2%=\n\t"
            "s_bitcmp1_b64 s[72:73], %[nw]\n\ts_cbranch_scc1 .Lwr%=\n"
            ".Lpb%=:\n\t"
            "s_add_i32 %[nw], %[nw], 1\n\tv_mov_b32 v38, %[nw]\n\tds_write_b32 %[aland], v38\n\t"
            "s_add_i32 %[sw], %[sw], 0x4000\n\ts_cmp_eq_u32 %[sw], 0x24000\n\ts_cselect_b32 %[sw], 0, %[sw]\n\ts_mov_b32 s71, 1\n\t"
            "s_cmp_lt_i32 %[nw], %[nl]\n\ts_cbranch_scc1 .Lmore%=\n"
            ".Lc2%=:\n\t"
            "s_add_i32 s67, s67, 9\n\ts_max_i32 %[lim], %[lim], s67\n\t"
            "s_cmp_ge_i32 %[ni], %[gate]\n\ts_cbranch_scc1 .Lno%=\n\t"
            "s_cmp_lt_i32 %[ni], %[lim]\n\ts_cbranch_scc0 .Lis%=\n\t"
            "global_load_lds_dwordx4 v44, s[60:61] nt\n\tglobal_load_lds_dwordx4 v45, s[60:61] offset:1024 nt\n\t"
            "global_load_lds_dwordx4 v46, s[60:61] offset:2048 nt\n\tglobal_load_lds_dwordx4 v47, s[60:61] offset:3072 nt\n"
            ".Lbk%=:\n\t"
            "s_add_i32 %[ni], %[ni], 1\n\ts_mov_b32 s75, 1\n\t"
            "s_add_i32 %[si], %[si], 0x4000\n\ts_cmp_eq_u32 %[si], 0x24000\n\ts_cselect_b32 %[si], 0, %[si]\n"
            ".Lprep%=:\n\t"
            "ds_read_b128 v[40:43], %[adone]\n\t"
            "s_add_i32 m0, %[si], %[dbase]\n\t"
            "s_lshr_b32 s66, %[ni], 1\n\ts_lshl_b32 s66, s66, 14\n\t"
            "s_bitcmp1_b32 %[ni], 0\n\ts_cselect_b32 s60, %[vpl], %[kpl]\n\ts_cselect_b32 s61, %[vph], %[kph]\n\ts_cselect_b64 vcc, -1, 0\n\t"
            "s_add_u32 s60, s60, s66\n\ts_addc_u32 s61, s61, 0\n\t"
            "v_cndmask_b32 v44, %[k0], %[vv], vcc\n\tv_cndmask_b32 v45, %[k1], %[vv], vcc\n\tv_cndmask_b32 v46, %[k2], %[vv], vcc\n\tv_cndmask_b32 v47, %[k3], %[vv], vcc\n\t"
            "s_branch .Ltop%=\n"
            ".Lmore%=:\n\ts_bitcmp1_b64 s[72:73], %[nw]\n\ts_cbranch_scc1 .Lwr%=\n\ts_branch .Lpb%=\n"
            ".Lno%=:\n\t"
            "s_cmp_ge_i32 %[gate], %[nkv]\n\ts_cbranch_scc1 .Lno2%=\n\t"
            "ds_read_b32 v40, %[agate]\n\ts_waitcnt lgkmcnt(0)\n\tv_readfirstlane_b32 s67, v40\n\ts_nop 3\n\t"
            "s_cmp_ge_u32 s67, 4\n\ts_cselect_b32 %[gate], %[g1], %[gate]\n\ts_cmp_ge_u32 s67, 8\n\ts_cselect_b32 %[gate], %[nkv], %[gate]\n\t"
            "s_cmp_lt_i32 %[ni], %[gate]\n\ts_cselect_b32 s71, 1, s71\n"
            ".Lno2%=:\n\t"
            "ds_read_b128 v[40:43], %[adone]\n\t"
            "s_cmp_lg_u32 s71, 0\n\ts_cbranch_scc1 .Ltop%=\n\t"
            "s_sub_i32 s66, %[ni], %[nl]\n\ts_cmp_eq_u32 s66, 0\n\ts_cbranch_scc1 .Lno3%=\n\t"
            "s_cmp_eq_u32 s66, 2\n\ts_cbranch_scc1 .Lw4%=\n\ts_waitcnt vmcnt(0)\n"
            ".Lw4%=:\n\ts_waitcnt vmcnt(4)\n\ts_add_i32 %[nl], %[nl], 1\n\ts_branch .Lc1%=\n"
            ".Lno3%=:\n\ts_cmp_ge_i32 %[nw], %[ntot]\n\ts_cbranch_scc1 .Lend%=\n\t"
            "s_cmp_lt_i32 %[gate], %[nkv]\n\ts_cbranch_scc1 .Lidle%=\n\t"
            "s_cmp_lt_i32 %[wc], 9\n\ts_cbranch_scc1 .Lwo0%=\n\t"
            "s_cmp_eq_u32 %[ni], %[nkv]\n\ts_cbranch_scc0 .Lidle%=\n\ts_cmp_eq_u32 %[ni], %[ntot]\n\ts_cbranch_scc1 .Lidle%=\n\t"
            "s_waitcnt vmcnt(0)\n\ts_lshl_b64 s[62:63], 0x1ff, %[nkv]\n\ts_or_b64 s[72:73], s[72:73], s[62:63]\n\ts_mov_b32 %[ni], %[ntot]\n\ts_mov_b32 %[nl], %[ntot]\n\ts_branch .Lc1%=\n"
            ".Lidle%=:\n\t"
            "s_sleep 1\n\ts_sub_i32 %[bud], %[bud], 1\n\ts_cmp_gt_i32 %[bud], 0\n\ts_cbranch_scc1 .Lc1%=\n\t"
            "s_mov_b32 %[lim], 0x3fffffff\n\ts_mov_b32 %[gate], %[nkv]\n\ts_branch .Lc1%=\n"
            ".Lrf%=:\n\t"
            "s_cmp_eq_u32 s75, 0\n\ts_cbranch_scc1 .Lno2%=\n\ts_cmp_ge_i32 %[wc], 9\n\ts_cbranch_scc1 .Lno2%=\n\t"
            WQUOTA(".Lno2%=")
            "s_mov_b32 s75, 0\n\tds_read_b128 v[40:43], %[adone]\n\ts_branch .Lwo%=\n"
            ".Lwo0%=:\n\t"
            WQUOTA(".Lidle%=")
            "s_waitcnt vmcnt(8)\n"
            ".Lwo%=:\n\t"
            "s_and_b32 s66, %[wc], 7\n\ts_mul_i32 s66, s66, 73728\n\ts_add_u32 s76, %[wpl], s66\n\ts_addc_u32 s77, %[wph], 0\n\t"
            "s_mul_i32 s66, %[wc], 36\n\ts_add_u32 s66, s66, .Lit2%=-.Lp%=\n\ts_add_u32 s62, s64, s66\n\ts_addc_u32 s63, s65, 0\n\t"
            "s_add_i32 %[wc], %[wc], 1\n\t"
            "s_setpc_b64 s[62:63]\n"
            ".Lit2%=:\n\t"
            ".set r_, 112\n\t.rept 8\n\t"
            "global_load_dwordx4 v[r_:r_+3], %[f0], s[76:77] nt\n\tglobal_load_dwordx4 v[r_+4:r_+7], %[f1], s[76:77] offset:1024 nt\n\t"
            "global_load_dwordx4 v[r_+8:r_+11], %[f2], s[76:77] offset:2048 nt\n\tglobal_load_dwordx4 v[r_+12:r_+15], %[f3], s[76:77] offset:3072 nt\n\ts_branch .Lc1%=\n\t"
            ".set r_, r_+16\n\t.endr\n\t"
            "global_load_dwordx4 v[240:243], %[h0], s[76:77] nt\n\tglobal_load_dwordx4 v[244:247], %[h1], s[76:77] offset:1024 nt\n\t"
            "global_load_dwordx4 v[248:251], %[h2], s[76:77] offset:2048 nt\n\tglobal_load_dwordx4 v[252:255], %[h3], s[76:77] offset:3072 nt\n\ts_branch .Lc1%=\n"
            ".Lis%=:\n\t"
            "s_cmp_ge_i32 %[ni], " STR(GATEA) "\n\ts_cbranch_scc1 .Lrf%=\n\t"
            "s_add_u32 s67, %[bi], .Lit%=-.Lp%=\n\ts_add_u32 s62, s64, s67\n\ts_addc_u32 s63, s65, 0\n\t"
            "s_bitset1_b64 s[72:73], %[ni]\n\ts_add_i32 %[bi], %[bi], 36\n\t"
            "s_setpc_b64 s[62:63]\n"
            ".Lit%=:\n\t"
            ".set r_, 48\n\t.rept 4\n\t"
            "global_load_dwordx4 v[r_:r_+3], v44, s[60:61] nt\n\tglobal_load_dwordx4 v[r_+4:r_+7], v45, s[60:61] offset:1024 nt\n\t"
            "global_load_dwordx4 v[r_+8:r_+11], v46, s[60:61] offset:2048 nt\n\tglobal_load_dwordx4 v[r_+12:r_+15], v47, s[60:61] offset:3072 nt\n\ts_branch .Lbk%=\n\t"
            ".set r_, r_+16\n\t.endr\n"
            ".Lwr%=:\n\t"
            "s_add_i32 s66, s67, 9\n\ts_max_i32 %[lim], %[lim], s66\n\t"
            "s_cmp_lt_i32 %[nw], %[lim]\n\ts_cbranch_scc0 .Lc2%=\n\t"
            "v_add_u32 v39, %[sw], %[wl]\n\t"
            "s_lshr_b32 s66, %[sw], 14\n\ts_mul_i32 s66, s66, 36\n\ts_add_i32 s66, s66, 144\n\ts_cmp_lt_i32 %[nw], %[nkv]\n\ts_cselect_b32 s66, %[bw], s66\n\t"
            "s_add_u32 s66, s66, .Lwt%=-.Lp%=\n\ts_add_u32 s62, s64, s66\n\ts_addc_u32 s63, s65, 0\n\t"
            "s_add_i32 %[bw], %[bw], 36\n\t"
            "s_setpc_b64 s[62:63]\n"
            ".Lwt%=:\n\t"
            ".set r_, 48\n\t.rept 13\n\t"
            "ds_write_b128 v39, v[r_:r_+3]\n\tds_write_b128 v39, v[r_+4:r_+7] offset:1024\n\tds_write_b128 v39, v[r_+8:r_+11] offset:2048\n\tds_write_b128 v39, v[r_+12:r_+15] offset:3072\n\ts_branch .Lpb%=\n\t"
            ".set r_, r_+16\n\t.endr\n"
            ".Lend%=:\n\ts_mov_b32 m0, s70\n\ts_waitcnt lgkmcnt(0)"
            : [ni] "+&s"(ni), [nl] "+&s"(nl), [nw] "+&s"(nw), [bi] "+&s"(bi), [bw] "+&s"(bw), [gate] "+&s"(gate), [si] "+&s"(si), [sw] "+&s"(sw), [lim] "+&s"(lim),
              [wc] "+&s"(wc), [bud] "+&s"(budget)
            : [nkv] "s"(nkv), [ntot] "s"(ntot), [g1] "s"(roleB ? HPRE : GATEA), [dbase] "s"(lds0 + j * 4096),
              [kpl] "s"((unsigned)(uintptr_t)kp), [kph] "s"((unsigned)((uintptr_t)kp >> 32)), [vpl] "s"((unsigned)(uintptr_t)vp), [vph] "s"((unsigned)((uintptr_t)vp >> 32)),
              [wpl] "s"((unsigned)(uintptr_t)wb), [wph] "s"((unsigned)((uintptr_t)wb >> 32)),
              [k0] "v"(vK[0]), [k1] "v"(vK[1]), [k2] "v"(vK[2]), [k3] "v"(vK[3]), [vv] "v"(vV), [f0] "v"(oF[0]), [f1] "v"(oF[1]), [f2] "v"(oF[2]), [f3] "v"(oF[3]),
              [h0] "v"(oH[0]), [h1] "v"(oH[1]), [h2] "v"(oH[2]), [h3] "v"(oH[3]),
              [wl] "v"(wl), [adone] "v"(a_done), [agate] "v"(a_gate), [aland] "v"(a_landed)
            : "memory", "scc", "vcc", "s60", "s61", "s62", "s63", "s64", "s65", "s66", "s67", "s70", "s71", "s72", "s73", "s75", "s76", "s77", CLOB_V38_255);
        if (roleB) {
            const unsigned li = (unsigned)((idx * 4 + j) * 40 + (lane < 40 ? lane : 0)), row = li / 5, c = li - row * 5; unsigned d;
            asm volatile("global_load_dword %0, %1, %2\n\ts_waitcnt vmcnt(0)" : "=&v"(d) : "v"(row * 1152 + (c < 4 ? c * 128 : 1024)), "s"(wb) : "memory");
            wait_lds(lds + L_GATE, 12, budget);
            DMA4("", wb, oH[0], oH[1], oH[2], oH[3], lds0 + 8 * USLOT + j * 4096);
#pragma nounroll
            for (int k = 0; k < 8; ++k) { const int p = (b + k) & 7; DMA4("", wb + (size_t)p * 73728, oF[0], oF[1], oF[2], oF[3], lds0 + p * USLOT + j * 4096); }
            WAITV(0); lds_put(a_landed, (unsigned)(nkv + NSL));
        }
        if (j != 0) return;
        wait_lds(lds + L_CNT + 8, 1, budget);
        tagf = rfl64(*(volatile LAS const u64*)(lds + L_TAG));
    } else {
        const int rg = wid;
        __builtin_amdgcn_s_setprio(2);
        if (!roleB) {
            unsigned d[5];
            const unsigned i0 = (idx - 16) * 288 + rg * 72 + lane, i1 = i0 - lane + 64 + (lane & 7);
            unsigned oc = (rg * 64 + lane) * 128; oc = oc < code_len && (idx & 3) == 0 ? oc : 0;
            asm volatile("global_load_dword %0, %5, %10 nt\n\tglobal_load_dword %1, %6, %11\n\tglobal_load_dword %2, %7, %10 nt\n\tglobal_load_dword %3, %8, %11\n\t"
                         "s_waitcnt lgkmcnt(0)\n\ts_barrier\n\tglobal_load_dword %4, %9, %12\n\ts_waitcnt vmcnt(0)"
                         : "=&v"(d[0]), "=&v"(d[1]), "=&v"(d[2]), "=&v"(d[3]), "=&v"(d[4])
                         : "v"((i0 >> 3) * 8192 + ks * 1024 + (i0 & 7) * 128), "v"(i0 * 128), "v"((i1 >> 3) * 8192 + ks * 1024 + (i1 & 7) * 128), "v"(i1 * 128), "v"(oc),
                           "s"(Wq), "s"(X), "s"(pc0) : "memory");
        }
        const u64 epoch = __hip_atomic_load((GAS const u64*)epo, RLX_AGENT);
        u64 tag;
        const int q4 = (lane & 15) >> 2, p4 = lane & 3, blk = (lane >> 4) & 1;
        int u = 0, avail = 0;
        unsigned so = lds0;
        unsigned a_landed = lds0 + L_LANDED, a_done = lds0 + L_DONE + 4 * rg; PIN(a_landed); PIN(a_done);
        auto acquire = [&]() AINL -> unsigned {
            avail = __builtin_amdgcn_readfirstlane(avail); budget = __builtin_amdgcn_readfirstlane(budget);
            asm volatile("s_cmp_gt_i32 %0, %2\n\ts_cbranch_scc0 .Las%=\n"
                         ".Lab%=:\n\t.subsection 1\n"
                         ".Las%=:\n\tds_read_b128 v[140:143], %3\n\ts_waitcnt lgkmcnt(0)\n\tv_min_u32 v142, v142, v143\n\tv_min3_u32 v140, v140, v141, v142\n\ts_nop 1\n\tv_readfirstlane_b32 %0, v140\n\ts_nop 3\n\t"
                         "s_cmp_gt_i32 %0, %2\n\ts_cbranch_scc1 .Lab%=\n\ts_sub_i32 %1, %1, 1\n\ts_cmp_lt_i32 %1, 1\n\ts_cbranch_scc1 .Lab%=\n\ts_sleep 1\n\ts_branch .Las%=\n\t.subsection 0"
                         : "+&s"(avail), "+&s"(budget) : "s"(__builtin_amdgcn_readfirstlane(u)), "v"(a_landed) : "memory", "scc", "v140", "v141", "v142", "v143");
            const unsigned o = so; ++u; so = so == lds0 + (NSL - 1) * USLOT ? lds0 : so + USLOT; return o; };
        auto release = [&](int c) AINL { lds_put(a_done, (unsigned)c); };
        auto signoff = [&](int cnt_idx, char* flag) AINL { WAITV(0);
            unsigned old = 0; if (lane == 0) old = __hip_atomic_fetch_add((LAS unsigned*)(lds + L_CNT + 4 * cnt_idx), 1u, RLX_WG);
            if (__builtin_amdgcn_readfirstlane(old) == 3 && lane == 0) __hip_atomic_store((GAS u64*)flag, tag, RLX_AGENT); };
        auto opengate = [&](unsigned n) AINL { asm volatile("" ::: "memory"); if (lane == 0) __hip_atomic_fetch_add((LAS unsigned*)(lds + L_GATE), n, RLX_WG); };
        const unsigned krd = (unsigned)(256 * r32 + 16 * (hi ^ (r32 & 15)));
        bf16x8 qf[8], kf[8]; unsigned ka[8];
#define KRD(s, off) kf[s] = ldsrd16(LP(ka[s]) + (off))
#define KADDR(s, o) do { ka[s] = (krd ^ ((s) << 5)) + (o); PIN(ka[s]); } while (0)
#define KFIRST() do { const unsigned ko_ = acquire(); _Pragma("unroll") for (int s_ = 0; s_ < 8; ++s_) { KADDR(s_, ko_); KRD(s_, 0); } } while (0)
        const unsigned fx = (unsigned)((pair * 4 + rg) * 8192 + lane * 16);
        if (roleB) {
            const int p4s = ((p4 & 1) << 1) | (p4 >> 1);
            unsigned wrd[4], xa[4];
#pragma unroll
            for (int dt = 0; dt < 4; ++dt) wrd[dt] = (unsigned)((rg >> 1) * 256 + 512 * (8 * hi + q4) + 16 * ((4 * dt + 2 * blk + (p4s >> 1)) ^ (q4 << 2)) + 8 * (p4s & 1));
            { const int row = (rg & 1) * 32 + r32;
#pragma unroll
              for (int s = 0; s < 4; ++s) xa[s] = (unsigned)(row * 128 + 16 * ((2 * s + hi) ^ ((row >> 1) & 7))); }
            f32x16 acc[4] = {};
            bf16x8 xf[4], xn[4], wf[8];
#define WRD(i, wo) wf[i] = trrd2(LP((wo) + wrd[(i) & 3]) + ((i) >> 2) * 8192, LP((wo) + wrd[(i) & 3]) + ((i) >> 2) * 8192 + 2048)
#define XRD(s, xo) ldsrd16(LP((xo) + xa[s]))
#define QG(i, wi, wo, X_) do { acc[(i) & 3] = MFMA32(wf[i], xf[2 * (wi) + ((i) >> 2)], acc[(i) & 3]); PIN(acc[(i) & 3]); SBAR(); if ((i) == 0) BAR(); WRD(i, wo); X_; SBAR(); } while (0)
            unsigned sb = lds0;
            asm volatile("s_waitcnt lgkmcnt(0)\n\ts_barrier" ::: "memory");
#pragma unroll
            for (int s = 0; s < 4; ++s) xf[s] = XRD(s, sb);
#pragma unroll
            for (int i = 0; i < 8; ++i) WRD(i, sb + USLOT);
            LGKM(0);
#define QODDPH() do { const unsigned wo = sb + 2 * USLOT; SBAR(); \
                QG(0, 0, wo, (void)0); QG(1, 0, wo, (void)0); QG(2, 0, wo, (void)0); QG(3, 0, wo, (void)0); QG(4, 0, wo, (void)0); QG(5, 0, wo, (void)0); QG(6, 0, wo, (void)0); QG(7, 0, wo, (void)0); } while (0)
#pragma nounroll
            for (int gi = 0; gi < 8; ++gi) {
                QODDPH();
                sb = sb == lds0 + 6 * USLOT ? lds0 : sb + 3 * USLOT;
                SBAR();
                QG(0, 1, sb + USLOT, xn[0] = XRD(0, sb)); QG(1, 1, sb + USLOT, xn[1] = XRD(1, sb)); QG(2, 1, sb + USLOT, (void)0); QG(3, 1, sb + USLOT, (void)0);
                QG(4, 1, sb + USLOT, xn[2] = XRD(2, sb)); QG(5, 1, sb + USLOT, (void)0); QG(6, 1, sb + USLOT, (void)0); QG(7, 1, sb + USLOT, xn[3] = XRD(3, sb));
                xf[0] = xn[0]; xf[1] = xn[1]; xf[2] = xn[2]; xf[3] = xn[3];
            }
            QODDPH();
#pragma unroll
            for (int dt = 0; dt < 4; ++dt) { acc[dt] = MFMA32(wf[dt], xf[2], acc[dt]); acc[dt] = MFMA32(wf[4 + dt], xf[3], acc[dt]); }
            tag = next_tag(rfl64(epoch));
#pragma unroll
            for (int f = 0; f < 8; ++f) { const f32x16& a = acc[f >> 1]; const int r = 8 * (f & 1);
                const u32x4 w = {pk2(a[r] * QSCALE, a[r + 1] * QSCALE), pk2(a[r + 2] * QSCALE, a[r + 3] * QSCALE), pk2(a[r + 4] * QSCALE, a[r + 5] * QSCALE), pk2(a[r + 6] * QSCALE, a[r + 7] * QSCALE)};
                qf[f] = __builtin_bit_cast(bf16x8, w);
                __builtin_amdgcn_raw_buffer_store_b128(w, wsr, (unsigned)WS_QX + fx + f * 1024, 0, AUX_SC1); }
            opengate(1);
            signoff(0, ctl + 256); opengate(1);
            KFIRST();
        } else {
            tag = next_tag(rfl64(epoch));
            KFIRST();
            POLL1(ctl + 256, tag, 1);
#pragma unroll
            for (int s = 0; s < 8; ++s) qf[s] = __builtin_bit_cast(bf16x8, __builtin_amdgcn_raw_buffer_load_b128(wsr, (unsigned)WS_QX + fx + s * 1024, 0, AUX_SC1));
            opengate(2);
        }
        f32x16 O0 = {}, O1 = {}, O2 = {}, O3 = {}, negm, SA, SB; float lsum = 0.f, mrow, ls0, ls1, mx0, mx1; u32x4 pw0, pw1, pk_; unsigned va[4]; s16x4 vh[16];
#define R_O0 "{v[192:207]}"
#define R_O1 "{v[208:223]}"
#define R_O2 "{v[224:239]}"
#define R_O3 "{v[240:255]}"
#define R_SA "{v[176:191]}"
#define R_SB "{v[160:175]}"
#define N_SA "176"
#define N_SB "160"
#define R_NM "{v[144:159]}"
#define PINR(x) asm volatile("" : "+" R_##x(x))
#define EXP2(x) __builtin_amdgcn_exp2f(x)
#define MX3(a, b, c) __builtin_fmaxf(__builtin_fmaxf((a), (b)), (c))
#define VRDH(g, t, off) vh[2 * (g) + (t)] = trrd(LP(va[(g) & 3]) + (off) + ((g) >> 2) * 4096 + (t) * 2048)
#define V2(g, vo) do { VRDH(g, 0, vo); VRDH(g, 1, vo); } while (0)
#define VF(g) __builtin_shufflevector(vh[2 * (g)], vh[2 * (g) + 1], 0, 1, 2, 3, 4, 5, 6, 7)
#define KSTEP(s, d) do { ka[s] += (d); PIN(ka[s]); } while (0)
#define VSTEP(c, d) do { va[c] += (d); PIN(va[c]); } while (0)
#define RESCALE(S, mx, mxb) do { float t0_; asm volatile( \
            "v_max_f32 %8, %8, %11\n\tv_cmp_lt_f32 vcc, %10, %8\n\ts_cbranch_vccnz .Lrs%=\n" \
            ".Lrb%=:\n\t.subsection 1\n" \
            ".Lrs%=:\n\ts_nop 15\n\tv_mov_b32 %9, %8\n\ts_nop 1\n\tv_permlane32_swap_b32 %8, %9\n\ts_nop 1\n\tv_max3_f32 %8, %8, %9, 0\n\t" \
            "v_add_f32 %6, %6, %8\n\tv_exp_f32 %9, -%8\n\t" \
            ".set r_, 0\n\t.rept 16\n\tv_sub_f32 v[" N_##S "+r_], v[" N_##S "+r_], %8\n\tv_sub_f32 v[144+r_], 0, %6\n\t.set r_, r_+1\n\t.endr\n\t" \
            "v_mul_f32 %7, %7, %9\n\t" \
            ".set r_, 192\n\t.rept 64\n\tv_mul_f32 v[r_], v[r_], %9\n\t.set r_, r_+1\n\t.endr\n\ts_nop 1\n\ts_branch .Lrb%=\n\t.subsection 0" \
            : "+" R_O0(O0), "+" R_O1(O1), "+" R_O2(O2), "+" R_O3(O3), "+" R_##S(S), "+" R_NM(negm), "+v"(mrow), "+v"(lsum), "+v"(mx), "=&v"(t0_) : "s"(THR), "v"(mxb) : "vcc"); } while (0)
#define G1(SN, SO, s, CIN, PK, KL) do { SN = MFMA32(kf[s], qf[s], CIN); PINR(SN); SBAR(); PINR(SO); KL; SO[2 * (s)] = EXP2(SO[2 * (s)]); SO[2 * (s) + 1] = EXP2(SO[2 * (s) + 1]); PK; PINR(SO); SBAR(); } while (0)
#define PKW(SO, W, i, r) do { W[i] = pk2(SO[r], SO[(r) + 1]); PIN(W); } while (0)
#define PH1(SN, SO, KL) do { SBAR(); \
        G1(SN, SO, 0, negm, (void)0, KL(0));          G1(SN, SO, 1, SN, PKW(SO, pw0, 0, 0), KL(1)); G1(SN, SO, 2, SN, PKW(SO, pw0, 1, 2), KL(2));  G1(SN, SO, 3, SN, PKW(SO, pw0, 2, 4), KL(3)); \
        G1(SN, SO, 4, SN, PKW(SO, pw0, 3, 6), KL(4)); G1(SN, SO, 5, SN, PKW(SO, pw1, 0, 8), KL(5)); G1(SN, SO, 6, SN, PKW(SO, pw1, 1, 10), KL(6)); G1(SN, SO, 7, SN, PKW(SO, pw1, 2, 12), KL(7)); } while (0)
#define G2(g, OC, W, X_) do { OC = MFMA32(VF(g), __builtin_bit_cast(bf16x8, W), OC); PINR(OC); SBAR(); X_; SBAR(); } while (0)
#define ADD4(SO, r) do { ls0 += SO[r]; ls1 += SO[(r) + 1]; ls0 += SO[(r) + 2]; ls1 += SO[(r) + 3]; PIN(ls0); PIN(ls1); } while (0)
#define PH2(SN, SO, vo, A0, A1, A2, A3) do { SBAR(); \
        G2(0, O0, pw0, { V2(0, vo); PKW(SO, pw1, 3, 14); ls0 = lsum + SO[0]; ls1 = SO[1] + SO[3]; ls0 += SO[2]; PIN(ls0); PIN(ls1); }); G2(1, O1, pw0, { V2(1, vo); ADD4(SO, 4); }); \
        G2(2, O2, pw0, { V2(2, vo); ADD4(SO, 8); }); G2(3, O3, pw0, { V2(3, vo); ADD4(SO, 12); }); \
        G2(4, O0, pw1, { V2(4, vo); mx0 = MX3(SN[0], SN[1], SN[2]); mx1 = MX3(SN[3], SN[4], SN[5]); lsum = ls0 + ls1; PIN(mx0); PIN(mx1); PIN(lsum); A0; }); \
        G2(5, O1, pw1, { V2(5, vo); mx0 = MX3(mx0, SN[6], SN[7]); mx1 = MX3(mx1, SN[8], SN[9]); PIN(mx0); PIN(mx1); A1; }); \
        G2(6, O2, pw1, { V2(6, vo); mx0 = MX3(mx0, SN[10], SN[11]); mx1 = MX3(mx1, SN[12], SN[13]); PIN(mx0); PIN(mx1); A2; }); \
        G2(7, O3, pw1, { V2(7, vo); mx0 = MX3(mx0, SN[14], SN[15]); A3; }); \
        RESCALE(SN, mx0, mx1); } while (0)
#define KL0(s) KRD(s, 0)
#define KL1(s) KRD(s, 8192)
#define KLN(s) (void)0
#define NOP_ (void)0
#define PEEK() do { pk_ = *(volatile LAS const u32x4*)LP(a_landed); } while (0)
#define SEEN() do { unsigned m_ = pk_[0] < pk_[1] ? pk_[0] : pk_[1], n_ = pk_[2] < pk_[3] ? pk_[2] : pk_[3]; const int a_ = __builtin_amdgcn_readfirstlane(m_ < n_ ? m_ : n_); avail = a_ > avail ? a_ : avail; } while (0)
        {
            SA = MFMA32(kf[0], qf[0], (f32x16){});
#pragma unroll
            for (int s = 1; s < 8; ++s) SA = MFMA32(kf[s], qf[s], SA);
#pragma unroll
            for (int s = 0; s < 8; ++s) KRD(s, 8192);
            float mx = MX3(SA[0], SA[1], SA[2]);
#pragma unroll
            for (int r = 3; r < 15; r += 2) mx = MX3(mx, SA[r], SA[r + 1]);
            mx = swap_max(__builtin_fmaxf(mx, SA[15])); mrow = mx;
#pragma unroll
            for (int r = 0; r < 16; ++r) { SA[r] -= mx; negm[r] = -mx; }
            asm volatile("" : "+" R_NM(negm));
        }
        unsigned vcur = acquire(), kcur = so;
#pragma unroll
        for (int c = 0; c < 4; ++c) { va[c] = (unsigned)(256 * (4 * hi + q4) + 16 * ((4 * c + 2 * blk + (p4 >> 1)) ^ (q4 << 2)) + 8 * (p4 & 1)) + vcur; PIN(va[c]); }
#pragma unroll
        for (int g = 0; g < 8; ++g) V2(g, 0);
#pragma unroll
        for (int s = 0; s < 8; ++s) KADDR(s, kcur);
        unsigned tprev = 0, gap = 0; int per = roleB ? PB0 : PA0;
#pragma nounroll
        for (int t = 0; t < NT - 1; ++t) {
            (void)acquire();
            if (roleB || t <= PEND) {
                unsigned now = (unsigned)__builtin_amdgcn_s_memrealtime();
#pragma nounroll
                for (int k = 0; k < 256 && now - tprev < gap; ++k) { __builtin_amdgcn_s_sleep(1); now = (unsigned)__builtin_amdgcn_s_memrealtime(); }
                tprev = now; gap = (unsigned)per; const int d_ = roleB ? PB1 : PA1, m_ = roleB ? PMINB : PMINA; per = per - d_ > m_ ? per - d_ : m_;
            }
            LGKM(15);
            PH1(SB, SA, KL0);
            LGKM(8);
            PH2(SB, SA, 8192, PEEK(), NOP_, NOP_, NOP_);
            LGKM(15);
            PH1(SA, SB, KL1);
            SEEN();
            release(u - 1);
            const unsigned vo = acquire();
            const unsigned dv = vo - vcur, dk = so - kcur; vcur = vo; kcur = so;
            VSTEP(0, dv); VSTEP(1, dv); VSTEP(2, dv); VSTEP(3, dv);
            LGKM(8);
            PH2(SA, SB, 0, { KSTEP(0, dk); KSTEP(1, dk); }, { KSTEP(2, dk); KSTEP(3, dk); }, { KSTEP(4, dk); KSTEP(5, dk); }, { KSTEP(6, dk); KSTEP(7, dk); });
        }
        u64 fpv = 0;
        if (!roleB) fpv = __hip_atomic_load((GAS const u64*)(ctl + 512), RLX_AGENT);
        PH1(SB, SA, KLN);
        release(u - 1);
        u32x4 pf[8]; float plse = 0.f; bool early = false;
#define PLOAD() do { _Pragma("unroll") for (int i = 0; i < 8; ++i) pf[i] = __builtin_amdgcn_raw_buffer_load_b128(wsr, (unsigned)WS_PX + fx + i * 1024, 0, AUX_SC1); \
            plse = __uint_as_float(__builtin_amdgcn_raw_buffer_load_b32(wsr, (unsigned)WS_PL + (pair * 4 + rg) * 256 + lane * 4, 0, AUX_SC1)); } while (0)
        if (!roleB && rfl64(fpv) == tag) { early = true; asm volatile("" ::: "memory"); PLOAD(); }
        PH2(SB, SA, 8192, NOP_, NOP_, NOP_, NOP_);
        {
#pragma unroll
            for (int r = 0; r < 16; ++r) { SB[r] = EXP2(SB[r]); lsum += SB[r]; }
            pw0 = (u32x4){pk2(SB[0], SB[1]), pk2(SB[2], SB[3]), pk2(SB[4], SB[5]), pk2(SB[6], SB[7])};
            pw1 = (u32x4){pk2(SB[8], SB[9]), pk2(SB[10], SB[11]), pk2(SB[12], SB[13]), pk2(SB[14], SB[15])};
            const bf16x8 p0 = __builtin_bit_cast(bf16x8, pw0), p1 = __builtin_bit_cast(bf16x8, pw1);
            O0 = MFMA32(VF(0), p0, O0); O1 = MFMA32(VF(1), p0, O1); O2 = MFMA32(VF(2), p0, O2); O3 = MFMA32(VF(3), p0, O3);
            O0 = MFMA32(VF(4), p1, O0); O1 = MFMA32(VF(5), p1, O1); O2 = MFMA32(VF(6), p1, O2); O3 = MFMA32(VF(7), p1, O3);
        }
        release(u);
        lsum = swap_add(lsum);
        const float lse = mrow + __builtin_amdgcn_logf(lsum);
        if (!roleB) {
            if (!early) { poll_flags(ctl + 512, tag, budget); PLOAD(); }
            const float M = __builtin_fmaxf(lse, plse), eo = EXP2(lse - M), ep = EXP2(plse - M), r = __builtin_amdgcn_rcpf(eo + ep);
            const float fo = eo * r * __builtin_amdgcn_rcpf(lsum), fp = ep * r;
#pragma unroll
            for (int f = 0; f < 8; ++f) { const f32x16& a = f < 2 ? O0 : f < 4 ? O1 : f < 6 ? O2 : O3; const int q = 8 * (f & 1); u32x4 w;
#pragma unroll
                for (int e = 0; e < 4; ++e) w[e] = pk2(__builtin_fmaf(a[q + 2 * e], fo, fp * bflo(pf[f][e])), __builtin_fmaf(a[q + 2 * e + 1], fo, fp * bfhi(pf[f][e])));
                __builtin_amdgcn_raw_buffer_store_b128(w, wsr, (unsigned)WS_OX + fx + f * 1024, 0, AUX_SC1); }
        } else {
            const float inv = __builtin_amdgcn_rcpf(lsum);
#pragma unroll
            for (int f = 0; f < 8; ++f) { const f32x16& a = f < 2 ? O0 : f < 4 ? O1 : f < 6 ? O2 : O3; const int r = 8 * (f & 1);
                const u32x4 w = {pk2(a[r] * inv, a[r + 1] * inv), pk2(a[r + 2] * inv, a[r + 3] * inv), pk2(a[r + 4] * inv, a[r + 5] * inv), pk2(a[r + 6] * inv, a[r + 7] * inv)};
                __builtin_amdgcn_raw_buffer_store_b128(w, wsr, (unsigned)WS_PX + fx + f * 1024, 0, AUX_SC1); }
            __builtin_amdgcn_raw_buffer_store_b32(__float_as_uint(lse), wsr, (unsigned)WS_PL + (pair * 4 + rg) * 256 + lane * 4, 0, AUX_SC1);
        }
        signoff(1, roleB ? ctl + 512 : ctl + 768);
        if (roleB) opengate(1);
        const int qh = rg & 1, nh = rg >> 1;
        *(volatile LAS u64*)(lds + L_TAG) = tag;
        u += NSL - 1; (void)acquire();
        unsigned wa[4], wh = lds0 + 131072 + (nh * 64 + hi) * 128 + 32 * q4 + 128 * blk + 8 * p4; PIN(wh);
#pragma unroll
        for (int c = 0; c < 4; ++c) { wa[c] = lds0 + ((((nh ^ (c >> 1)) * 64 + hi) * 4 + q4) * 256 + 64 * ((2 * nh + (c & 1)) ^ q4) + 32 * blk + 8 * p4); PIN(wa[c]); }
#define AKS(i) ((i) < 48 ? (i) / 3 : 16 + ((i) - 48) / 2)
#define ATL(i) ((i) < 48 ? (i) % 3 : ((i) - 48) % 2)
#define AWA(i) wa[((i) < 48 ? 0 : 2) + (ATL(i) & 1)]
#define ARD(i) (ATL(i) == 2 ? trrd2(LP(wh) + AKS(i) * 512, LP(wh) + AKS(i) * 512 + 256) : trrd2(LP(AWA(i)) + (AKS(i) & 15) * 4096, LP(AWA(i)) + (AKS(i) & 15) * 4096 + 2048))
        constexpr int AD = 7, OPRE = 10;
        bf16x8 af[AD], of[32];
#pragma unroll
        for (int i = 0; i < AD; ++i) af[i] = ARD(i);
        const int pr0 = b * HKV + ks * 2 + nh;
        POLL1(ws + WS_CT + (size_t)(pr0 ^ (lane & 1)) * 1024 + 768, tag, 2);
        const unsigned ob0 = (unsigned)WS_OX + (pr0 * 4 + qh) * 8192 + lane * 16, ob1 = (unsigned)WS_OX + ((pr0 ^ 1) * 4 + qh) * 8192 + lane * 16;
#define OLD(f) of[f] = __builtin_bit_cast(bf16x8, __builtin_amdgcn_raw_buffer_load_b128(wsr, ((f) < 16 ? ob0 : ob1) + ((f) & 3) * 1024, (((f) >> 3) & 1) * 16384 + (((f) >> 2) & 1) * 4096, AUX_SC1))
#pragma unroll
        for (int f = 0; f < OPRE; ++f) OLD(f);
        f32x16 acc[3] = {};
        SBAR();
#pragma unroll
        for (int i = 0; i < 80; ++i) { acc[ATL(i)] = MFMA32(af[i % AD], of[AKS(i)], acc[ATL(i)]); PIN(acc[ATL(i)]); SBAR(); if (i + AD < 80) af[i % AD] = ARD(i + AD);
            if ((i & 1) == 0 && OPRE + i / 2 < 32) OLD(OPRE + i / 2); SBAR(); }
        const unsigned sb = (unsigned)WS_SLAB + ((b * 4 + nq) * 8 + (r32 >> 2)) * 20480 + ks * 2560 + ((lane & 3) * 2 + hi) * 16;
#pragma unroll
        for (int k = 0; k < 5; ++k) { const int kk = k == 0 ? 4 : k - 1; const f32x16& a = acc[kk >> 1]; const int r = 8 * (kk & 1);
            const u32x4 v = {pk2(a[r], a[r + 1]), pk2(a[r + 2], a[r + 3]), pk2(a[r + 4], a[r + 5]), pk2(a[r + 6], a[r + 7])};
            __builtin_amdgcn_raw_buffer_store_b128(v, wsr, sb + (kk < 4 ? (qh * 8 + nh * 4 + kk) * 128 : 2048 + (nh * 2 + qh) * 128), 0, AUX_SC1); }
        signoff(2, fsl + ks * 256);
        tagf = tag;
    }
    {
        POLL1(fsl + (lane & 7) * 256, tagf, 3);
        const int e = wid * 32 + r32;
        const unsigned sb = (unsigned)WS_SLAB + ((b * 4 + nq) * 8 + ks) * 20480 + hi * 10240 + e * 16;
        u32x4 v[4];
#pragma unroll
        for (int k = 0; k < 4; ++k) v[k] = __builtin_amdgcn_raw_buffer_load_b128(wsr, sb + k * 2560, 0, AUX_SC1);
        if (ks == 0 && nq == 0 && tid == 0) __hip_atomic_store((GAS u64*)epo, tagf, RLX_AGENT);
        float s[8], t[4];
#pragma unroll
        for (int c = 0; c < 4; ++c) { s[2 * c] = (bflo(v[0][c]) + bflo(v[1][c])) + (bflo(v[2][c]) + bflo(v[3][c])); s[2 * c + 1] = (bfhi(v[0][c]) + bfhi(v[1][c])) + (bfhi(v[2][c]) + bfhi(v[3][c])); }
#pragma unroll
        for (int c = 0; c < 4; ++c) { auto rr = __builtin_amdgcn_permlane32_swap(__float_as_uint(s[c]), __float_as_uint(s[4 + c]), false, false); t[c] = __uint_as_float(rr[0]) + __uint_as_float(rr[1]); }
        int qh = e >> 6, col = nq * 128 + ((e >> 3) & 7) * 16;
        if (wid == 4) {
#pragma unroll
            for (int c = 0; c < 4; ++c) t[c] += __shfl_xor(t[c], 16);
            qh = (e >> 3) & 1; col = 512 + nq * 16; }
        if (e < 144) *(u32x2*)(out + ((size_t)(b * SQ + qh * 32 + ks * 4 + ((e >> 1) & 3)) * DM + col + hi * 8 + (e & 1) * 4) * 2) = (u32x2){pk2(t[0], t[1]), pk2(t[2], t[3])};
    }
}

extern "C" void kernel_launch(void* const* d_in, const int* in_sizes, int n_in, void* d_out, int out_size, void* d_ws, size_t ws_size, hipStream_t stream) {
    (void)hipFuncSetAttribute((const void*)gqa_block_k, hipFuncAttributeMaxDynamicSharedMemorySize, LDS_BYTES);
    hipLaunchKernelGGL(gqa_block_k, dim3(256), dim3(512), LDS_BYTES, stream, (const char*)d_in[0], (const char*)d_in[1], (const char*)d_in[2], (const char*)d_in[3],
                       (const char*)d_in[4], (char*)d_out, (char*)d_ws);
}
```

```cpp
#define GSEL 128
#include <hip/hip_runtime.h>
#include <hip/hip_bf16.h>
#include <stdint.h>

typedef short bf16x8 __attribute__((ext_vector_type(8)));
typedef short s16x4 __attribute__((ext_vector_type(4)));
typedef float f32x16 __attribute__((ext_vector_type(16)));
typedef float f32x2 __attribute__((ext_vector_type(2)));
typedef unsigned u32x4 __attribute__((ext_vector_type(4)));
typedef unsigned u32x2 __attribute__((ext_vector_type(2)));
typedef __bf16 bf2_t __attribute__((ext_vector_type(2)));
typedef unsigned long long u64;
#define LAS __attribute__((address_space(3)))
#define GAS __attribute__((address_space(1)))
typedef LAS const char* lcptr;
typedef LAS char* lptr;
#define DI __device__ __forceinline__
#define AINL __attribute__((always_inline))
#define MFMA32(a, b, c) __builtin_amdgcn_mfma_f32_32x32x16_bf16((a), (b), (c), 0, 0, 0)
#define WAITV(N) asm volatile("s_waitcnt vmcnt(" #N ")" ::: "memory")
#define BAR() asm volatile("s_barrier" ::: "memory")
#define PIN(x) asm volatile("" : "+v"(x))
#define SBAR() __builtin_amdgcn_sched_barrier(0)
#define LGKM(n) do { __builtin_amdgcn_s_waitcnt(0xC07F | ((n) << 8)); SBAR(); } while (0)
#define LP(a) ((lcptr)(uintptr_t)(a))
#define STR2(x) #x
#define STR(x) STR2(x)
#define CLOB_V38_255 "v38", "v39", "v40", "v41", "v42", "v43", "v44", "v45", "v46", "v47", "v48", "v49", "v50", "v51", "v52", "v53", "v54", "v55", "v56", "v57", "v58", "v59", "v60", "v61", "v62", "v63", "v64", "v65", "v66", "v67", "v68", "v69", "v70", "v71", "v72", "v73", "v74", "v75", "v76", "v77", "v78", "v79", "v80", "v81", "v82", "v83", "v84", "v85", "v86", "v87", "v88", "v89", "v90", "v91", "v92", "v93", "v94", "v95", "v96", "v97", "v98", "v99", "v100", "v101", "v102", "v103", "v104", "v105", "v106", "v107", "v108", "v109", "v110", "v111", "v112", "v113", "v114", "v115", "v116", "v117", "v118", "v119", "v120", "v121", "v122", "v123", "v124", "v125", "v126", "v127", "v128", "v129", "v130", "v131", "v132", "v133", "v134", "v135", "v136", "v137", "v138", "v139", "v140", "v141", "v142", "v143", "v144", "v145", "v146", "v147", "v148", "v149", "v150", "v151", "v152", "v153", "v154", "v155", "v156", "v157", "v158", "v159", "v160", "v161", "v162", "v163", "v164", "v165", "v166", "v167", "v168", "v169", "v170", "v171", "v172", "v173", "v174", "v175", "v176", "v177", "v178", "v179", "v180", "v181", "v182", "v183", "v184", "v185", "v186", "v187", "v188", "v189", "v190", "v191", "v192", "v193", "v194", "v195", "v196", "v197", "v198", "v199", "v200", "v201", "v202", "v203", "v204", "v205", "v206", "v207", "v208", "v209", "v210", "v211", "v212", "v213", "v214", "v215", "v216", "v217", "v218", "v219", "v220", "v221", "v222", "v223", "v224", "v225", "v226", "v227", "v228", "v229", "v230", "v231", "v232", "v233", "v234", "v235", "v236", "v237", "v238", "v239", "v240", "v241", "v242", "v243", "v244", "v245", "v246", "v247", "v248", "v249", "v250", "v251", "v252", "v253", "v254", "v255"
#define RLX_AGENT __ATOMIC_RELAXED, __HIP_MEMORY_SCOPE_AGENT
#define RLX_WG __ATOMIC_RELAXED, __HIP_MEMORY_SCOPE_WORKGROUP
#define AUX_SC1 16

#define NTA 18
#define GATEA 10
#define HPRE 2
#define PA0 110
#define PA1 2
#define PMINA 103
#define PB0 150
#define PB1 9
#define PMINB 103
#define PEND 13

constexpr int SQ = 64, DM = 576, HKV = 16, SK = 2048;
constexpr float QSCALE = 0.08838834764831845f * 1.4426950408889634f;
constexpr float THR = 8.f;
constexpr int NT_A = NTA, NT_B = 32 - NTA;
constexpr int USLOT = 16384, NSL = 9, MISC = NSL * USLOT, LDS_BYTES = 163840;
constexpr int L_LANDED = MISC, L_DONE = MISC + 16, L_CNT = MISC + 32, L_GATE = MISC + 44, L_TAG = MISC + 64;
constexpr int SPIN_CAP = 1 << 20;
constexpr size_t WS_CT = 0;
constexpr size_t WS_EP = 128 * 1024, WS_FS = WS_EP + 8 * 256;
constexpr size_t WS_QX = 1 << 20;
constexpr size_t WS_PX = WS_QX + ((size_t)4 << 20);
constexpr size_t WS_PL = WS_PX + ((size_t)4 << 20);
constexpr size_t WS_OX = WS_PL + ((size_t)1 << 20);
constexpr size_t WS_SLAB = WS_OX + ((size_t)4 << 20);
constexpr int WS_USED = 32 << 20;

DI unsigned pk2(float a, float b) { bf2_t r = __builtin_convertvector((f32x2){a, b}, bf2_t); return __builtin_bit_cast(unsigned, r); }
DI float bflo(unsigned v) { return __uint_as_float(v << 16); }
DI float bfhi(unsigned v) { return __uint_as_float(v & 0xffff0000u); }
DI s16x4 trrd(lcptr p) { typedef short v4i16_t __attribute__((ext_vector_type(4))); return __builtin_bit_cast(s16x4, __builtin_amdgcn_ds_read_tr16_b64_v4i16((LAS v4i16_t*)p)); }
DI bf16x8 trrd2(lcptr p0, lcptr p1) { s16x4 a = trrd(p0), b = trrd(p1); return __builtin_shufflevector(a, b, 0, 1, 2, 3, 4, 5, 6, 7); }
DI bf16x8 ldsrd16(lcptr p) { return *(LAS const bf16x8*)p; }
DI float swap_max(float m) { auto rr = __builtin_amdgcn_permlane32_swap(__float_as_uint(m), __float_as_uint(m), false, false); return __builtin_fmaxf(__uint_as_float(rr[0]), __uint_as_float(rr[1])); }
DI float swap_add(float m) { auto rr = __builtin_amdgcn_permlane32_swap(__float_as_uint(m), __float_as_uint(m), false, false); return __uint_as_float(rr[0]) + __uint_as_float(rr[1]); }
DI u64 next_tag(u64 e) { return e * 6364136223846793005ull + 1442695040888963407ull; }
DI u64 rfl64(u64 v) { return ((u64)(unsigned)__builtin_amdgcn_readfirstlane((unsigned)(v >> 32)) << 32) | (unsigned)__builtin_amdgcn_readfirstlane((unsigned)v); }
DI void lds_put(unsigned a, unsigned val) { asm volatile("ds_write_b32 %0, %1" :: "v"(a), "v"(val) : "memory"); }
DI void poll_flags(const char* p, u64 tag, int& budget) {
#pragma nounroll
    while (!__all(__hip_atomic_load((GAS const u64*)p, RLX_AGENT) == tag) && --budget > 0) {}
    asm volatile("" ::: "memory"); }
DI void wait_lds(lcptr p, unsigned need, int& budget) {
#pragma nounroll
    while ((unsigned)__builtin_amdgcn_readfirstlane(*(volatile LAS const unsigned*)p) < need && --budget > 0) __builtin_amdgcn_s_sleep(1); }

#define DMA4(POL, sbase, v0, v1, v2, v3, ldst) do { unsigned sv_; asm volatile("s_mov_b32 %0, m0\n\ts_mov_b32 m0, %6\n\ts_nop 0\n\t" \
    "global_load_lds_dwordx4 %1, %5" POL "\n\tglobal_load_lds_dwordx4 %2, %5 offset:1024" POL "\n\tglobal_load_lds_dwordx4 %3, %5 offset:2048" POL "\n\tglobal_load_lds_dwordx4 %4, %5 offset:3072" POL "\n\t" \
    "s_mov_b32 m0, %0" : "=&s"(sv_) : "v"(v0), "v"(v1), "v"(v2), "v"(v3), "s"(sbase), "s"(__builtin_amdgcn_readfirstlane(ldst)) : "memory"); } while (0)

__global__ __launch_bounds__(512, 2) void gqa_block_k(const char* __restrict__ X, const char* __restrict__ Kc, const char* __restrict__ Vc, const char* __restrict__ Wq,
                                                      const char* __restrict__ Wo, char* __restrict__ out, char* __restrict__ ws) {
    extern __shared__ __attribute__((aligned(16))) char lds_[];
    const int tid = threadIdx.x, lane = tid & 63, r32 = lane & 31, hi = lane >> 5;
    const int wid = __builtin_amdgcn_readfirstlane(tid >> 6);
    const int bid = blockIdx.x, ks = bid & 7, idx = bid >> 3;
    const int kvh = ks * 2 + (idx & 1), b = (idx >> 1) & 7;
    const bool roleB = idx < 16;
    const int pair = b * HKV + kvh;
    const int nq = (idx & 1) + (roleB ? 0 : 2);
    const lptr lds = (lptr)lds_;
    const unsigned lds0 = (unsigned)(uintptr_t)lds;
    const int NT = roleB ? NT_B : NT_A;
    char* const ctl = ws + WS_CT + (size_t)pair * 1024;
    char* const epo = ws + WS_EP + b * 256;
    char* const fsl = ws + WS_FS + (size_t)(b * 4 + nq) * 8 * 256;
    const __amdgpu_buffer_rsrc_t wsr = __builtin_amdgcn_make_buffer_rsrc(ws, 0, WS_USED, 0x00020000);
    const int tstart_ = (int)(unsigned)__builtin_amdgcn_s_memrealtime();
    u64 pc0; unsigned code_len;
    asm volatile("s_getpc_b64 %0\n.Lcs%=:\n\ts_mov_b32 %1, .Lce%=-.Lcs%=\n\t.subsection 2\n.Lce%=:\n\t.fill 128, 4, 0xBF800000\n\t.subsection 0" : "=s"(pc0), "=s"(code_len));
    if (tid < 16) *(LAS unsigned*)(lds + MISC + tid * 4) = 0u;
    int budget = SPIN_CAP;
    u64 tagf;

    if (wid >= 4) {
        const int j = wid - 4, lr = lane >> 4, lc = lane & 15;
        const size_t koff = ((size_t)pair * SK + (roleB ? NT_A * 64 : 0)) * 256;
        const char* kp = Kc + koff; const char* vp = Vc + koff;
        unsigned vK[4];
#pragma unroll
        for (int i = 0; i < 4; ++i) vK[i] = (unsigned)((16 * j + lr) * 256 + 16 * (lc ^ (4 * i + lr)));
        const unsigned vV = (unsigned)((16 * j + lr) * 256 + 16 * (lc ^ (lr << 2)));
        unsigned a_landed = lds0 + L_LANDED + 4 * j, a_done = lds0 + L_DONE; PIN(a_landed); PIN(a_done);
        if (roleB) {
            unsigned vX[2], vW[4];
#pragma unroll
            for (int i = 0; i < 2; ++i) { const int row = 16 * j + 8 * i + (lane >> 3); vX[i] = (unsigned)(row * 1152 + 16 * ((lane & 7) ^ ((row >> 1) & 7)) - 1024 * i); }
#pragma unroll
            for (int i = 0; i < 4; ++i) vW[i] = (unsigned)((8 * j + 2 * i + hi) * 8192 + 16 * (r32 ^ (((2 * i + hi) & 3) << 2)) - 1024 * i);
            const char* xb = X + (size_t)b * SQ * DM * 2; const char* wb = Wq + kvh * 512;
            int xo = b * 128, sb = 0, cnt = 7;
#define QADV "s_add_i32 %[xo], %[xo], 128\n\ts_cmp_eq_u32 %[xo], 1152\n\ts_cselect_b32 %[xo], 0, %[xo]\n\ts_add_i32 %[sb], %[sb], 0xc000\n\ts_cmp_eq_u32 %[sb], 0x24000\n\ts_cselect_b32 %[sb], 0, %[sb]\n\t"
#define QW4 "global_load_lds_dwordx4 %[w0], s[60:61]\n\tglobal_load_lds_dwordx4 %[w1], s[60:61] offset:1024\n\tglobal_load_lds_dwordx4 %[w2], s[60:61] offset:2048\n\tglobal_load_lds_dwordx4 %[w3], s[60:61] offset:3072\n\t"
#define QEVEN0 "s_add_u32 s62, %[xl], %[xo]\n\ts_addc_u32 s63, %[xh], 0\n\ts_lshl_b32 s66, %[xo], 12\n\ts_add_u32 s60, %[wl], s66\n\ts_addc_u32 s61, %[wh], 0\n\ts_add_i32 m0, %[sb], %[dx]\n\ts_add_i32 s66, %[sb], %[dw]\n\t"
#define QEVEN1 "global_load_lds_dwordx4 %[x0], s[62:63]\n\tglobal_load_lds_dwordx4 %[x1], s[62:63] offset:1024\n\ts_mov_b32 m0, s66\n\ts_nop 0\n\t" QW4
#define QODD0 "s_add_u32 s60, s60, 0x40000\n\ts_addc_u32 s61, s61, 0\n\ts_add_i32 m0, m0, 0x4000\n\t"
            asm volatile(
                "s_mov_b32 s70, m0\n\t"
                QEVEN0 QEVEN1 QODD0 "s_nop 0\n\t" QW4 QADV QEVEN0 QEVEN1 QODD0
                "s_waitcnt vmcnt(10)\n"
                ".Lqr%=:\n\t"
                "s_barrier\n\t" QW4 QADV QEVEN0 "s_waitcnt vmcnt(10)\n\t"
                "s_barrier\n\t" QEVEN1 QODD0 "s_sub_i32 %[cnt], %[cnt], 1\n\ts_cmp_gt_i32 %[cnt], 0\n\ts_waitcnt vmcnt(10)\n\ts_cbranch_scc1 .Lqr%=\n\t"
                "s_barrier\n\t" QW4 "s_waitcnt vmcnt(10)\n\t"
                "s_barrier\n\ts_waitcnt vmcnt(4)\n\t"
                "s_barrier\n\ts_waitcnt vmcnt(0)\n\t"
                "s_barrier\n\ts_mov_b32 m0, s70"
                : [xo] "+&s"(xo), [sb] "+&s"(sb), [cnt] "+&s"(cnt)
                : [xl] "s"((unsigned)(uintptr_t)xb), [xh] "s"((unsigned)((uintptr_t)xb >> 32)), [wl] "s"((unsigned)(uintptr_t)wb), [wh] "s"((unsigned)((uintptr_t)wb >> 32)),
                  [dx] "s"(lds0 + j * 2048), [dw] "s"(lds0 + USLOT + j * 4096),
                  [x0] "v"(vX[0]), [x1] "v"(vX[1]), [w0] "v"(vW[0]), [w1] "v"(vW[1]), [w2] "v"(vW[2]), [w3] "v"(vW[3])
                : "memory", "scc", "s60", "s61", "s62", "s63", "s66", "s70");
        } else {
            DMA4(" nt", kp, vK[0], vK[1], vK[2], vK[3], lds0 + j * 4096); DMA4(" nt", vp, vV, vV, vV, vV, lds0 + USLOT + j * 4096);
            BAR();
        }
        const char* wb = Wo + (size_t)ks * 512 * 1152;
        unsigned oF[4], oH[4];
#pragma unroll
        for (int i = 0; i < 4; ++i) { oF[i] = (unsigned)(((4 * j + i) * 4 + lr) * 1152 + nq * 256 + 16 * (lc ^ (lr << 2)) - 1024 * i);
            oH[i] = (unsigned)((j * 128 + i * 32 + (lane >> 1)) * 1152 + 1024 + nq * 32 + (lane & 1) * 16 - 1024 * i); }
        const unsigned wl = (unsigned)(lds0 + j * 4096 + lane * 16), a_gate = lds0 + L_GATE;
        const int nkv = 2 * NT, ntot = roleB ? nkv : nkv + NSL, n0 = roleB ? 0 : 2;
        int ni = n0, nl = 0, nw = 0, bi = 0, bw = 0, gate = roleB ? 0 : GATEA, si = n0 * USLOT, sw = 0, lim = NSL, wc = roleB ? 9 : 0;
        static_assert(GATEA >= NSL && GATEA <= NSL + 4, "four register buffers");
#define WQUOTA(L) "s_sub_i32 s67, %[ni], " STR(GATEA) "\n\ts_lshr_b32 s67, s67, 1\n\ts_add_i32 s67, s67, 3\n\ts_cmp_ge_i32 %[wc], s67\n\ts_cbranch_scc1 " L "\n\t"
        asm volatile(
            "s_mov_b32 s70, m0\n\ts_mov_b64 s[72:73], 0\n\ts_mov_b32 s75, 0\n\ts_getpc_b64 s[64:65]\n"
            ".Lp%=:\n\t"
            "s_branch .Lprep%=\n"
            ".Ltop%=:\n\t"
            "s_sub_i32 s66, %[ni], %[nl]\n\ts_cmp_lt_i32 s66, 3\n\ts_cbranch_scc1 .Lc1%=\n\t"
            "s_waitcnt vmcnt(8)\n\ts_add_i32 %[nl], %[nl], 1\n\t"
            ".Lc1%=:\n\t"
            "s_waitcnt lgkmcnt(0)\n\tv_min_u32 v42, v42, v43\n\tv_min3_u32 v40, v40, v41, v42\n\ts_mov_b32 s71, 0\n\ts_cmp_lt_i32 %[nw], %[nl]\n\tv_readfirstlane_b32 s67, v40\n\t"
            "s_cbranch_scc0 .Lc2%=\n\t"
            "s_bitcmp1_b64 s[72:73], %[nw]\n\ts_cbranch_scc1 .Lwr%=\n"
            ".Lpb%=:\n\t"
            "s_add_i32 %[nw], %[nw], 1\n\tv_mov_b32 v38, %[nw]\n\tds_write_b32 %[aland], v38\n\t"
            "s_add_i32 %[sw], %[sw], 0x4000\n\ts_cmp_eq_u32 %[sw], 0x24000\n\ts_cselect_b32 %[sw], 0, %[sw]\n\ts_mov_b32 s71, 1\n\t"
            "s_cmp_lt_i32 %[nw], %[nl]\n\ts_cbranch_scc1 .Lmore%=\n"
            ".Lc2%=:\n\t"
            "s_add_i32 s67, s67, 9\n\ts_max_i32 %[lim], %[lim], s67\n\t"
            "s_cmp_ge_i32 %[ni], %[gate]\n\ts_cbranch_scc1 .Lno%=\n\t"
            "s_cmp_lt_i32 %[ni], %[lim]\n\ts_cbranch_scc0 .Lis%=\n\t"
            "global_load_lds_dwordx4 v44, s[60:61] nt\n\tglobal_load_lds_dwordx4 v45, s[60:61] offset:1024 nt\n\t"
            "global_load_lds_dwordx4 v46, s[60:61] offset:2048 nt\n\tglobal_load_lds_dwordx4 v47, s[60:61] offset:3072 nt\n"
            ".Lbk%=:\n\t"
            "s_add_i32 %[ni], %[ni], 1\n\ts_mov_b32 s75, 1\n\t"
            "s_add_i32 %[si], %[si], 0x4000\n\ts_cmp_eq_u32 %[si], 0x24000\n\ts_cselect_b32 %[si], 0, %[si]\n"
            ".Lprep%=:\n\t"
            "ds_read_b128 v[40:43], %[adone]\n\t"
            "s_add_i32 m0, %[si], %[dbase]\n\t"
            "s_lshr_b32 s66, %[ni], 1\n\ts_lshl_b32 s66, s66, 14\n\t"
            "s_bitcmp1_b32 %[ni], 0\n\ts_cselect_b32 s60, %[vpl], %[kpl]\n\ts_cselect_b32 s61, %[vph], %[kph]\n\ts_cselect_b64 vcc, -1, 0\n\t"
            "s_add_u32 s60, s60, s66\n\ts_addc_u32 s61, s61, 0\n\t"
            "v_cndmask_b32 v44, %[k0], %[vv], vcc\n\tv_cndmask_b32 v45, %[k1], %[vv], vcc\n\tv_cndmask_b32 v46, %[k2], %[vv], vcc\n\tv_cndmask_b32 v47, %[k3], %[vv], vcc\n\t"
            "s_branch .Ltop%=\n"
            ".Lmore%=:\n\ts_bitcmp1_b64 s[72:73], %[nw]\n\ts_cbranch_scc1 .Lwr%=\n\ts_branch .Lpb%=\n"
            ".Lno%=:\n\t"
            "s_cmp_ge_i32 %[gate], %[nkv]\n\ts_cbranch_scc1 .Lno2%=\n\t"
            "ds_read_b32 v40, %[agate]\n\ts_waitcnt lgkmcnt(0)\n\tv_readfirstlane_b32 s67, v40\n\ts_nop 3\n\t"
            "s_cmp_ge_u32 s67, 4\n\ts_cselect_b32 %[gate], %[g1], %[gate]\n\ts_cmp_ge_u32 s67, 8\n\ts_cselect_b32 %[gate], %[nkv], %[gate]\n\t"
            "s_cmp_lt_i32 %[ni], %[gate]\n\ts_cselect_b32 s71, 1, s71\n"
            ".Lno2%=:\n\t"
            "ds_read_b128 v[40:43], %[adone]\n\t"
            "s_cmp_lg_u32 s71, 0\n\ts_cbranch_scc1 .Ltop%=\n\t"
            "s_sub_i32 s66, %[ni], %[nl]\n\ts_cmp_eq_u32 s66, 0\n\ts_cbranch_scc1 .Lno3%=\n\t"
            "s_cmp_eq_u32 s66, 2\n\ts_cbranch_scc1 .Lw4%=\n\ts_waitcnt vmcnt(0)\n"
            ".Lw4%=:\n\ts_waitcnt vmcnt(4)\n\ts_add_i32 %[nl], %[nl], 1\n\ts_branch .Lc1%=\n"
            ".Lno3%=:\n\ts_cmp_ge_i32 %[nw], %[ntot]\n\ts_cbranch_scc1 .Lend%=\n\t"
            "s_cmp_lt_i32 %[gate], %[nkv]\n\ts_cbranch_scc1 .Lidle%=\n\t"
            "s_cmp_lt_i32 %[wc], 9\n\ts_cbranch_scc1 .Lwo0%=\n\t"
            "s_cmp_eq_u32 %[ni], %[nkv]\n\ts_cbranch_scc0 .Lidle%=\n\ts_cmp_eq_u32 %[ni], %[ntot]\n\ts_cbranch_scc1 .Lidle%=\n\t"
            "s_waitcnt vmcnt(0)\n\ts_lshl_b64 s[62:63], 0x1ff, %[nkv]\n\ts_or_b64 s[72:73], s[72:73], s[62:63]\n\ts_mov_b32 %[ni], %[ntot]\n\ts_mov_b32 %[nl], %[ntot]\n\ts_branch .Lc1%=\n"
            ".Lidle%=:\n\t"
            "s_sleep 1\n\ts_sub_i32 %[bud], %[bud], 1\n\ts_cmp_gt_i32 %[bud], 0\n\ts_cbranch_scc1 .Lc1%=\n\t"
            "s_mov_b32 %[lim], 0x3fffffff\n\ts_mov_b32 %[gate], %[nkv]\n\ts_branch .Lc1%=\n"
            ".Lrf%=:\n\t"
            "s_cmp_eq_u32 s75, 0\n\ts_cbranch_scc1 .Lno2%=\n\ts_cmp_ge_i32 %[wc], 9\n\ts_cbranch_scc1 .Lno2%=\n\t"
            WQUOTA(".Lno2%=")
            "s_mov_b32 s75, 0\n\tds_read_b128 v[40:43], %[adone]\n\ts_branch .Lwo%=\n"
            ".Lwo0%=:\n\t"
            WQUOTA(".Lidle%=")
            "s_waitcnt vmcnt(8)\n"
            ".Lwo%=:\n\t"
            "s_and_b32 s66, %[wc], 7\n\ts_mul_i32 s66, s66, 73728\n\ts_add_u32 s76, %[wpl], s66\n\ts_addc_u32 s77, %[wph], 0\n\t"
            "s_mul_i32 s66, %[wc], 36\n\ts_add_u32 s66, s66, .Lit2%=-.Lp%=\n\ts_add_u32 s62, s64, s66\n\ts_addc_u32 s63, s65, 0\n\t"
            "s_add_i32 %[wc], %[wc], 1\n\t"
            "s_setpc_b64 s[62:63]\n"
            ".Lit2%=:\n\t"
            ".set r_, 112\n\t.rept 8\n\t"
            "global_load_dwordx4 v[r_:r_+3], %[f0], s[76:77] nt\n\tglobal_load_dwordx4 v[r_+4:r_+7], %[f1], s[76:77] offset:1024 nt\n\t"
            "global_load_dwordx4 v[r_+8:r_+11], %[f2], s[76:77] offset:2048 nt\n\tglobal_load_dwordx4 v[r_+12:r_+15], %[f3], s[76:77] offset:3072 nt\n\ts_branch .Lc1%=\n\t"
            ".set r_, r_+16\n\t.endr\n\t"
            "global_load_dwordx4 v[240:243], %[h0], s[76:77] nt\n\tglobal_load_dwordx4 v[244:247], %[h1], s[76:77] offset:1024 nt\n\t"
            "global_load_dwordx4 v[248:251], %[h2], s[76:77] offset:2048 nt\n\tglobal_load_dwordx4 v[252:255], %[h3], s[76:77] offset:3072 nt\n\ts_branch .Lc1%=\n"
            ".Lis%=:\n\t"
            "s_cmp_ge_i32 %[ni], " STR(GATEA) "\n\ts_cbranch_scc1 .Lrf%=\n\t"
            "s_add_u32 s67, %[bi], .Lit%=-.Lp%=\n\ts_add_u32 s62, s64, s67\n\ts_addc_u32 s63, s65, 0\n\t"
            "s_bitset1_b64 s[72:73], %[ni]\n\ts_add_i32 %[bi], %[bi], 36\n\t"
            "s_setpc_b64 s[62:63]\n"
            ".Lit%=:\n\t"
            ".set r_, 48\n\t.rept 4\n\t"
            "global_load_dwordx4 v[r_:r_+3], v44, s[60:61] nt\n\tglobal_load_dwordx4 v[r_+4:r_+7], v45, s[60:61] offset:1024 nt\n\t"
            "global_load_dwordx4 v[r_+8:r_+11], v46, s[60:61] offset:2048 nt\n\tglobal_load_dwordx4 v[r_+12:r_+15], v47, s[60:61] offset:3072 nt\n\ts_branch .Lbk%=\n\t"
            ".set r_, r_+16\n\t.endr\n"
            ".Lwr%=:\n\t"
            "s_add_i32 s66, s67, 9\n\ts_max_i32 %[lim], %[lim], s66\n\t"
            "s_cmp_lt_i32 %[nw], %[lim]\n\ts_cbranch_scc0 .Lc2%=\n\t"
            "v_add_u32 v39, %[sw], %[wl]\n\t"
            "s_lshr_b32 s66, %[sw], 14\n\ts_mul_i32 s66, s66, 36\n\ts_add_i32 s66, s66, 144\n\ts_cmp_lt_i32 %[nw], %[nkv]\n\ts_cselect_b32 s66, %[bw], s66\n\t"
            "s_add_u32 s66, s66, .Lwt%=-.Lp%=\n\ts_add_u32 s62, s64, s66\n\ts_addc_u32 s63, s65, 0\n\t"
            "s_add_i32 %[bw], %[bw], 36\n\t"
            "s_setpc_b64 s[62:63]\n"
            ".Lwt%=:\n\t"
            ".set r_, 48\n\t.rept 13\n\t"
            "ds_write_b128 v39, v[r_:r_+3]\n\tds_write_b128 v39, v[r_+4:r_+7] offset:1024\n\tds_write_b128 v39, v[r_+8:r_+11] offset:2048\n\tds_write_b128 v39, v[r_+12:r_+15] offset:3072\n\ts_branch .Lpb%=\n\t"
            ".set r_, r_+16\n\t.endr\n"
            ".Lend%=:\n\ts_mov_b32 m0, s70\n\ts_waitcnt lgkmcnt(0)"
            : [ni] "+&s"(ni), [nl] "+&s"(nl), [nw] "+&s"(nw), [bi] "+&s"(bi), [bw] "+&s"(bw), [gate] "+&s"(gate), [si] "+&s"(si), [sw] "+&s"(sw), [lim] "+&s"(lim),
              [wc] "+&s"(wc), [bud] "+&s"(budget)
            : [nkv] "s"(nkv), [ntot] "s"(ntot), [g1] "s"(roleB ? HPRE : GATEA), [dbase] "s"(lds0 + j * 4096),
              [kpl] "s"((unsigned)(uintptr_t)kp), [kph] "s"((unsigned)((uintptr_t)kp >> 32)), [vpl] "s"((unsigned)(uintptr_t)vp), [vph] "s"((unsigned)((uintptr_t)vp >> 32)),
              [wpl] "s"((unsigned)(uintptr_t)wb), [wph] "s"((unsigned)((uintptr_t)wb >> 32)),
              [k0] "v"(vK[0]), [k1] "v"(vK[1]), [k2] "v"(vK[2]), [k3] "v"(vK[3]), [vv] "v"(vV), [f0] "v"(oF[0]), [f1] "v"(oF[1]), [f2] "v"(oF[2]), [f3] "v"(oF[3]),
              [h0] "v"(oH[0]), [h1] "v"(oH[1]), [h2] "v"(oH[2]), [h3] "v"(oH[3]),
              [wl] "v"(wl), [adone] "v"(a_done), [agate] "v"(a_gate), [aland] "v"(a_landed)
            : "memory", "scc", "vcc", "s60", "s61", "s62", "s63", "s64", "s65", "s66", "s67", "s70", "s71", "s72", "s73", "s75", "s76", "s77", CLOB_V38_255);
        if (roleB) {
            const unsigned li = (unsigned)((idx * 4 + j) * 40 + (lane < 40 ? lane : 0)), row = li / 5, c = li - row * 5; unsigned d;
            asm volatile("global_load_dword %0, %1, %2\n\ts_waitcnt vmcnt(0)" : "=&v"(d) : "v"(row * 1152 + (c < 4 ? c * 128 : 1024)), "s"(wb) : "memory");
            wait_lds(lds + L_GATE, 12, budget);
            DMA4("", wb, oH[0], oH[1], oH[2], oH[3], lds0 + 8 * USLOT + j * 4096);
#pragma nounroll
            for (int k = 0; k < 8; ++k) { const int p = (b + k) & 7; DMA4("", wb + (size_t)p * 73728, oF[0], oF[1], oF[2], oF[3], lds0 + p * USLOT + j * 4096); }
            WAITV(0); lds_put(a_landed, (unsigned)(nkv + NSL));
        }
        if (j != 0) return;
        wait_lds(lds + L_CNT + 8, 1, budget);
        tagf = rfl64(*(volatile LAS const u64*)(lds + L_TAG));
    } else {
        const int rg = wid;
        __builtin_amdgcn_s_setprio(2);
        if (!roleB) {
            unsigned d[5];
            const unsigned i0 = (idx - 16) * 288 + rg * 72 + lane, i1 = i0 - lane + 64 + (lane & 7);
            unsigned oc = (rg * 64 + lane) * 128; oc = oc < code_len && (idx & 3) == 0 ? oc : 0;
            asm volatile("global_load_dword %0, %5, %10 nt\n\tglobal_load_dword %1, %6, %11\n\tglobal_load_dword %2, %7, %10 nt\n\tglobal_load_dword %3, %8, %11\n\t"
                         "s_waitcnt lgkmcnt(0)\n\ts_barrier\n\tglobal_load_dword %4, %9, %12\n\ts_waitcnt vmcnt(0)"
                         : "=&v"(d[0]), "=&v"(d[1]), "=&v"(d[2]), "=&v"(d[3]), "=&v"(d[4])
                         : "v"((i0 >> 3) * 8192 + ks * 1024 + (i0 & 7) * 128), "v"(i0 * 128), "v"((i1 >> 3) * 8192 + ks * 1024 + (i1 & 7) * 128), "v"(i1 * 128), "v"(oc),
                           "s"(Wq), "s"(X), "s"(pc0) : "memory");
        }
        const u64 epoch = __hip_atomic_load((GAS const u64*)epo, RLX_AGENT);
        u64 tag;
        const int q4 = (lane & 15) >> 2, p4 = lane & 3, blk = (lane >> 4) & 1;
        int u = 0, avail = 0;
        unsigned so = lds0;
        unsigned a_landed = lds0 + L_LANDED, a_done = lds0 + L_DONE + 4 * rg; PIN(a_landed); PIN(a_done);
        auto acquire = [&]() AINL -> unsigned {
            avail = __builtin_amdgcn_readfirstlane(avail); budget = __builtin_amdgcn_readfirstlane(budget);
            asm volatile("s_cmp_gt_i32 %0, %2\n\ts_cbranch_scc0 .Las%=\n"
                         ".Lab%=:\n\t.subsection 1\n"
                         ".Las%=:\n\tds_read_b128 v[140:143], %3\n\ts_waitcnt lgkmcnt(0)\n\tv_min_u32 v142, v142, v143\n\tv_min3_u32 v140, v140, v141, v142\n\ts_nop 1\n\tv_readfirstlane_b32 %0, v140\n\ts_nop 3\n\t"
                         "s_cmp_gt_i32 %0, %2\n\ts_cbranch_scc1 .Lab%=\n\ts_sub_i32 %1, %1, 1\n\ts_cmp_lt_i32 %1, 1\n\ts_cbranch_scc1 .Lab%=\n\ts_sleep 1\n\ts_branch .Las%=\n\t.subsection 0"
                         : "+&s"(avail), "+&s"(budget) : "s"(__builtin_amdgcn_readfirstlane(u)), "v"(a_landed) : "memory", "scc", "v140", "v141", "v142", "v143");
            const unsigned o = so; ++u; so = so == lds0 + (NSL - 1) * USLOT ? lds0 : so + USLOT; return o; };
        auto release = [&](int c) AINL { lds_put(a_done, (unsigned)c); };
        auto signoff = [&](int cnt_idx, char* flag) AINL { WAITV(0);
            unsigned old = 0; if (lane == 0) old = __hip_atomic_fetch_add((LAS unsigned*)(lds + L_CNT + 4 * cnt_idx), 1u, RLX_WG);
            if (__builtin_amdgcn_readfirstlane(old) == 3 && lane == 0) __hip_atomic_store((GAS u64*)flag, tag, RLX_AGENT); };
        auto opengate = [&](unsigned n) AINL { asm volatile("" ::: "memory"); if (lane == 0) __hip_atomic_fetch_add((LAS unsigned*)(lds + L_GATE), n, RLX_WG); };
        const unsigned krd = (unsigned)(256 * r32 + 16 * (hi ^ (r32 & 15)));
        bf16x8 qf[8], kf[8]; unsigned ka[8];
#define KRD(s, off) kf[s] = ldsrd16(LP(ka[s]) + (off))
#define KADDR(s, o) do { ka[s] = (krd ^ ((s) << 5)) + (o); PIN(ka[s]); } while (0)
#define KFIRST() do { const unsigned ko_ = acquire(); _Pragma("unroll") for (int s_ = 0; s_ < 8; ++s_) { KADDR(s_, ko_); KRD(s_, 0); } } while (0)
        const unsigned fx = (unsigned)((pair * 4 + rg) * 8192 + lane * 16);
        if (roleB) {
            const int p4s = ((p4 & 1) << 1) | (p4 >> 1);
            unsigned wrd[4], xa[4];
#pragma unroll
            for (int dt = 0; dt < 4; ++dt) wrd[dt] = (unsigned)((rg >> 1) * 256 + 512 * (8 * hi + q4) + 16 * ((4 * dt + 2 * blk + (p4s >> 1)) ^ (q4 << 2)) + 8 * (p4s & 1));
            { const int row = (rg & 1) * 32 + r32;
#pragma unroll
              for (int s = 0; s < 4; ++s) xa[s] = (unsigned)(row * 128 + 16 * ((2 * s + hi) ^ ((row >> 1) & 7))); }
            f32x16 acc[4] = {};
            bf16x8 xf[4], xn[4], wf[8];
#define WRD(i, wo) wf[i] = trrd2(LP((wo) + wrd[(i) & 3]) + ((i) >> 2) * 8192, LP((wo) + wrd[(i) & 3]) + ((i) >> 2) * 8192 + 2048)
#define XRD(s, xo) ldsrd16(LP((xo) + xa[s]))
#define QG(i, wi, wo, X_) do { acc[(i) & 3] = MFMA32(wf[i], xf[2 * (wi) + ((i) >> 2)], acc[(i) & 3]); PIN(acc[(i) & 3]); SBAR(); if ((i) == 0) BAR(); WRD(i, wo); X_; SBAR(); } while (0)
            unsigned sb = lds0;
            asm volatile("s_waitcnt lgkmcnt(0)\n\ts_barrier" ::: "memory");
#pragma unroll
            for (int s = 0; s < 4; ++s) xf[s] = XRD(s, sb);
#pragma unroll
            for (int i = 0; i < 8; ++i) WRD(i, sb + USLOT);
            LGKM(0);
#define QODDPH() do { const unsigned wo = sb + 2 * USLOT; SBAR(); \
                QG(0, 0, wo, (void)0); QG(1, 0, wo, (void)0); QG(2, 0, wo, (void)0); QG(3, 0, wo, (void)0); QG(4, 0, wo, (void)0); QG(5, 0, wo, (void)0); QG(6, 0, wo, (void)0); QG(7, 0, wo, (void)0); } while (0)
#pragma nounroll
            for (int gi = 0; gi < 8; ++gi) {
                QODDPH();
                sb = sb == lds0 + 6 * USLOT ? lds0 : sb + 3 * USLOT;
                SBAR();
                QG(0, 1, sb + USLOT, xn[0] = XRD(0, sb)); QG(1, 1, sb + USLOT, xn[1] = XRD(1, sb)); QG(2, 1, sb + USLOT, (void)0); QG(3, 1, sb + USLOT, (void)0);
                QG(4, 1, sb + USLOT, xn[2] = XRD(2, sb)); QG(5, 1, sb + USLOT, (void)0); QG(6, 1, sb + USLOT, (void)0); QG(7, 1, sb + USLOT, xn[3] = XRD(3, sb));
                xf[0] = xn[0]; xf[1] = xn[1]; xf[2] = xn[2]; xf[3] = xn[3];
            }
            QODDPH();
#pragma unroll
            for (int dt = 0; dt < 4; ++dt) { acc[dt] = MFMA32(wf[dt], xf[2], acc[dt]); acc[dt] = MFMA32(wf[4 + dt], xf[3], acc[dt]); }
            tag = next_tag(rfl64(epoch));
#pragma unroll
            for (int f = 0; f < 8; ++f) { const f32x16& a = acc[f >> 1]; const int r = 8 * (f & 1);
                const u32x4 w = {pk2(a[r] * QSCALE, a[r + 1] * QSCALE), pk2(a[r + 2] * QSCALE, a[r + 3] * QSCALE), pk2(a[r + 4] * QSCALE, a[r + 5] * QSCALE), pk2(a[r + 6] * QSCALE, a[r + 7] * QSCALE)};
                qf[f] = __builtin_bit_cast(bf16x8, w);
                __builtin_amdgcn_raw_buffer_store_b128(w, wsr, (unsigned)WS_QX + fx + f * 1024, 0, AUX_SC1); }
            opengate(1);
            signoff(0, ctl + 256); opengate(1);
            KFIRST();
        } else {
            tag = next_tag(rfl64(epoch));
            KFIRST();
            poll_flags(ctl + 256, tag, budget);
#pragma unroll
            for (int s = 0; s < 8; ++s) qf[s] = __builtin_bit_cast(bf16x8, __builtin_amdgcn_raw_buffer_load_b128(wsr, (unsigned)WS_QX + fx + s * 1024, 0, AUX_SC1));
            opengate(2);
        }
        f32x16 O0 = {}, O1 = {}, O2 = {}, O3 = {}, negm, SA, SB; float lsum = 0.f, mrow, ls0, ls1, mx0, mx1; u32x4 pw0, pw1, pk_; unsigned va[4]; s16x4 vh[16];
#define R_O0 "{v[192:207]}"
#define R_O1 "{v[208:223]}"
#define R_O2 "{v[224:239]}"
#define R_O3 "{v[240:255]}"
#define R_SA "{v[176:191]}"
#define R_SB "{v[160:175]}"
#define N_SA "176"
#define N_SB "160"
#define R_NM "{v[144:159]}"
#define PINR(x) asm volatile("" : "+" R_##x(x))
#define EXP2(x) __builtin_amdgcn_exp2f(x)
#define MX3(a, b, c) __builtin_fmaxf(__builtin_fmaxf((a), (b)), (c))
#define VRDH(g, t, off) vh[2 * (g) + (t)] = trrd(LP(va[(g) & 3]) + (off) + ((g) >> 2) * 4096 + (t) * 2048)
#define V2(g, vo) do { VRDH(g, 0, vo); VRDH(g, 1, vo); } while (0)
#define VF(g) __builtin_shufflevector(vh[2 * (g)], vh[2 * (g) + 1], 0, 1, 2, 3, 4, 5, 6, 7)
#define KSTEP(s, d) do { ka[s] += (d); PIN(ka[s]); } while (0)
#define VSTEP(c, d) do { va[c] += (d); PIN(va[c]); } while (0)
#define RESCALE(S, mx, mxb) do { float t0_; asm volatile( \
            "v_max_f32 %8, %8, %11\n\tv_cmp_lt_f32 vcc, %10, %8\n\ts_cbranch_vccnz .Lrs%=\n" \
            ".Lrb%=:\n\t.subsection 1\n" \
            ".Lrs%=:\n\ts_nop 15\n\tv_mov_b32 %9, %8\n\ts_nop 1\n\tv_permlane32_swap_b32 %8, %9\n\ts_nop 1\n\tv_max3_f32 %8, %8, %9, 0\n\t" \
            "v_add_f32 %6, %6, %8\n\tv_exp_f32 %9, -%8\n\t" \
            ".set r_, 0\n\t.rept 16\n\tv_sub_f32 v[" N_##S "+r_], v[" N_##S "+r_], %8\n\tv_sub_f32 v[144+r_], 0, %6\n\t.set r_, r_+1\n\t.endr\n\t" \
            "v_mul_f32 %7, %7, %9\n\t" \
            ".set r_, 192\n\t.rept 64\n\tv_mul_f32 v[r_], v[r_], %9\n\t.set r_, r_+1\n\t.endr\n\ts_nop 1\n\ts_branch .Lrb%=\n\t.subsection 0" \
            : "+" R_O0(O0), "+" R_O1(O1), "+" R_O2(O2), "+" R_O3(O3), "+" R_##S(S), "+" R_NM(negm), "+v"(mrow), "+v"(lsum), "+v"(mx), "=&v"(t0_) : "s"(THR), "v"(mxb) : "vcc"); } while (0)
#define G1(SN, SO, s, CIN, PK, KL) do { SN = MFMA32(kf[s], qf[s], CIN); PINR(SN); SBAR(); PINR(SO); KL; SO[2 * (s)] = EXP2(SO[2 * (s)]); SO[2 * (s) + 1] = EXP2(SO[2 * (s) + 1]); PK; PINR(SO); SBAR(); } while (0)
#define PKW(SO, W, i, r) do { W[i] = pk2(SO[r], SO[(r) + 1]); PIN(W); } while (0)
#define PH1(SN, SO, KL) do { SBAR(); \
        G1(SN, SO, 0, negm, (void)0, KL(0));          G1(SN, SO, 1, SN, PKW(SO, pw0, 0, 0), KL(1)); G1(SN, SO, 2, SN, PKW(SO, pw0, 1, 2), KL(2));  G1(SN, SO, 3, SN, PKW(SO, pw0, 2, 4), KL(3)); \
        G1(SN, SO, 4, SN, PKW(SO, pw0, 3, 6), KL(4)); G1(SN, SO, 5, SN, PKW(SO, pw1, 0, 8), KL(5)); G1(SN, SO, 6, SN, PKW(SO, pw1, 1, 10), KL(6)); G1(SN, SO, 7, SN, PKW(SO, pw1, 2, 12), KL(7)); } while (0)
#define G2(g, OC, W, X_) do { OC = MFMA32(VF(g), __builtin_bit_cast(bf16x8, W), OC); PINR(OC); SBAR(); X_; SBAR(); } while (0)
#define ADD4(SO, r) do { ls0 += SO[r]; ls1 += SO[(r) + 1]; ls0 += SO[(r) + 2]; ls1 += SO[(r) + 3]; PIN(ls0); PIN(ls1); } while (0)
#define PH2(SN, SO, vo, A0, A1, A2, A3) do { SBAR(); \
        G2(0, O0, pw0, { V2(0, vo); PKW(SO, pw1, 3, 14); ls0 = lsum + SO[0]; ls1 = SO[1] + SO[3]; ls0 += SO[2]; PIN(ls0); PIN(ls1); }); G2(1, O1, pw0, { V2(1, vo); ADD4(SO, 4); }); \
        G2(2, O2, pw0, { V2(2, vo); ADD4(SO, 8); }); G2(3, O3, pw0, { V2(3, vo); ADD4(SO, 12); }); \
        G2(4, O0, pw1, { V2(4, vo); mx0 = MX3(SN[0], SN[1], SN[2]); mx1 = MX3(SN[3], SN[4], SN[5]); lsum = ls0 + ls1; PIN(mx0); PIN(mx1); PIN(lsum); A0; }); \
        G2(5, O1, pw1, { V2(5, vo); mx0 = MX3(mx0, SN[6], SN[7]); mx1 = MX3(mx1, SN[8], SN[9]); PIN(mx0); PIN(mx1); A1; }); \
        G2(6, O2, pw1, { V2(6, vo); mx0 = MX3(mx0, SN[10], SN[11]); mx1 = MX3(mx1, SN[12], SN[13]); PIN(mx0); PIN(mx1); A2; }); \
        G2(7, O3, pw1, { V2(7, vo); mx0 = MX3(mx0, SN[14], SN[15]); A3; }); \
        RESCALE(SN, mx0, mx1); } while (0)
#define KL0(s) KRD(s, 0)
#define KL1(s) KRD(s, 8192)
#define KLN(s) (void)0
#define NOP_ (void)0
#define PEEK() do { pk_ = *(volatile LAS const u32x4*)LP(a_landed); } while (0)
#define SEEN() do { unsigned m_ = pk_[0] < pk_[1] ? pk_[0] : pk_[1], n_ = pk_[2] < pk_[3] ? pk_[2] : pk_[3]; const int a_ = __builtin_amdgcn_readfirstlane(m_ < n_ ? m_ : n_); avail = a_ > avail ? a_ : avail; } while (0)
        {
            SA = MFMA32(kf[0], qf[0], (f32x16){});
#pragma unroll
            for (int s = 1; s < 8; ++s) SA = MFMA32(kf[s], qf[s], SA);
#pragma unroll
            for (int s = 0; s < 8; ++s) KRD(s, 8192);
            float mx = MX3(SA[0], SA[1], SA[2]);
#pragma unroll
            for (int r = 3; r < 15; r += 2) mx = MX3(mx, SA[r], SA[r + 1]);
            mx = swap_max(__builtin_fmaxf(mx, SA[15])); mrow = mx;
#pragma unroll
            for (int r = 0; r < 16; ++r) { SA[r] -= mx; negm[r] = -mx; }
            asm volatile("" : "+" R_NM(negm));
        }
        unsigned vcur = acquire(), kcur = so;
#pragma unroll
        for (int c = 0; c < 4; ++c) { va[c] = (unsigned)(256 * (4 * hi + q4) + 16 * ((4 * c + 2 * blk + (p4 >> 1)) ^ (q4 << 2)) + 8 * (p4 & 1)) + vcur; PIN(va[c]); }
#pragma unroll
        for (int g = 0; g < 8; ++g) V2(g, 0);
#pragma unroll
        for (int s = 0; s < 8; ++s) KADDR(s, kcur);
        int tnext = (int)(unsigned)__builtin_amdgcn_s_memrealtime(), per = roleB ? PB0 : PA0;
#pragma nounroll
        for (int t = 0; t < NT - 1; ++t) {
            (void)acquire();
            if (roleB || t <= PEND) {
                int now = (int)(unsigned)__builtin_amdgcn_s_memrealtime();
#pragma nounroll
                while (now - tnext < 0) { __builtin_amdgcn_s_sleep(1); now = (int)(unsigned)__builtin_amdgcn_s_memrealtime(); }
                tnext = now + per; const int d_ = roleB ? PB1 : PA1, m_ = roleB ? PMINB : PMINA; per = per - d_ > m_ ? per - d_ : m_;
            }
            LGKM(15);
            PH1(SB, SA, KL0);
            LGKM(8);
            PH2(SB, SA, 8192, PEEK(), NOP_, NOP_, NOP_);
            LGKM(15);
            PH1(SA, SB, KL1);
            SEEN();
            release(u - 1);
            const unsigned vo = acquire();
            const unsigned dv = vo - vcur, dk = so - kcur; vcur = vo; kcur = so;
            VSTEP(0, dv); VSTEP(1, dv); VSTEP(2, dv); VSTEP(3, dv);
            LGKM(8);
            PH2(SA, SB, 0, { KSTEP(0, dk); KSTEP(1, dk); }, { KSTEP(2, dk); KSTEP(3, dk); }, { KSTEP(4, dk); KSTEP(5, dk); }, { KSTEP(6, dk); KSTEP(7, dk); });
        }
        u64 fpv = 0;
        if (!roleB) fpv = __hip_atomic_load((GAS const u64*)(ctl + 512), RLX_AGENT);
        PH1(SB, SA, KLN);
        release(u - 1);
        u32x4 pf[8]; float plse = 0.f; bool early = false;
#define PLOAD() do { _Pragma("unroll") for (int i = 0; i < 8; ++i) pf[i] = __builtin_amdgcn_raw_buffer_load_b128(wsr, (unsigned)WS_PX + fx + i * 1024, 0, AUX_SC1); \
            plse = __uint_as_float(__builtin_amdgcn_raw_buffer_load_b32(wsr, (unsigned)WS_PL + (pair * 4 + rg) * 256 + lane * 4, 0, AUX_SC1)); } while (0)
        if (!roleB && rfl64(fpv) == tag) { early = true; asm volatile("" ::: "memory"); PLOAD(); }
        PH2(SB, SA, 8192, NOP_, NOP_, NOP_, NOP_);
        {
#pragma unroll
            for (int r = 0; r < 16; ++r) { SB[r] = EXP2(SB[r]); lsum += SB[r]; }
            pw0 = (u32x4){pk2(SB[0], SB[1]), pk2(SB[2], SB[3]), pk2(SB[4], SB[5]), pk2(SB[6], SB[7])};
            pw1 = (u32x4){pk2(SB[8], SB[9]), pk2(SB[10], SB[11]), pk2(SB[12], SB[13]), pk2(SB[14], SB[15])};
            const bf16x8 p0 = __builtin_bit_cast(bf16x8, pw0), p1 = __builtin_bit_cast(bf16x8, pw1);
            O0 = MFMA32(VF(0), p0, O0); O1 = MFMA32(VF(1), p0, O1); O2 = MFMA32(VF(2), p0, O2); O3 = MFMA32(VF(3), p0, O3);
            O0 = MFMA32(VF(4), p1, O0); O1 = MFMA32(VF(5), p1, O1); O2 = MFMA32(VF(6), p1, O2); O3 = MFMA32(VF(7), p1, O3);
        }
        release(u);
        lsum = swap_add(lsum);
        const float lse = mrow + __builtin_amdgcn_logf(lsum);
        if (!roleB) {
            if (!early) { poll_flags(ctl + 512, tag, budget); PLOAD(); }
            const float M = __builtin_fmaxf(lse, plse), eo = EXP2(lse - M), ep = EXP2(plse - M), r = __builtin_amdgcn_rcpf(eo + ep);
            const float fo = eo * r * __builtin_amdgcn_rcpf(lsum), fp = ep * r;
#pragma unroll
            for (int f = 0; f < 8; ++f) { const f32x16& a = f < 2 ? O0 : f < 4 ? O1 : f < 6 ? O2 : O3; const int q = 8 * (f & 1); u32x4 w;
#pragma unroll
                for (int e = 0; e < 4; ++e) w[e] = pk2(__builtin_fmaf(a[q + 2 * e], fo, fp * bflo(pf[f][e])), __builtin_fmaf(a[q + 2 * e + 1], fo, fp * bfhi(pf[f][e])));
                __builtin_amdgcn_raw_buffer_store_b128(w, wsr, (unsigned)WS_OX + fx + f * 1024, 0, AUX_SC1); }
        } else {
            const float inv = __builtin_amdgcn_rcpf(lsum);
#pragma unroll
            for (int f = 0; f < 8; ++f) { const f32x16& a = f < 2 ? O0 : f < 4 ? O1 : f < 6 ? O2 : O3; const int r = 8 * (f & 1);
                const u32x4 w = {pk2(a[r] * inv, a[r + 1] * inv), pk2(a[r + 2] * inv, a[r + 3] * inv), pk2(a[r + 4] * inv, a[r + 5] * inv), pk2(a[r + 6] * inv, a[r + 7] * inv)};
                __builtin_amdgcn_raw_buffer_store_b128(w, wsr, (unsigned)WS_PX + fx + f * 1024, 0, AUX_SC1); }
            __builtin_amdgcn_raw_buffer_store_b32(__float_as_uint(lse), wsr, (unsigned)WS_PL + (pair * 4 + rg) * 256 + lane * 4, 0, AUX_SC1);
        }
        signoff(1, roleB ? ctl + 512 : ctl + 768);
        if (roleB) opengate(1);
        const int qh = rg & 1, nh = rg >> 1;
        *(volatile LAS u64*)(lds + L_TAG) = tag;
        u += NSL - 1; (void)acquire();
        unsigned wa[4], wh = lds0 + 131072 + (nh * 64 + hi) * 128 + 32 * q4 + 128 * blk + 8 * p4; PIN(wh);
#pragma unroll
        for (int c = 0; c < 4; ++c) { wa[c] = lds0 + ((((nh ^ (c >> 1)) * 64 + hi) * 4 + q4) * 256 + 64 * ((2 * nh + (c & 1)) ^ q4) + 32 * blk + 8 * p4); PIN(wa[c]); }
#define AKS(i) ((i) < 48 ? (i) / 3 : 16 + ((i) - 48) / 2)
#define ATL(i) ((i) < 48 ? (i) % 3 : ((i) - 48) % 2)
#define AWA(i) wa[((i) < 48 ? 0 : 2) + (ATL(i) & 1)]
#define ARD(i) (ATL(i) == 2 ? trrd2(LP(wh) + AKS(i) * 512, LP(wh) + AKS(i) * 512 + 256) : trrd2(LP(AWA(i)) + (AKS(i) & 15) * 4096, LP(AWA(i)) + (AKS(i) & 15) * 4096 + 2048))
        constexpr int AD = 7, OPRE = 10;
        bf16x8 af[AD], of[32];
#pragma unroll
        for (int i = 0; i < AD; ++i) af[i] = ARD(i);
        const int pr0 = b * HKV + ks * 2 + nh;
        poll_flags(ws + WS_CT + (size_t)(pr0 ^ (lane & 1)) * 1024 + 768, tag, budget);
        const unsigned ob0 = (unsigned)WS_OX + (pr0 * 4 + qh) * 8192 + lane * 16, ob1 = (unsigned)WS_OX + ((pr0 ^ 1) * 4 + qh) * 8192 + lane * 16;
#define OLD(f) of[f] = __builtin_bit_cast(bf16x8, __builtin_amdgcn_raw_buffer_load_b128(wsr, ((f) < 16 ? ob0 : ob1) + ((f) & 3) * 1024, (((f) >> 3) & 1) * 16384 + (((f) >> 2) & 1) * 4096, AUX_SC1))
#pragma unroll
        for (int f = 0; f < OPRE; ++f) OLD(f);
        f32x16 acc[3] = {};
        SBAR();
#pragma unroll
        for (int i = 0; i < 80; ++i) { acc[ATL(i)] = MFMA32(af[i % AD], of[AKS(i)], acc[ATL(i)]); PIN(acc[ATL(i)]); SBAR(); if (i + AD < 80) af[i % AD] = ARD(i + AD);
            if ((i & 1) == 0 && OPRE + i / 2 < 32) OLD(OPRE + i / 2); SBAR(); }
        const unsigned sb = (unsigned)WS_SLAB + ((b * 4 + nq) * 8 + (r32 >> 2)) * 20480 + ks * 2560 + ((lane & 3) * 2 + hi) * 16;
#pragma unroll
        for (int k = 0; k < 5; ++k) { const int kk = k == 0 ? 4 : k - 1; const f32x16& a = acc[kk >> 1]; const int r = 8 * (kk & 1);
            const u32x4 v = {pk2(a[r], a[r + 1]), pk2(a[r + 2], a[r + 3]), pk2(a[r + 4], a[r + 5]), pk2(a[r + 6], a[r + 7])};
            __builtin_amdgcn_raw_buffer_store_b128(v, wsr, sb + (kk < 4 ? (qh * 8 + nh * 4 + kk) * 128 : 2048 + (nh * 2 + qh) * 128), 0, AUX_SC1); }
        signoff(2, fsl + ks * 256);
        tagf = tag;
    }
    {
        poll_flags(fsl + (lane & 7) * 256, tagf, budget);
        const int e = wid * 32 + r32;
        const unsigned sb = (unsigned)WS_SLAB + ((b * 4 + nq) * 8 + ks) * 20480 + hi * 10240 + e * 16;
        u32x4 v[4];
#pragma unroll
        for (int k = 0; k < 4; ++k) v[k] = __builtin_amdgcn_raw_buffer_load_b128(wsr, sb + k * 2560, 0, AUX_SC1);
        if (ks == 0 && nq == 0 && tid == 0) __hip_atomic_store((GAS u64*)epo, tagf, RLX_AGENT);
        float s[8], t[4];
#pragma unroll
        for (int c = 0; c < 4; ++c) { s[2 * c] = (bflo(v[0][c]) + bflo(v[1][c])) + (bflo(v[2][c]) + bflo(v[3][c])); s[2 * c + 1] = (bfhi(v[0][c]) + bfhi(v[1][c])) + (bfhi(v[2][c]) + bfhi(v[3][c])); }
#pragma unroll
        for (int c = 0; c < 4; ++c) { auto rr = __builtin_amdgcn_permlane32_swap(__float_as_uint(s[c]), __float_as_uint(s[4 + c]), false, false); t[c] = __uint_as_float(rr[0]) + __uint_as_float(rr[1]); }
        int qh = e >> 6, col = nq * 128 + ((e >> 3) & 7) * 16;
        if (wid == 4) {
#pragma unroll
            for (int c = 0; c < 4; ++c) t[c] += __shfl_xor(t[c], 16);
            qh = (e >> 3) & 1; col = 512 + nq * 16; }
        if (e < 144) *(u32x2*)(out + ((size_t)(b * SQ + qh * 32 + ks * 4 + ((e >> 1) & 3)) * DM + col + hi * 8 + (e & 1) * 4) * 2) = (u32x2){pk2(t[0], t[1]), pk2(t[2], t[3])};
    }
    if ((GSEL >> ks) & 1) while ((int)(unsigned)__builtin_amdgcn_s_memrealtime() - tstart_ < 5500) __builtin_amdgcn_s_sleep(1);
}

extern "C" void kernel_launch(void* const* d_in, const int* in_sizes, int n_in, void* d_out, int out_size, void* d_ws, size_t ws_size, hipStream_t stream) {
    (void)hipFuncSetAttribute((const void*)gqa_block_k, hipFuncAttributeMaxDynamicSharedMemorySize, LDS_BYTES);
    hipLaunchKernelGGL(gqa_block_k, dim3(256), dim3(512), LDS_BYTES, stream, (const char*)d_in[0], (const char*)d_in[1], (const char*)d_in[2], (const char*)d_in[3],
                       (const char*)d_in[4], (char*)d_out, (char*)d_ws);
}
```

```cpp
#define ONEPOLL 0
#define PSL 4
#define PEND 13
#include <hip/hip_runtime.h>
#include <hip/hip_bf16.h>
#include <stdint.h>

typedef short bf16x8 __attribute__((ext_vector_type(8)));
typedef short s16x4 __attribute__((ext_vector_type(4)));
typedef float f32x16 __attribute__((ext_vector_type(16)));
typedef float f32x2 __attribute__((ext_vector_type(2)));
typedef unsigned u32x4 __attribute__((ext_vector_type(4)));
typedef unsigned u32x2 __attribute__((ext_vector_type(2)));
typedef __bf16 bf2_t __attribute__((ext_vector_type(2)));
typedef unsigned long long u64;
#define LAS __attribute__((address_space(3)))
#define GAS __attribute__((address_space(1)))
typedef LAS const char* lcptr;
typedef LAS char* lptr;
#define DI __device__ __forceinline__
#define AINL __attribute__((always_inline))
#define MFMA32(a, b, c) __builtin_amdgcn_mfma_f32_32x32x16_bf16((a), (b), (c), 0, 0, 0)
#define WAITV(N) asm volatile("s_waitcnt vmcnt(" #N ")" ::: "memory")
#define BAR() asm volatile("s_barrier" ::: "memory")
#define PIN(x) asm volatile("" : "+v"(x))
#define SBAR() __builtin_amdgcn_sched_barrier(0)
#define LGKM(n) do { __builtin_amdgcn_s_waitcnt(0xC07F | ((n) << 8)); SBAR(); } while (0)
#define LP(a) ((lcptr)(uintptr_t)(a))
#define STR2(x) #x
#define STR(x) STR2(x)
#define CLOB_V38_255 "v38", "v39", "v40", "v41", "v42", "v43", "v44", "v45", "v46", "v47", "v48", "v49", "v50", "v51", "v52", "v53", "v54", "v55", "v56", "v57", "v58", "v59", "v60", "v61", "v62", "v63", "v64", "v65", "v66", "v67", "v68", "v69", "v70", "v71", "v72", "v73", "v74", "v75", "v76", "v77", "v78", "v79", "v80", "v81", "v82", "v83", "v84", "v85", "v86", "v87", "v88", "v89", "v90", "v91", "v92", "v93", "v94", "v95", "v96", "v97", "v98", "v99", "v100", "v101", "v102", "v103", "v104", "v105", "v106", "v107", "v108", "v109", "v110", "v111", "v112", "v113", "v114", "v115", "v116", "v117", "v118", "v119", "v120", "v121", "v122", "v123", "v124", "v125", "v126", "v127", "v128", "v129", "v130", "v131", "v132", "v133", "v134", "v135", "v136", "v137", "v138", "v139", "v140", "v141", "v142", "v143", "v144", "v145", "v146", "v147", "v148", "v149", "v150", "v151", "v152", "v153", "v154", "v155", "v156", "v157", "v158", "v159", "v160", "v161", "v162", "v163", "v164", "v165", "v166", "v167", "v168", "v169", "v170", "v171", "v172", "v173", "v174", "v175", "v176", "v177", "v178", "v179", "v180", "v181", "v182", "v183", "v184", "v185", "v186", "v187", "v188", "v189", "v190", "v191", "v192", "v193", "v194", "v195", "v196", "v197", "v198", "v199", "v200", "v201", "v202", "v203", "v204", "v205", "v206", "v207", "v208", "v209", "v210", "v211", "v212", "v213", "v214", "v215", "v216", "v217", "v218", "v219", "v220", "v221", "v222", "v223", "v224", "v225", "v226", "v227", "v228", "v229", "v230", "v231", "v232", "v233", "v234", "v235", "v236", "v237", "v238", "v239", "v240", "v241", "v242", "v243", "v244", "v245", "v246", "v247", "v248", "v249", "v250", "v251", "v252", "v253", "v254", "v255"
#define RLX_AGENT __ATOMIC_RELAXED, __HIP_MEMORY_SCOPE_AGENT
#define RLX_WG __ATOMIC_RELAXED, __HIP_MEMORY_SCOPE_WORKGROUP
#define AUX_SC1 16

#define NTA 18
#define GATEA 10
#define HPRE 2
#define PA0 110
#define PA1 2
#define PMINA 103
#define PB0 150
#define PB1 9
#define PMINB 103

constexpr int SQ = 64, DM = 576, HKV = 16, SK = 2048;
constexpr float QSCALE = 0.08838834764831845f * 1.4426950408889634f;
constexpr float THR = 8.f;
constexpr int NT_A = NTA, NT_B = 32 - NTA;
constexpr int USLOT = 16384, NSL = 9, MISC = NSL * USLOT, LDS_BYTES = 163840;
constexpr int L_LANDED = MISC, L_DONE = MISC + 16, L_CNT = MISC + 32, L_GATE = MISC + 44, L_SEEN = MISC + 48, L_TAG = MISC + 64;
constexpr int SPIN_CAP = 1 << 20;
constexpr size_t WS_CT = 0;
constexpr size_t WS_EP = 128 * 1024, WS_FS = WS_EP + 8 * 256;
constexpr size_t WS_QX = 1 << 20;
constexpr size_t WS_PX = WS_QX + ((size_t)4 << 20);
constexpr size_t WS_PL = WS_PX + ((size_t)4 << 20);
constexpr size_t WS_OX = WS_PL + ((size_t)1 << 20);
constexpr size_t WS_SLAB = WS_OX + ((size_t)4 << 20);
constexpr int WS_USED = 32 << 20;

DI unsigned pk2(float a, float b) { bf2_t r = __builtin_convertvector((f32x2){a, b}, bf2_t); return __builtin_bit_cast(unsigned, r); }
DI float bflo(unsigned v) { return __uint_as_float(v << 16); }
DI float bfhi(unsigned v) { return __uint_as_float(v & 0xffff0000u); }
DI s16x4 trrd(lcptr p) { typedef short v4i16_t __attribute__((ext_vector_type(4))); return __builtin_bit_cast(s16x4, __builtin_amdgcn_ds_read_tr16_b64_v4i16((LAS v4i16_t*)p)); }
DI bf16x8 trrd2(lcptr p0, lcptr p1) { s16x4 a = trrd(p0), b = trrd(p1); return __builtin_shufflevector(a, b, 0, 1, 2, 3, 4, 5, 6, 7); }
DI bf16x8 ldsrd16(lcptr p) { return *(LAS const bf16x8*)p; }
DI float swap_max(float m) { auto rr = __builtin_amdgcn_permlane32_swap(__float_as_uint(m), __float_as_uint(m), false, false); return __builtin_fmaxf(__uint_as_float(rr[0]), __uint_as_float(rr[1])); }
DI float swap_add(float m) { auto rr = __builtin_amdgcn_permlane32_swap(__float_as_uint(m), __float_as_uint(m), false, false); return __uint_as_float(rr[0]) + __uint_as_float(rr[1]); }
DI u64 next_tag(u64 e) { return e * 6364136223846793005ull + 1442695040888963407ull; }
DI u64 rfl64(u64 v) { return ((u64)(unsigned)__builtin_amdgcn_readfirstlane((unsigned)(v >> 32)) << 32) | (unsigned)__builtin_amdgcn_readfirstlane((unsigned)v); }
DI void lds_put(unsigned a, unsigned val) { asm volatile("ds_write_b32 %0, %1" :: "v"(a), "v"(val) : "memory"); }
DI void poll_flags(const char* p, u64 tag, int& budget) {
#pragma nounroll
    while (!__all(__hip_atomic_load((GAS const u64*)p, RLX_AGENT) == tag) && --budget > 0) { if (PSL) __builtin_amdgcn_s_sleep(PSL); }
    asm volatile("" ::: "memory"); }
DI void wait_lds_fast(lcptr p, unsigned need, int& budget) {
#pragma nounroll
    while ((unsigned)__builtin_amdgcn_readfirstlane(*(volatile LAS const unsigned*)p) < need && --budget > 0) {}
    asm volatile("" ::: "memory"); }
#define POLL1(ptr, tg, k) do { if (ONEPOLL) { if (wid == 0) { poll_flags(ptr, tg, budget); *(volatile LAS unsigned*)(lds + L_SEEN) = (k); } else wait_lds_fast(lds + L_SEEN, (k), budget); } else poll_flags(ptr, tg, budget); } while (0)
DI void wait_lds(lcptr p, unsigned need, int& budget) {
#pragma nounroll
    while ((unsigned)__builtin_amdgcn_readfirstlane(*(volatile LAS const unsigned*)p) < need && --budget > 0) __builtin_amdgcn_s_sleep(1); }

#define DMA4(POL, sbase, v0, v1, v2, v3, ldst) do { unsigned sv_; asm volatile("s_mov_b32 %0, m0\n\ts_mov_b32 m0, %6\n\ts_nop 0\n\t" \
    "global_load_lds_dwordx4 %1, %5" POL "\n\tglobal_load_lds_dwordx4 %2, %5 offset:1024" POL "\n\tglobal_load_lds_dwordx4 %3, %5 offset:2048" POL "\n\tglobal_load_lds_dwordx4 %4, %5 offset:3072" POL "\n\t" \
    "s_mov_b32 m0, %0" : "=&s"(sv_) : "v"(v0), "v"(v1), "v"(v2), "v"(v3), "s"(sbase), "s"(__builtin_amdgcn_readfirstlane(ldst)) : "memory"); } while (0)

__global__ __launch_bounds__(512, 2) void gqa_block_k(const char* __restrict__ X, const char* __restrict__ Kc, const char* __restrict__ Vc, const char* __restrict__ Wq,
                                                      const char* __restrict__ Wo, char* __restrict__ out, char* __restrict__ ws) {
    extern __shared__ __attribute__((aligned(16))) char lds_[];
    const int tid = threadIdx.x, lane = tid & 63, r32 = lane & 31, hi = lane >> 5;
    const int wid = __builtin_amdgcn_readfirstlane(tid >> 6);
    const int bid = blockIdx.x, ks = bid & 7, idx = bid >> 3;
    const int kvh = ks * 2 + (idx & 1), b = (idx >> 1) & 7;
    const bool roleB = idx < 16;
    const int pair = b * HKV + kvh;
    const int nq = (idx & 1) + (roleB ? 0 : 2);
    const lptr lds = (lptr)lds_;
    const unsigned lds0 = (unsigned)(uintptr_t)lds;
    const int NT = roleB ? NT_B : NT_A;
    char* const ctl = ws + WS_CT + (size_t)pair * 1024;
    char* const epo = ws + WS_EP + b * 256;
    char* const fsl = ws + WS_FS + (size_t)(b * 4 + nq) * 8 * 256;
    const __amdgpu_buffer_rsrc_t wsr = __builtin_amdgcn_make_buffer_rsrc(ws, 0, WS_USED, 0x00020000);
    u64 pc0; unsigned code_len;
    asm volatile("s_getpc_b64 %0\n.Lcs%=:\n\ts_mov_b32 %1, .Lce%=-.Lcs%=\n\t.subsection 2\n.Lce%=:\n\t.fill 128, 4, 0xBF800000\n\t.subsection 0" : "=s"(pc0), "=s"(code_len));
    if (tid < 16) *(LAS unsigned*)(lds + MISC + tid * 4) = 0u;
    int budget = SPIN_CAP;
    u64 tagf;

    if (wid >= 4) {
        const int j = wid - 4, lr = lane >> 4, lc = lane & 15;
        const size_t koff = ((size_t)pair * SK + (roleB ? NT_A * 64 : 0)) * 256;
        const char* kp = Kc + koff; const char* vp = Vc + koff;
        unsigned vK[4];
#pragma unroll
        for (int i = 0; i < 4; ++i) vK[i] = (unsigned)((16 * j + lr) * 256 + 16 * (lc ^ (4 * i + lr)));
        const unsigned vV = (unsigned)((16 * j + lr) * 256 + 16 * (lc ^ (lr << 2)));
        unsigned a_landed = lds0 + L_LANDED + 4 * j, a_done = lds0 + L_DONE; PIN(a_landed); PIN(a_done);
        if (roleB) {
            unsigned vX[2], vW[4];
#pragma unroll
            for (int i = 0; i < 2; ++i) { const int row = 16 * j + 8 * i + (lane >> 3); vX[i] = (unsigned)(row * 1152 + 16 * ((lane & 7) ^ ((row >> 1) & 7)) - 1024 * i); }
#pragma unroll
            for (int i = 0; i < 4; ++i) vW[i] = (unsigned)((8 * j + 2 * i + hi) * 8192 + 16 * (r32 ^ (((2 * i + hi) & 3) << 2)) - 1024 * i);
            const char* xb = X + (size_t)b * SQ * DM * 2; const char* wb = Wq + kvh * 512;
            int xo = b * 128, sb = 0, cnt = 7;
#define QADV "s_add_i32 %[xo], %[xo], 128\n\ts_cmp_eq_u32 %[xo], 1152\n\ts_cselect_b32 %[xo], 0, %[xo]\n\ts_add_i32 %[sb], %[sb], 0xc000\n\ts_cmp_eq_u32 %[sb], 0x24000\n\ts_cselect_b32 %[sb], 0, %[sb]\n\t"
#define QW4 "global_load_lds_dwordx4 %[w0], s[60:61]\n\tglobal_load_lds_dwordx4 %[w1], s[60:61] offset:1024\n\tglobal_load_lds_dwordx4 %[w2], s[60:61] offset:2048\n\tglobal_load_lds_dwordx4 %[w3], s[60:61] offset:3072\n\t"
#define QEVEN0 "s_add_u32 s62, %[xl], %[xo]\n\ts_addc_u32 s63, %[xh], 0\n\ts_lshl_b32 s66, %[xo], 12\n\ts_add_u32 s60, %[wl], s66\n\ts_addc_u32 s61, %[wh], 0\n\ts_add_i32 m0, %[sb], %[dx]\n\ts_add_i32 s66, %[sb], %[dw]\n\t"
#define QEVEN1 "global_load_lds_dwordx4 %[x0], s[62:63]\n\tglobal_load_lds_dwordx4 %[x1], s[62:63] offset:1024\n\ts_mov_b32 m0, s66\n\ts_nop 0\n\t" QW4
#define QODD0 "s_add_u32 s60, s60, 0x40000\n\ts_addc_u32 s61, s61, 0\n\ts_add_i32 m0, m0, 0x4000\n\t"
            asm volatile(
                "s_mov_b32 s70, m0\n\t"
                QEVEN0 QEVEN1 QODD0 "s_nop 0\n\t" QW4 QADV QEVEN0 QEVEN1 QODD0
                "s_waitcnt vmcnt(10)\n"
                ".Lqr%=:\n\t"
                "s_barrier\n\t" QW4 QADV QEVEN0 "s_waitcnt vmcnt(10)\n\t"
                "s_barrier\n\t" QEVEN1 QODD0 "s_sub_i32 %[cnt], %[cnt], 1\n\ts_cmp_gt_i32 %[cnt], 0\n\ts_waitcnt vmcnt(10)\n\ts_cbranch_scc1 .Lqr%=\n\t"
                "s_barrier\n\t" QW4 "s_waitcnt vmcnt(10)\n\t"
                "s_barrier\n\ts_waitcnt vmcnt(4)\n\t"
                "s_barrier\n\ts_waitcnt vmcnt(0)\n\t"
                "s_barrier\n\ts_mov_b32 m0, s70"
                : [xo] "+&s"(xo), [sb] "+&s"(sb), [cnt] "+&s"(cnt)
                : [xl] "s"((unsigned)(uintptr_t)xb), [xh] "s"((unsigned)((uintptr_t)xb >> 32)), [wl] "s"((unsigned)(uintptr_t)wb), [wh] "s"((unsigned)((uintptr_t)wb >> 32)),
                  [dx] "s"(lds0 + j * 2048), [dw] "s"(lds0 + USLOT + j * 4096),
                  [x0] "v"(vX[0]), [x1] "v"(vX[1]), [w0] "v"(vW[0]), [w1] "v"(vW[1]), [w2] "v"(vW[2]), [w3] "v"(vW[3])
                : "memory", "scc", "s60", "s61", "s62", "s63", "s66", "s70");
        } else {
            DMA4(" nt", kp, vK[0], vK[1], vK[2], vK[3], lds0 + j * 4096); DMA4(" nt", vp, vV, vV, vV, vV, lds0 + USLOT + j * 4096);
            BAR();
        }
        const char* wb = Wo + (size_t)ks * 512 * 1152;
        unsigned oF[4], oH[4];
#pragma unroll
        for (int i = 0; i < 4; ++i) { oF[i] = (unsigned)(((4 * j + i) * 4 + lr) * 1152 + nq * 256 + 16 * (lc ^ (lr << 2)) - 1024 * i);
            oH[i] = (unsigned)((j * 128 + i * 32 + (lane >> 1)) * 1152 + 1024 + nq * 32 + (lane & 1) * 16 - 1024 * i); }
        const unsigned wl = (unsigned)(lds0 + j * 4096 + lane * 16), a_gate = lds0 + L_GATE;
        const int nkv = 2 * NT, ntot = roleB ? nkv : nkv + NSL, n0 = roleB ? 0 : 2;
        int ni = n0, nl = 0, nw = 0, bi = 0, bw = 0, gate = roleB ? 0 : GATEA, si = n0 * USLOT, sw = 0, lim = NSL, wc = roleB ? 9 : 0;
        static_assert(GATEA >= NSL && GATEA <= NSL + 4, "four register buffers");
#define WQUOTA(L) "s_sub_i32 s67, %[ni], " STR(GATEA) "\n\ts_lshr_b32 s67, s67, 1\n\ts_add_i32 s67, s67, 3\n\ts_cmp_ge_i32 %[wc], s67\n\ts_cbranch_scc1 " L "\n\t"
        asm volatile(
            "s_mov_b32 s70, m0\n\ts_mov_b64 s[72:73], 0\n\ts_mov_b32 s75, 0\n\ts_getpc_b64 s[64:65]\n"
            ".Lp%=:\n\t"
            "s_branch .Lprep%=\n"
            ".Ltop%=:\n\t"
            "s_sub_i32 s66, %[ni], %[nl]\n\ts_cmp_lt_i32 s66, 3\n\ts_cbranch_scc1 .Lc1%=\n\t"
            "s_waitcnt vmcnt(8)\n\ts_add_i32 %[nl], %[nl], 1\n\t"
            ".Lc1%=:\n\t"
            "s_waitcnt lgkmcnt(0)\n\tv_min_u32 v42, v42, v43\n\tv_min3_u32 v40, v40, v41, v42\n\ts_mov_b32 s71, 0\n\ts_cmp_lt_i32 %[nw], %[nl]\n\tv_readfirstlane_b32 s67, v40\n\t"
            "s_cbranch_scc0 .Lc2%=\n\t"
            "s_bitcmp1_b64 s[72:73], %[nw]\n\ts_cbranch_scc1 .Lwr%=\n"
            ".Lpb%=:\n\t"
            "s_add_i32 %[nw], %[nw], 1\n\tv_mov_b32 v38, %[nw]\n\tds_write_b32 %[aland], v38\n\t"
            "s_add_i32 %[sw], %[sw], 0x4000\n\ts_cmp_eq_u32 %[sw], 0x24000\n\ts_cselect_b32 %[sw], 0, %[sw]\n\ts_mov_b32 s71, 1\n\t"
            "s_cmp_lt_i32 %[nw], %[nl]\n\ts_cbranch_scc1 .Lmore%=\n"
            ".Lc2%=:\n\t"
            "s_add_i32 s67, s67, 9\n\ts_max_i32 %[lim], %[lim], s67\n\t"
            "s_cmp_ge_i32 %[ni], %[gate]\n\ts_cbranch_scc1 .Lno%=\n\t"
            "s_cmp_lt_i32 %[ni], %[lim]\n\ts_cbranch_scc0 .Lis%=\n\t"
            "global_load_lds_dwordx4 v44, s[60:61] nt\n\tglobal_load_lds_dwordx4 v45, s[60:61] offset:1024 nt\n\t"
            "global_load_lds_dwordx4 v46, s[60:61] offset:2048 nt\n\tglobal_load_lds_dwordx4 v47, s[60:61] offset:3072 nt\n"
            ".Lbk%=:\n\t"
            "s_add_i32 %[ni], %[ni], 1\n\ts_mov_b32 s75, 1\n\t"
            "s_add_i32 %[si], %[si], 0x4000\n\ts_cmp_eq_u32 %[si], 0x24000\n\ts_cselect_b32 %[si], 0, %[si]\n"
            ".Lprep%=:\n\t"
            "ds_read_b128 v[40:43], %[adone]\n\t"
            "s_add_i32 m0, %[si], %[dbase]\n\t"
            "s_lshr_b32 s66, %[ni], 1\n\ts_lshl_b32 s66, s66, 14\n\t"
            "s_bitcmp1_b32 %[ni], 0\n\ts_cselect_b32 s60, %[vpl], %[kpl]\n\ts_cselect_b32 s61, %[vph], %[kph]\n\ts_cselect_b64 vcc, -1, 0\n\t"
            "s_add_u32 s60, s60, s66\n\ts_addc_u32 s61, s61, 0\n\t"
            "v_cndmask_b32 v44, %[k0], %[vv], vcc\n\tv_cndmask_b32 v45, %[k1], %[vv], vcc\n\tv_cndmask_b32 v46, %[k2], %[vv], vcc\n\tv_cndmask_b32 v47, %[k3], %[vv], vcc\n\t"
            "s_branch .Ltop%=\n"
            ".Lmore%=:\n\ts_bitcmp1_b64 s[72:73], %[nw]\n\ts_cbranch_scc1 .Lwr%=\n\ts_branch .Lpb%=\n"
            ".Lno%=:\n\t"
            "s_cmp_ge_i32 %[gate], %[nkv]\n\ts_cbranch_scc1 .Lno2%=\n\t"
            "ds_read_b32 v40, %[agate]\n\ts_waitcnt lgkmcnt(0)\n\tv_readfirstlane_b32 s67, v40\n\ts_nop 3\n\t"
            "s_cmp_ge_u32 s67, 4\n\ts_cselect_b32 %[gate], %[g1], %[gate]\n\ts_cmp_ge_u32 s67, 8\n\ts_cselect_b32 %[gate], %[nkv], %[gate]\n\t"
            "s_cmp_lt_i32 %[ni], %[gate]\n\ts_cselect_b32 s71, 1, s71\n"
            ".Lno2%=:\n\t"
            "ds_read_b128 v[40:43], %[adone]\n\t"
            "s_cmp_lg_u32 s71, 0\n\ts_cbranch_scc1 .Ltop%=\n\t"
            "s_sub_i32 s66, %[ni], %[nl]\n\ts_cmp_eq_u32 s66, 0\n\ts_cbranch_scc1 .Lno3%=\n\t"
            "s_cmp_eq_u32 s66, 2\n\ts_cbranch_scc1 .Lw4%=\n\ts_waitcnt vmcnt(0)\n"
            ".Lw4%=:\n\ts_waitcnt vmcnt(4)\n\ts_add_i32 %[nl], %[nl], 1\n\ts_branch .Lc1%=\n"
            ".Lno3%=:\n\ts_cmp_ge_i32 %[nw], %[ntot]\n\ts_cbranch_scc1 .Lend%=\n\t"
            "s_cmp_lt_i32 %[gate], %[nkv]\n\ts_cbranch_scc1 .Lidle%=\n\t"
            "s_cmp_lt_i32 %[wc], 9\n\ts_cbranch_scc1 .Lwo0%=\n\t"
            "s_cmp_eq_u32 %[ni], %[nkv]\n\ts_cbranch_scc0 .Lidle%=\n\ts_cmp_eq_u32 %[ni], %[ntot]\n\ts_cbranch_scc1 .Lidle%=\n\t"
            "s_waitcnt vmcnt(0)\n\ts_lshl_b64 s[62:63], 0x1ff, %[nkv]\n\ts_or_b64 s[72:73], s[72:73], s[62:63]\n\ts_mov_b32 %[ni], %[ntot]\n\ts_mov_b32 %[nl], %[ntot]\n\ts_branch .Lc1%=\n"
            ".Lidle%=:\n\t"
            "s_sleep 1\n\ts_sub_i32 %[bud], %[bud], 1\n\ts_cmp_gt_i32 %[bud], 0\n\ts_cbranch_scc1 .Lc1%=\n\t"
            "s_mov_b32 %[lim], 0x3fffffff\n\ts_mov_b32 %[gate], %[nkv]\n\ts_branch .Lc1%=\n"
            ".Lrf%=:\n\t"
            "s_cmp_eq_u32 s75, 0\n\ts_cbranch_scc1 .Lno2%=\n\ts_cmp_ge_i32 %[wc], 9\n\ts_cbranch_scc1 .Lno2%=\n\t"
            WQUOTA(".Lno2%=")
            "s_mov_b32 s75, 0\n\tds_read_b128 v[40:43], %[adone]\n\ts_branch .Lwo%=\n"
            ".Lwo0%=:\n\t"
            WQUOTA(".Lidle%=")
            "s_waitcnt vmcnt(8)\n"
            ".Lwo%=:\n\t"
            "s_and_b32 s66, %[wc], 7\n\ts_mul_i32 s66, s66, 73728\n\ts_add_u32 s76, %[wpl], s66\n\ts_addc_u32 s77, %[wph], 0\n\t"
            "s_mul_i32 s66, %[wc], 36\n\ts_add_u32 s66, s66, .Lit2%=-.Lp%=\n\ts_add_u32 s62, s64, s66\n\ts_addc_u32 s63, s65, 0\n\t"
            "s_add_i32 %[wc], %[wc], 1\n\t"
            "s_setpc_b64 s[62:63]\n"
            ".Lit2%=:\n\t"
            ".set r_, 112\n\t.rept 8\n\t"
            "global_load_dwordx4 v[r_:r_+3], %[f0], s[76:77] nt\n\tglobal_load_dwordx4 v[r_+4:r_+7], %[f1], s[76:77] offset:1024 nt\n\t"
            "global_load_dwordx4 v[r_+8:r_+11], %[f2], s[76:77] offset:2048 nt\n\tglobal_load_dwordx4 v[r_+12:r_+15], %[f3], s[76:77] offset:3072 nt\n\ts_branch .Lc1%=\n\t"
            ".set r_, r_+16\n\t.endr\n\t"
            "global_load_dwordx4 v[240:243], %[h0], s[76:77] nt\n\tglobal_load_dwordx4 v[244:247], %[h1], s[76:77] offset:1024 nt\n\t"
            "global_load_dwordx4 v[248:251], %[h2], s[76:77] offset:2048 nt\n\tglobal_load_dwordx4 v[252:255], %[h3], s[76:77] offset:3072 nt\n\ts_branch .Lc1%=\n"
            ".Lis%=:\n\t"
            "s_cmp_ge_i32 %[ni], " STR(GATEA) "\n\ts_cbranch_scc1 .Lrf%=\n\t"
            "s_add_u32 s67, %[bi], .Lit%=-.Lp%=\n\ts_add_u32 s62, s64, s67\n\ts_addc_u32 s63, s65, 0\n\t"
            "s_bitset1_b64 s[72:73], %[ni]\n\ts_add_i32 %[bi], %[bi], 36\n\t"
            "s_setpc_b64 s[62:63]\n"
            ".Lit%=:\n\t"
            ".set r_, 48\n\t.rept 4\n\t"
            "global_load_dwordx4 v[r_:r_+3], v44, s[60:61] nt\n\tglobal_load_dwordx4 v[r_+4:r_+7], v45, s[60:61] offset:1024 nt\n\t"
            "global_load_dwordx4 v[r_+8:r_+11], v46, s[60:61] offset:2048 nt\n\tglobal_load_dwordx4 v[r_+12:r_+15], v47, s[60:61] offset:3072 nt\n\ts_branch .Lbk%=\n\t"
            ".set r_, r_+16\n\t.endr\n"
            ".Lwr%=:\n\t"
            "s_add_i32 s66, s67, 9\n\ts_max_i32 %[lim], %[lim], s66\n\t"
            "s_cmp_lt_i32 %[nw], %[lim]\n\ts_cbranch_scc0 .Lc2%=\n\t"
            "v_add_u32 v39, %[sw], %[wl]\n\t"
            "s_lshr_b32 s66, %[sw], 14\n\ts_mul_i32 s66, s66, 36\n\ts_add_i32 s66, s66, 144\n\ts_cmp_lt_i32 %[nw], %[nkv]\n\ts_cselect_b32 s66, %[bw], s66\n\t"
            "s_add_u32 s66, s66, .Lwt%=-.Lp%=\n\ts_add_u32 s62, s64, s66\n\ts_addc_u32 s63, s65, 0\n\t"
            "s_add_i32 %[bw], %[bw], 36\n\t"
            "s_setpc_b64 s[62:63]\n"
            ".Lwt%=:\n\t"
            ".set r_, 48\n\t.rept 13\n\t"
            "ds_write_b128 v39, v[r_:r_+3]\n\tds_write_b128 v39, v[r_+4:r_+7] offset:1024\n\tds_write_b128 v39, v[r_+8:r_+11] offset:2048\n\tds_write_b128 v39, v[r_+12:r_+15] offset:3072\n\ts_branch .Lpb%=\n\t"
            ".set r_, r_+16\n\t.endr\n"
            ".Lend%=:\n\ts_mov_b32 m0, s70\n\ts_waitcnt lgkmcnt(0)"
            : [ni] "+&s"(ni), [nl] "+&s"(nl), [nw] "+&s"(nw), [bi] "+&s"(bi), [bw] "+&s"(bw), [gate] "+&s"(gate), [si] "+&s"(si), [sw] "+&s"(sw), [lim] "+&s"(lim),
              [wc] "+&s"(wc), [bud] "+&s"(budget)
            : [nkv] "s"(nkv), [ntot] "s"(ntot), [g1] "s"(roleB ? HPRE : GATEA), [dbase] "s"(lds0 + j * 4096),
              [kpl] "s"((unsigned)(uintptr_t)kp), [kph] "s"((unsigned)((uintptr_t)kp >> 32)), [vpl] "s"((unsigned)(uintptr_t)vp), [vph] "s"((unsigned)((uintptr_t)vp >> 32)),
              [wpl] "s"((unsigned)(uintptr_t)wb), [wph] "s"((unsigned)((uintptr_t)wb >> 32)),
              [k0] "v"(vK[0]), [k1] "v"(vK[1]), [k2] "v"(vK[2]), [k3] "v"(vK[3]), [vv] "v"(vV), [f0] "v"(oF[0]), [f1] "v"(oF[1]), [f2] "v"(oF[2]), [f3] "v"(oF[3]),
              [h0] "v"(oH[0]), [h1] "v"(oH[1]), [h2] "v"(oH[2]), [h3] "v"(oH[3]),
              [wl] "v"(wl), [adone] "v"(a_done), [agate] "v"(a_gate), [aland] "v"(a_landed)
            : "memory", "scc", "vcc", "s60", "s61", "s62", "s63", "s64", "s65", "s66", "s67", "s70", "s71", "s72", "s73", "s75", "s76", "s77", CLOB_V38_255);
        if (roleB) {
            const unsigned li = (unsigned)((idx * 4 + j) * 40 + (lane < 40 ? lane : 0)), row = li / 5, c = li - row * 5; unsigned d;
            asm volatile("global_load_dword %0, %1, %2\n\ts_waitcnt vmcnt(0)" : "=&v"(d) : "v"(row * 1152 + (c < 4 ? c * 128 : 1024)), "s"(wb) : "memory");
            wait_lds(lds + L_GATE, 12, budget);
            DMA4("", wb, oH[0], oH[1], oH[2], oH[3], lds0 + 8 * USLOT + j * 4096);
#pragma nounroll
            for (int k = 0; k < 8; ++k) { const int p = (b + k) & 7; DMA4("", wb + (size_t)p * 73728, oF[0], oF[1], oF[2], oF[3], lds0 + p * USLOT + j * 4096); }
            WAITV(0); lds_put(a_landed, (unsigned)(nkv + NSL));
        }
        if (j != 0) return;
        wait_lds(lds + L_CNT + 8, 1, budget);
        tagf = rfl64(*(volatile LAS const u64*)(lds + L_TAG));
    } else {
        const int rg = wid;
        __builtin_amdgcn_s_setprio(2);
        if (!roleB) {
            unsigned d[5];
            const unsigned i0 = (idx - 16) * 288 + rg * 72 + lane, i1 = i0 - lane + 64 + (lane & 7);
            unsigned oc = (rg * 64 + lane) * 128; oc = oc < code_len && (idx & 3) == 0 ? oc : 0;
            asm volatile("global_load_dword %0, %5, %10 nt\n\tglobal_load_dword %1, %6, %11\n\tglobal_load_dword %2, %7, %10 nt\n\tglobal_load_dword %3, %8, %11\n\t"
                         "s_waitcnt lgkmcnt(0)\n\ts_barrier\n\tglobal_load_dword %4, %9, %12\n\ts_waitcnt vmcnt(0)"
                         : "=&v"(d[0]), "=&v"(d[1]), "=&v"(d[2]), "=&v"(d[3]), "=&v"(d[4])
                         : "v"((i0 >> 3) * 8192 + ks * 1024 + (i0 & 7) * 128), "v"(i0 * 128), "v"((i1 >> 3) * 8192 + ks * 1024 + (i1 & 7) * 128), "v"(i1 * 128), "v"(oc),
                           "s"(Wq), "s"(X), "s"(pc0) : "memory");
        }
        const u64 epoch = __hip_atomic_load((GAS const u64*)epo, RLX_AGENT);
        u64 tag;
        const int q4 = (lane & 15) >> 2, p4 = lane & 3, blk = (lane >> 4) & 1;
        int u = 0, avail = 0;
        unsigned so = lds0;
        unsigned a_landed = lds0 + L_LANDED, a_done = lds0 + L_DONE + 4 * rg; PIN(a_landed); PIN(a_done);
        auto acquire = [&]() AINL -> unsigned {
            avail = __builtin_amdgcn_readfirstlane(avail); budget = __builtin_amdgcn_readfirstlane(budget);
            asm volatile("s_cmp_gt_i32 %0, %2\n\ts_cbranch_scc0 .Las%=\n"
                         ".Lab%=:\n\t.subsection 1\n"
                         ".Las%=:\n\tds_read_b128 v[140:143], %3\n\ts_waitcnt lgkmcnt(0)\n\tv_min_u32 v142, v142, v143\n\tv_min3_u32 v140, v140, v141, v142\n\ts_nop 1\n\tv_readfirstlane_b32 %0, v140\n\ts_nop 3\n\t"
                         "s_cmp_gt_i32 %0, %2\n\ts_cbranch_scc1 .Lab%=\n\ts_sub_i32 %1, %1, 1\n\ts_cmp_lt_i32 %1, 1\n\ts_cbranch_scc1 .Lab%=\n\ts_sleep 1\n\ts_branch .Las%=\n\t.subsection 0"
                         : "+&s"(avail), "+&s"(budget) : "s"(__builtin_amdgcn_readfirstlane(u)), "v"(a_landed) : "memory", "scc", "v140", "v141", "v142", "v143");
            const unsigned o = so; ++u; so = so == lds0 + (NSL - 1) * USLOT ? lds0 : so + USLOT; return o; };
        auto release = [&](int c) AINL { lds_put(a_done, (unsigned)c); };
        auto signoff = [&](int cnt_idx, char* flag) AINL { WAITV(0);
            unsigned old = 0; if (lane == 0) old = __hip_atomic_fetch_add((LAS unsigned*)(lds + L_CNT + 4 * cnt_idx), 1u, RLX_WG);
            if (__builtin_amdgcn_readfirstlane(old) == 3 && lane == 0) __hip_atomic_store((GAS u64*)flag, tag, RLX_AGENT); };
        auto opengate = [&](unsigned n) AINL { asm volatile("" ::: "memory"); if (lane == 0) __hip_atomic_fetch_add((LAS unsigned*)(lds + L_GATE), n, RLX_WG); };
        const unsigned krd = (unsigned)(256 * r32 + 16 * (hi ^ (r32 & 15)));
        bf16x8 qf[8], kf[8]; unsigned ka[8];
#define KRD(s, off) kf[s] = ldsrd16(LP(ka[s]) + (off))
#define KADDR(s, o) do { ka[s] = (krd ^ ((s) << 5)) + (o); PIN(ka[s]); } while (0)
#define KFIRST() do { const unsigned ko_ = acquire(); _Pragma("unroll") for (int s_ = 0; s_ < 8; ++s_) { KADDR(s_, ko_); KRD(s_, 0); } } while (0)
        const unsigned fx = (unsigned)((pair * 4 + rg) * 8192 + lane * 16);
        if (roleB) {
            const int p4s = ((p4 & 1) << 1) | (p4 >> 1);
            unsigned wrd[4], xa[4];
#pragma unroll
            for (int dt = 0; dt < 4; ++dt) wrd[dt] = (unsigned)((rg >> 1) * 256 + 512 * (8 * hi + q4) + 16 * ((4 * dt + 2 * blk + (p4s >> 1)) ^ (q4 << 2)) + 8 * (p4s & 1));
            { const int row = (rg & 1) * 32 + r32;
#pragma unroll
              for (int s = 0; s < 4; ++s) xa[s] = (unsigned)(row * 128 + 16 * ((2 * s + hi) ^ ((row >> 1) & 7))); }
            f32x16 acc[4] = {};
            bf16x8 xf[4], xn[4], wf[8];
#define WRD(i, wo) wf[i] = trrd2(LP((wo) + wrd[(i) & 3]) + ((i) >> 2) * 8192, LP((wo) + wrd[(i) & 3]) + ((i) >> 2) * 8192 + 2048)
#define XRD(s, xo) ldsrd16(LP((xo) + xa[s]))
#define QG(i, wi, wo, X_) do { acc[(i) & 3] = MFMA32(wf[i], xf[2 * (wi) + ((i) >> 2)], acc[(i) & 3]); PIN(acc[(i) & 3]); SBAR(); if ((i) == 0) BAR(); WRD(i, wo); X_; SBAR(); } while (0)
            unsigned sb = lds0;
            asm volatile("s_waitcnt lgkmcnt(0)\n\ts_barrier" ::: "memory");
#pragma unroll
            for (int s = 0; s < 4; ++s) xf[s] = XRD(s, sb);
#pragma unroll
            for (int i = 0; i < 8; ++i) WRD(i, sb + USLOT);
            LGKM(0);
#define QODDPH() do { const unsigned wo = sb + 2 * USLOT; SBAR(); \
                QG(0, 0, wo, (void)0); QG(1, 0, wo, (void)0); QG(2, 0, wo, (void)0); QG(3, 0, wo, (void)0); QG(4, 0, wo, (void)0); QG(5, 0, wo, (void)0); QG(6, 0, wo, (void)0); QG(7, 0, wo, (void)0); } while (0)
#pragma nounroll
            for (int gi = 0; gi < 8; ++gi) {
                QODDPH();
                sb = sb == lds0 + 6 * USLOT ? lds0 : sb + 3 * USLOT;
                SBAR();
                QG(0, 1, sb + USLOT, xn[0] = XRD(0, sb)); QG(1, 1, sb + USLOT, xn[1] = XRD(1, sb)); QG(2, 1, sb + USLOT, (void)0); QG(3, 1, sb + USLOT, (void)0);
                QG(4, 1, sb + USLOT, xn[2] = XRD(2, sb)); QG(5, 1, sb + USLOT, (void)0); QG(6, 1, sb + USLOT, (void)0); QG(7, 1, sb + USLOT, xn[3] = XRD(3, sb));
                xf[0] = xn[0]; xf[1] = xn[1]; xf[2] = xn[2]; xf[3] = xn[3];
            }
            QODDPH();
#pragma unroll
            for (int dt = 0; dt < 4; ++dt) { acc[dt] = MFMA32(wf[dt], xf[2], acc[dt]); acc[dt] = MFMA32(wf[4 + dt], xf[3], acc[dt]); }
            tag = next_tag(rfl64(epoch));
#pragma unroll
            for (int f = 0; f < 8; ++f) { const f32x16& a = acc[f >> 1]; const int r = 8 * (f & 1);
                const u32x4 w = {pk2(a[r] * QSCALE, a[r + 1] * QSCALE), pk2(a[r + 2] * QSCALE, a[r + 3] * QSCALE), pk2(a[r + 4] * QSCALE, a[r + 5] * QSCALE), pk2(a[r + 6] * QSCALE, a[r + 7] * QSCALE)};
                qf[f] = __builtin_bit_cast(bf16x8, w);
                __builtin_amdgcn_raw_buffer_store_b128(w, wsr, (unsigned)WS_QX + fx + f * 1024, 0, AUX_SC1); }
            opengate(1);
            signoff(0, ctl + 256); opengate(1);
            KFIRST();
        } else {
            tag = next_tag(rfl64(epoch));
            KFIRST();
            POLL1(ctl + 256, tag, 1);
#pragma unroll
            for (int s = 0; s < 8; ++s) qf[s] = __builtin_bit_cast(bf16x8, __builtin_amdgcn_raw_buffer_load_b128(wsr, (unsigned)WS_QX + fx + s * 1024, 0, AUX_SC1));
            opengate(2);
        }
        f32x16 O0 = {}, O1 = {}, O2 = {}, O3 = {}, negm, SA, SB; float lsum = 0.f, mrow, ls0, ls1, mx0, mx1; u32x4 pw0, pw1, pk_; unsigned va[4]; s16x4 vh[16];
#define R_O0 "{v[192:207]}"
#define R_O1 "{v[208:223]}"
#define R_O2 "{v[224:239]}"
#define R_O3 "{v[240:255]}"
#define R_SA "{v[176:191]}"
#define R_SB "{v[160:175]}"
#define N_SA "176"
#define N_SB "160"
#define R_NM "{v[144:159]}"
#define PINR(x) asm volatile("" : "+" R_##x(x))
#define EXP2(x) __builtin_amdgcn_exp2f(x)
#define MX3(a, b, c) __builtin_fmaxf(__builtin_fmaxf((a), (b)), (c))
#define VRDH(g, t, off) vh[2 * (g) + (t)] = trrd(LP(va[(g) & 3]) + (off) + ((g) >> 2) * 4096 + (t) * 2048)
#define V2(g, vo) do { VRDH(g, 0, vo); VRDH(g, 1, vo); } while (0)
#define VF(g) __builtin_shufflevector(vh[2 * (g)], vh[2 * (g) + 1], 0, 1, 2, 3, 4, 5, 6, 7)
#define KSTEP(s, d) do { ka[s] += (d); PIN(ka[s]); } while (0)
#define VSTEP(c, d) do { va[c] += (d); PIN(va[c]); } while (0)
#define RESCALE(S, mx, mxb) do { float t0_; asm volatile( \
            "v_max_f32 %8, %8, %11\n\tv_cmp_lt_f32 vcc, %10, %8\n\ts_cbranch_vccnz .Lrs%=\n" \
            ".Lrb%=:\n\t.subsection 1\n" \
            ".Lrs%=:\n\ts_nop 15\n\tv_mov_b32 %9, %8\n\ts_nop 1\n\tv_permlane32_swap_b32 %8, %9\n\ts_nop 1\n\tv_max3_f32 %8, %8, %9, 0\n\t" \
            "v_add_f32 %6, %6, %8\n\tv_exp_f32 %9, -%8\n\t" \
            ".set r_, 0\n\t.rept 16\n\tv_sub_f32 v[" N_##S "+r_], v[" N_##S "+r_], %8\n\tv_sub_f32 v[144+r_], 0, %6\n\t.set r_, r_+1\n\t.endr\n\t" \
            "v_mul_f32 %7, %7, %9\n\t" \
            ".set r_, 192\n\t.rept 64\n\tv_mul_f32 v[r_], v[r_], %9\n\t.set r_, r_+1\n\t.endr\n\ts_nop 1\n\ts_branch .Lrb%=\n\t.subsection 0" \
            : "+" R_O0(O0), "+" R_O1(O1), "+" R_O2(O2), "+" R_O3(O3), "+" R_##S(S), "+" R_NM(negm), "+v"(mrow), "+v"(lsum), "+v"(mx), "=&v"(t0_) : "s"(THR), "v"(mxb) : "vcc"); } while (0)
#define G1(SN, SO, s, CIN, PK, KL) do { SN = MFMA32(kf[s], qf[s], CIN); PINR(SN); SBAR(); PINR(SO); KL; SO[2 * (s)] = EXP2(SO[2 * (s)]); SO[2 * (s) + 1] = EXP2(SO[2 * (s) + 1]); PK; PINR(SO); SBAR(); } while (0)
#define PKW(SO, W, i, r) do { W[i] = pk2(SO[r], SO[(r) + 1]); PIN(W); } while (0)
#define PH1(SN, SO, KL) do { SBAR(); \
        G1(SN, SO, 0, negm, (void)0, KL(0));          G1(SN, SO, 1, SN, PKW(SO, pw0, 0, 0), KL(1)); G1(SN, SO, 2, SN, PKW(SO, pw0, 1, 2), KL(2));  G1(SN, SO, 3, SN, PKW(SO, pw0, 2, 4), KL(3)); \
        G1(SN, SO, 4, SN, PKW(SO, pw0, 3, 6), KL(4)); G1(SN, SO, 5, SN, PKW(SO, pw1, 0, 8), KL(5)); G1(SN, SO, 6, SN, PKW(SO, pw1, 1, 10), KL(6)); G1(SN, SO, 7, SN, PKW(SO, pw1, 2, 12), KL(7)); } while (0)
#define G2(g, OC, W, X_) do { OC = MFMA32(VF(g), __builtin_bit_cast(bf16x8, W), OC); PINR(OC); SBAR(); X_; SBAR(); } while (0)
#define ADD4(SO, r) do { ls0 += SO[r]; ls1 += SO[(r) + 1]; ls0 += SO[(r) + 2]; ls1 += SO[(r) + 3]; PIN(ls0); PIN(ls1); } while (0)
#define PH2(SN, SO, vo, A0, A1, A2, A3) do { SBAR(); \
        G2(0, O0, pw0, { V2(0, vo); PKW(SO, pw1, 3, 14); ls0 = lsum + SO[0]; ls1 = SO[1] + SO[3]; ls0 += SO[2]; PIN(ls0); PIN(ls1); }); G2(1, O1, pw0, { V2(1, vo); ADD4(SO, 4); }); \
        G2(2, O2, pw0, { V2(2, vo); ADD4(SO, 8); }); G2(3, O3, pw0, { V2(3, vo); ADD4(SO, 12); }); \
        G2(4, O0, pw1, { V2(4, vo); mx0 = MX3(SN[0], SN[1], SN[2]); mx1 = MX3(SN[3], SN[4], SN[5]); lsum = ls0 + ls1; PIN(mx0); PIN(mx1); PIN(lsum); A0; }); \
        G2(5, O1, pw1, { V2(5, vo); mx0 = MX3(mx0, SN[6], SN[7]); mx1 = MX3(mx1, SN[8], SN[9]); PIN(mx0); PIN(mx1); A1; }); \
        G2(6, O2, pw1, { V2(6, vo); mx0 = MX3(mx0, SN[10], SN[11]); mx1 = MX3(mx1, SN[12], SN[13]); PIN(mx0); PIN(mx1); A2; }); \
        G2(7, O3, pw1, { V2(7, vo); mx0 = MX3(mx0, SN[14], SN[15]); A3; }); \
        RESCALE(SN, mx0, mx1); } while (0)
#define KL0(s) KRD(s, 0)
#define KL1(s) KRD(s, 8192)
#define KLN(s) (void)0
#define NOP_ (void)0
#define PEEK() do { pk_ = *(volatile LAS const u32x4*)LP(a_landed); } while (0)
#define SEEN() do { unsigned m_ = pk_[0] < pk_[1] ? pk_[0] : pk_[1], n_ = pk_[2] < pk_[3] ? pk_[2] : pk_[3]; const int a_ = __builtin_amdgcn_readfirstlane(m_ < n_ ? m_ : n_); avail = a_ > avail ? a_ : avail; } while (0)
        {
            SA = MFMA32(kf[0], qf[0], (f32x16){});
#pragma unroll
            for (int s = 1; s < 8; ++s) SA = MFMA32(kf[s], qf[s], SA);
#pragma unroll
            for (int s = 0; s < 8; ++s) KRD(s, 8192);
            float mx = MX3(SA[0], SA[1], SA[2]);
#pragma unroll
            for (int r = 3; r < 15; r += 2) mx = MX3(mx, SA[r], SA[r + 1]);
            mx = swap_max(__builtin_fmaxf(mx, SA[15])); mrow = mx;
#pragma unroll
            for (int r = 0; r < 16; ++r) { SA[r] -= mx; negm[r] = -mx; }
            asm volatile("" : "+" R_NM(negm));
        }
        unsigned vcur = acquire(), kcur = so;
#pragma unroll
        for (int c = 0; c < 4; ++c) { va[c] = (unsigned)(256 * (4 * hi + q4) + 16 * ((4 * c + 2 * blk + (p4 >> 1)) ^ (q4 << 2)) + 8 * (p4 & 1)) + vcur; PIN(va[c]); }
#pragma unroll
        for (int g = 0; g < 8; ++g) V2(g, 0);
#pragma unroll
        for (int s = 0; s < 8; ++s) KADDR(s, kcur);
        unsigned tprev = 0, gap = 0; int per = roleB ? PB0 : PA0;
#pragma nounroll
        for (int t = 0; t < NT - 1; ++t) {
            (void)acquire();
            if (roleB || t <= PEND) {
                unsigned now = (unsigned)__builtin_amdgcn_s_memrealtime();
#pragma nounroll
                for (int k = 0; k < 256 && now - tprev < gap; ++k) { __builtin_amdgcn_s_sleep(1); now = (unsigned)__builtin_amdgcn_s_memrealtime(); }
                tprev = now; gap = (unsigned)per; const int d_ = roleB ? PB1 : PA1, m_ = roleB ? PMINB : PMINA; per = per - d_ > m_ ? per - d_ : m_;
            }
            LGKM(15);
            PH1(SB, SA, KL0);
            LGKM(8);
            PH2(SB, SA, 8192, PEEK(), NOP_, NOP_, NOP_);
            LGKM(15);
            PH1(SA, SB, KL1);
            SEEN();
            release(u - 1);
            const unsigned vo = acquire();
            const unsigned dv = vo - vcur, dk = so - kcur; vcur = vo; kcur = so;
            VSTEP(0, dv); VSTEP(1, dv); VSTEP(2, dv); VSTEP(3, dv);
            LGKM(8);
            PH2(SA, SB, 0, { KSTEP(0, dk); KSTEP(1, dk); }, { KSTEP(2, dk); KSTEP(3, dk); }, { KSTEP(4, dk); KSTEP(5, dk); }, { KSTEP(6, dk); KSTEP(7, dk); });
        }
        u64 fpv = 0;
        if (!roleB) fpv = __hip_atomic_load((GAS const u64*)(ctl + 512), RLX_AGENT);
        PH1(SB, SA, KLN);
        release(u - 1);
        u32x4 pf[8]; float plse = 0.f; bool early = false;
#define PLOAD() do { _Pragma("unroll") for (int i = 0; i < 8; ++i) pf[i] = __builtin_amdgcn_raw_buffer_load_b128(wsr, (unsigned)WS_PX + fx + i * 1024, 0, AUX_SC1); \
            plse = __uint_as_float(__builtin_amdgcn_raw_buffer_load_b32(wsr, (unsigned)WS_PL + (pair * 4 + rg) * 256 + lane * 4, 0, AUX_SC1)); } while (0)
        if (!roleB && rfl64(fpv) == tag) { early = true; asm volatile("" ::: "memory"); PLOAD(); }
        PH2(SB, SA, 8192, NOP_, NOP_, NOP_, NOP_);
        {
#pragma unroll
            for (int r = 0; r < 16; ++r) { SB[r] = EXP2(SB[r]); lsum += SB[r]; }
            pw0 = (u32x4){pk2(SB[0], SB[1]), pk2(SB[2], SB[3]), pk2(SB[4], SB[5]), pk2(SB[6], SB[7])};
            pw1 = (u32x4){pk2(SB[8], SB[9]), pk2(SB[10], SB[11]), pk2(SB[12], SB[13]), pk2(SB[14], SB[15])};
            const bf16x8 p0 = __builtin_bit_cast(bf16x8, pw0), p1 = __builtin_bit_cast(bf16x8, pw1);
            O0 = MFMA32(VF(0), p0, O0); O1 = MFMA32(VF(1), p0, O1); O2 = MFMA32(VF(2), p0, O2); O3 = MFMA32(VF(3), p0, O3);
            O0 = MFMA32(VF(4), p1, O0); O1 = MFMA32(VF(5), p1, O1); O2 = MFMA32(VF(6), p1, O2); O3 = MFMA32(VF(7), p1, O3);
        }
        release(u);
        lsum = swap_add(lsum);
        const float lse = mrow + __builtin_amdgcn_logf(lsum);
        if (!roleB) {
            if (!early) { poll_flags(ctl + 512, tag, budget); PLOAD(); }
            const float M = __builtin_fmaxf(lse, plse), eo = EXP2(lse - M), ep = EXP2(plse - M), r = __builtin_amdgcn_rcpf(eo + ep);
            const float fo = eo * r * __builtin_amdgcn_rcpf(lsum), fp = ep * r;
#pragma unroll
            for (int f = 0; f < 8; ++f) { const f32x16& a = f < 2 ? O0 : f < 4 ? O1 : f < 6 ? O2 : O3; const int q = 8 * (f & 1); u32x4 w;
#pragma unroll
                for (int e = 0; e < 4; ++e) w[e] = pk2(__builtin_fmaf(a[q + 2 * e], fo, fp * bflo(pf[f][e])), __builtin_fmaf(a[q + 2 * e + 1], fo, fp * bfhi(pf[f][e])));
                __builtin_amdgcn_raw_buffer_store_b128(w, wsr, (unsigned)WS_OX + fx + f * 1024, 0, AUX_SC1); }
        } else {
            const float inv = __builtin_amdgcn_rcpf(lsum);
#pragma unroll
            for (int f = 0; f < 8; ++f) { const f32x16& a = f < 2 ? O0 : f < 4 ? O1 : f < 6 ? O2 : O3; const int r = 8 * (f & 1);
                const u32x4 w = {pk2(a[r] * inv, a[r + 1] * inv), pk2(a[r + 2] * inv, a[r + 3] * inv), pk2(a[r + 4] * inv, a[r + 5] * inv), pk2(a[r + 6] * inv, a[r + 7] * inv)};
                __builtin_amdgcn_raw_buffer_store_b128(w, wsr, (unsigned)WS_PX + fx + f * 1024, 0, AUX_SC1); }
            __builtin_amdgcn_raw_buffer_store_b32(__float_as_uint(lse), wsr, (unsigned)WS_PL + (pair * 4 + rg) * 256 + lane * 4, 0, AUX_SC1);
        }
        signoff(1, roleB ? ctl + 512 : ctl + 768);
        if (roleB) opengate(1);
        const int qh = rg & 1, nh = rg >> 1;
        *(volatile LAS u64*)(lds + L_TAG) = tag;
        u += NSL - 1; (void)acquire();
        unsigned wa[4], wh = lds0 + 131072 + (nh * 64 + hi) * 128 + 32 * q4 + 128 * blk + 8 * p4; PIN(wh);
#pragma unroll
        for (int c = 0; c < 4; ++c) { wa[c] = lds0 + ((((nh ^ (c >> 1)) * 64 + hi) * 4 + q4) * 256 + 64 * ((2 * nh + (c & 1)) ^ q4) + 32 * blk + 8 * p4); PIN(wa[c]); }
#define AKS(i) ((i) < 48 ? (i) / 3 : 16 + ((i) - 48) / 2)
#define ATL(i) ((i) < 48 ? (i) % 3 : ((i) - 48) % 2)
#define AWA(i) wa[((i) < 48 ? 0 : 2) + (ATL(i) & 1)]
#define ARD(i) (ATL(i) == 2 ? trrd2(LP(wh) + AKS(i) * 512, LP(wh) + AKS(i) * 512 + 256) : trrd2(LP(AWA(i)) + (AKS(i) & 15) * 4096, LP(AWA(i)) + (AKS(i) & 15) * 4096 + 2048))
        constexpr int AD = 7, OPRE = 10;
        bf16x8 af[AD], of[32];
#pragma unroll
        for (int i = 0; i < AD; ++i) af[i] = ARD(i);
        const int pr0 = b * HKV + ks * 2 + nh;
        POLL1(ws + WS_CT + (size_t)(pr0 ^ (lane & 1)) * 1024 + 768, tag, 2);
        const unsigned ob0 = (unsigned)WS_OX + (pr0 * 4 + qh) * 8192 + lane * 16, ob1 = (unsigned)WS_OX + ((pr0 ^ 1) * 4 + qh) * 8192 + lane * 16;
#define OLD(f) of[f] = __builtin_bit_cast(bf16x8, __builtin_amdgcn_raw_buffer_load_b128(wsr, ((f) < 16 ? ob0 : ob1) + ((f) & 3) * 1024, (((f) >> 3) & 1) * 16384 + (((f) >> 2) & 1) * 4096, AUX_SC1))
#pragma unroll
        for (int f = 0; f < OPRE; ++f) OLD(f);
        f32x16 acc[3] = {};
        SBAR();
#pragma unroll
        for (int i = 0; i < 80; ++i) { acc[ATL(i)] = MFMA32(af[i % AD], of[AKS(i)], acc[ATL(i)]); PIN(acc[ATL(i)]); SBAR(); if (i + AD < 80) af[i % AD] = ARD(i + AD);
            if ((i & 1) == 0 && OPRE + i / 2 < 32) OLD(OPRE + i / 2); SBAR(); }
        const unsigned sb = (unsigned)WS_SLAB + ((b * 4 + nq) * 8 + (r32 >> 2)) * 20480 + ks * 2560 + ((lane & 3) * 2 + hi) * 16;
#pragma unroll
        for (int k = 0; k < 5; ++k) { const int kk = k == 0 ? 4 : k - 1; const f32x16& a = acc[kk >> 1]; const int r = 8 * (kk & 1);
            const u32x4 v = {pk2(a[r], a[r + 1]), pk2(a[r + 2], a[r + 3]), pk2(a[r + 4], a[r + 5]), pk2(a[r + 6], a[r + 7])};
            __builtin_amdgcn_raw_buffer_store_b128(v, wsr, sb + (kk < 4 ? (qh * 8 + nh * 4 + kk) * 128 : 2048 + (nh * 2 + qh) * 128), 0, AUX_SC1); }
        signoff(2, fsl + ks * 256);
        tagf = tag;
    }
    {
        POLL1(fsl + (lane & 7) * 256, tagf, 3);
        const int e = wid * 32 + r32;
        const unsigned sb = (unsigned)WS_SLAB + ((b * 4 + nq) * 8 + ks) * 20480 + hi * 10240 + e * 16;
        u32x4 v[4];
#pragma unroll
        for (int k = 0; k < 4; ++k) v[k] = __builtin_amdgcn_raw_buffer_load_b128(wsr, sb + k * 2560, 0, AUX_SC1);
        if (ks == 0 && nq == 0 && tid == 0) __hip_atomic_store((GAS u64*)epo, tagf, RLX_AGENT);
        float s[8], t[4];
#pragma unroll
        for (int c = 0; c < 4; ++c) { s[2 * c] = (bflo(v[0][c]) + bflo(v[1][c])) + (bflo(v[2][c]) + bflo(v[3][c])); s[2 * c + 1] = (bfhi(v[0][c]) + bfhi(v[1][c])) + (bfhi(v[2][c]) + bfhi(v[3][c])); }
#pragma unroll
        for (int c = 0; c < 4; ++c) { auto rr = __builtin_amdgcn_permlane32_swap(__float_as_uint(s[c]), __float_as_uint(s[4 + c]), false, false); t[c] = __uint_as_float(rr[0]) + __uint_as_float(rr[1]); }
        int qh = e >> 6, col = nq * 128 + ((e >> 3) & 7) * 16;
        if (wid == 4) {
#pragma unroll
            for (int c = 0; c < 4; ++c) t[c] += __shfl_xor(t[c], 16);
            qh = (e >> 3) & 1; col = 512 + nq * 16; }
        if (e < 144) *(u32x2*)(out + ((size_t)(b * SQ + qh * 32 + ks * 4 + ((e >> 1) & 3)) * DM + col + hi * 8 + (e & 1) * 4) * 2) = (u32x2){pk2(t[0], t[1]), pk2(t[2], t[3])};
    }
}

extern "C" void kernel_launch(void* const* d_in, const int* in_sizes, int n_in, void* d_out, int out_size, void* d_ws, size_t ws_size, hipStream_t stream) {
    (void)hipFuncSetAttribute((const void*)gqa_block_k, hipFuncAttributeMaxDynamicSharedMemorySize, LDS_BYTES);
    hipLaunchKernelGGL(gqa_block_k, dim3(256), dim3(512), LDS_BYTES, stream, (const char*)d_in[0], (const char*)d_in[1], (const char*)d_in[2], (const char*)d_in[3],
                       (const char*)d_in[4], (char*)d_out, (char*)d_ws);
}
```

```cpp
#include <hip/hip_runtime.h>
#include <hip/hip_bf16.h>
#include <stdint.h>

typedef short bf16x8 __attribute__((ext_vector_type(8)));
typedef short s16x4 __attribute__((ext_vector_type(4)));
typedef float f32x16 __attribute__((ext_vector_type(16)));
typedef float f32x2 __attribute__((ext_vector_type(2)));
typedef unsigned u32x4 __attribute__((ext_vector_type(4)));
typedef unsigned u32x2 __attribute__((ext_vector_type(2)));
typedef __bf16 bf2_t __attribute__((ext_vector_type(2)));
typedef unsigned long long u64;
#define LAS __attribute__((address_space(3)))
#define GAS __attribute__((address_space(1)))
typedef LAS const char* lcptr;
typedef LAS char* lptr;
#define DI __device__ __forceinline__
#define AINL __attribute__((always_inline))
#define MFMA32(a, b, c) __builtin_amdgcn_mfma_f32_32x32x16_bf16((a), (b), (c), 0, 0, 0)
#define WAITV(N) asm volatile("s_waitcnt vmcnt(" #N ")" ::: "memory")
#define BAR() asm volatile("s_barrier" ::: "memory")
#define PIN(x) asm volatile("" : "+v"(x))
#define SBAR() __builtin_amdgcn_sched_barrier(0)
#define LGKM(n) do { __builtin_amdgcn_s_waitcnt(0xC07F | ((n) << 8)); SBAR(); } while (0)
#define LP(a) ((lcptr)(uintptr_t)(a))
#define STR2(x) #x
#define STR(x) STR2(x)
#define CLOB_V38_255 "v38", "v39", "v40", "v41", "v42", "v43", "v44", "v45", "v46", "v47", "v48", "v49", "v50", "v51", "v52", "v53", "v54", "v55", "v56", "v57", "v58", "v59", "v60", "v61", "v62", "v63", "v64", "v65", "v66", "v67", "v68", "v69", "v70", "v71", "v72", "v73", "v74", "v75", "v76", "v77", "v78", "v79", "v80", "v81", "v82", "v83", "v84", "v85", "v86", "v87", "v88", "v89", "v90", "v91", "v92", "v93", "v94", "v95", "v96", "v97", "v98", "v99", "v100", "v101", "v102", "v103", "v104", "v105", "v106", "v107", "v108", "v109", "v110", "v111", "v112", "v113", "v114", "v115", "v116", "v117", "v118", "v119", "v120", "v121", "v122", "v123", "v124", "v125", "v126", "v127", "v128", "v129", "v130", "v131", "v132", "v133", "v134", "v135", "v136", "v137", "v138", "v139", "v140", "v141", "v142", "v143", "v144", "v145", "v146", "v147", "v148", "v149", "v150", "v151", "v152", "v153", "v154", "v155", "v156", "v157", "v158", "v159", "v160", "v161", "v162", "v163", "v164", "v165", "v166", "v167", "v168", "v169", "v170", "v171", "v172", "v173", "v174", "v175", "v176", "v177", "v178", "v179", "v180", "v181", "v182", "v183", "v184", "v185", "v186", "v187", "v188", "v189", "v190", "v191", "v192", "v193", "v194", "v195", "v196", "v197", "v198", "v199", "v200", "v201", "v202", "v203", "v204", "v205", "v206", "v207", "v208", "v209", "v210", "v211", "v212", "v213", "v214", "v215", "v216", "v217", "v218", "v219", "v220", "v221", "v222", "v223", "v224", "v225", "v226", "v227", "v228", "v229", "v230", "v231", "v232", "v233", "v234", "v235", "v236", "v237", "v238", "v239", "v240", "v241", "v242", "v243", "v244", "v245", "v246", "v247", "v248", "v249", "v250", "v251", "v252", "v253", "v254", "v255"
#define RLX_AGENT __ATOMIC_RELAXED, __HIP_MEMORY_SCOPE_AGENT
#define RLX_WG __ATOMIC_RELAXED, __HIP_MEMORY_SCOPE_WORKGROUP
#define AUX_SC1 16

#define NTA 18
#define GATEA 10
#define HPRE 2
#define PA0 110
#define PA1 2
#define PMINA 103
#define PB0 150
#define PB1 9
#define PMINB 103
#define PEND 13

constexpr int SQ = 64, DM = 576, HKV = 16, SK = 2048;
constexpr float QSCALE = 0.08838834764831845f * 1.4426950408889634f;
constexpr float THR = 8.f;
constexpr int NT_A = NTA, NT_B = 32 - NTA;
constexpr int USLOT = 16384, NSL = 9, MISC = NSL * USLOT, LDS_BYTES = 163840;
constexpr int L_LANDED = MISC, L_DONE = MISC + 16, L_CNT = MISC + 32, L_GATE = MISC + 44, L_TAG = MISC + 64;
constexpr int SPIN_CAP = 1 << 20;
constexpr size_t WS_CT = 0;
constexpr size_t WS_EP = 128 * 1024, WS_FS = WS_EP + 8 * 256;
constexpr size_t WS_QX = 1 << 20;
constexpr size_t WS_PX = WS_QX + ((size_t)4 << 20);
constexpr size_t WS_PL = WS_PX + ((size_t)4 << 20);
constexpr size_t WS_OX = WS_PL + ((size_t)1 << 20);
constexpr size_t WS_SLAB = WS_OX + ((size_t)4 << 20);
constexpr int WS_USED = 32 << 20;

DI unsigned pk2(float a, float b) { bf2_t r = __builtin_convertvector((f32x2){a, b}, bf2_t); return __builtin_bit_cast(unsigned, r); }
DI float bflo(unsigned v) { return __uint_as_float(v << 16); }
DI float bfhi(unsigned v) { return __uint_as_float(v & 0xffff0000u); }
DI s16x4 trrd(lcptr p) { typedef short v4i16_t __attribute__((ext_vector_type(4))); return __builtin_bit_cast(s16x4, __builtin_amdgcn_ds_read_tr16_b64_v4i16((LAS v4i16_t*)p)); }
DI bf16x8 trrd2(lcptr p0, lcptr p1) { s16x4 a = trrd(p0), b = trrd(p1); return __builtin_shufflevector(a, b, 0, 1, 2, 3, 4, 5, 6, 7); }
DI bf16x8 ldsrd16(lcptr p) { return *(LAS const bf16x8*)p; }
DI float swap_max(float m) { auto rr = __builtin_amdgcn_permlane32_swap(__float_as_uint(m), __float_as_uint(m), false, false); return __builtin_fmaxf(__uint_as_float(rr[0]), __uint_as_float(rr[1])); }
DI float swap_add(float m) { auto rr = __builtin_amdgcn_permlane32_swap(__float_as_uint(m), __float_as_uint(m), false, false); return __uint_as_float(rr[0]) + __uint_as_float(rr[1]); }
DI u64 next_tag(u64 e) { return e * 6364136223846793005ull + 1442695040888963407ull; }
DI u64 rfl64(u64 v) { return ((u64)(unsigned)__builtin_amdgcn_readfirstlane((unsigned)(v >> 32)) << 32) | (unsigned)__builtin_amdgcn_readfirstlane((unsigned)v); }
DI void lds_put(unsigned a, unsigned val) { asm volatile("ds_write_b32 %0, %1" :: "v"(a), "v"(val) : "memory"); }
DI void poll_flags(const char* p, u64 tag, int& budget) {
#pragma nounroll
    while (!__all(__hip_atomic_load((GAS const u64*)p, RLX_AGENT) == tag) && --budget > 0) {}
    asm volatile("" ::: "memory"); }
DI void wait_lds(lcptr p, unsigned need, int& budget) {
#pragma nounroll
    while ((unsigned)__builtin_amdgcn_readfirstlane(*(volatile LAS const unsigned*)p) < need && --budget > 0) __builtin_amdgcn_s_sleep(1); }

#define DMA4(POL, sbase, v0, v1, v2, v3, ldst) do { unsigned sv_; asm volatile("s_mov_b32 %0, m0\n\ts_mov_b32 m0, %6\n\ts_nop 0\n\t" \
    "global_load_lds_dwordx4 %1, %5" POL "\n\tglobal_load_lds_dwordx4 %2, %5 offset:1024" POL "\n\tglobal_load_lds_dwordx4 %3, %5 offset:2048" POL "\n\tglobal_load_lds_dwordx4 %4, %5 offset:3072" POL "\n\t" \
    "s_mov_b32 m0, %0" : "=&s"(sv_) : "v"(v0), "v"(v1), "v"(v2), "v"(v3), "s"(sbase), "s"(__builtin_amdgcn_readfirstlane(ldst)) : "memory"); } while (0)

__global__ __launch_bounds__(512, 2) void gqa_block_k(const char* __restrict__ X, const char* __restrict__ Kc, const char* __restrict__ Vc, const char* __restrict__ Wq,
                                                      const char* __restrict__ Wo, char* __restrict__ out, char* __restrict__ ws) {
    extern __shared__ __attribute__((aligned(16))) char lds_[];
    const int tid = threadIdx.x, lane = tid & 63, r32 = lane & 31, hi = lane >> 5;
    const int wid = __builtin_amdgcn_readfirstlane(tid >> 6);
    const int bid = blockIdx.x, ks = bid & 7, idx = bid >> 3;
    const int kvh = ks * 2 + (idx & 1), b = (idx >> 1) & 7;
    const bool roleB = idx < 16;
    const int pair = b * HKV + kvh;
    const int nq = (idx & 1) + (roleB ? 0 : 2);
    const lptr lds = (lptr)lds_;
    const unsigned lds0 = (unsigned)(uintptr_t)lds;
    const int NT = roleB ? NT_B : NT_A;
    char* const ctl = ws + WS_CT + (size_t)pair * 1024;
    char* const epo = ws + WS_EP + b * 256;
    char* const fsl = ws + WS_FS + (size_t)(b * 4 + nq) * 8 * 256;
    const __amdgpu_buffer_rsrc_t wsr = __builtin_amdgcn_make_buffer_rsrc(ws, 0, WS_USED, 0x00020000);
    u64 pc0; unsigned code_len;
    asm volatile("s_getpc_b64 %0\n.Lcs%=:\n\ts_mov_b32 %1, .Lce%=-.Lcs%=\n\t.subsection 2\n.Lce%=:\n\t.fill 128, 4, 0xBF800000\n\t.subsection 0" : "=s"(pc0), "=s"(code_len));
    if (tid < 16) *(LAS unsigned*)(lds + MISC + tid * 4) = 0u;
    int budget = SPIN_CAP;
    u64 tagf;

    if (wid >= 4) {
        const int j = wid - 4, lr = lane >> 4, lc = lane & 15;
        const size_t koff = ((size_t)pair * SK + (roleB ? NT_A * 64 : 0)) * 256;
        const char* kp = Kc + koff; const char* vp = Vc + koff;
        unsigned vK[4];
#pragma unroll
        for (int i = 0; i < 4; ++i) vK[i] = (unsigned)((16 * j + lr) * 256 + 16 * (lc ^ (4 * i + lr)));
        const unsigned vV = (unsigned)((16 * j + lr) * 256 + 16 * (lc ^ (lr << 2)));
        unsigned a_landed = lds0 + L_LANDED + 4 * j, a_done = lds0 + L_DONE; PIN(a_landed); PIN(a_done);
        if (roleB) {
            unsigned vX[2], vW[4];
#pragma unroll
            for (int i = 0; i < 2; ++i) { const int row = 16 * j + 8 * i + (lane >> 3); vX[i] = (unsigned)(row * 1152 + 16 * ((lane & 7) ^ ((row >> 1) & 7)) - 1024 * i); }
#pragma unroll
            for (int i = 0; i < 4; ++i) vW[i] = (unsigned)((8 * j + 2 * i + hi) * 8192 + 16 * (r32 ^ (((2 * i + hi) & 3) << 2)) - 1024 * i);
            const char* xb = X + (size_t)b * SQ * DM * 2; const char* wb = Wq + kvh * 512;
            int xo = b * 128, sb = 0, cnt = 7;
#define QADV "s_add_i32 %[xo], %[xo], 128\n\ts_cmp_eq_u32 %[xo], 1152\n\ts_cselect_b32 %[xo], 0, %[xo]\n\ts_add_i32 %[sb], %[sb], 0xc000\n\ts_cmp_eq_u32 %[sb], 0x24000\n\ts_cselect_b32 %[sb], 0, %[sb]\n\t"
#define QW4 "global_load_lds_dwordx4 %[w0], s[60:61]\n\tglobal_load_lds_dwordx4 %[w1], s[60:61] offset:1024\n\tglobal_load_lds_dwordx4 %[w2], s[60:61] offset:2048\n\tglobal_load_lds_dwordx4 %[w3], s[60:61] offset:3072\n\t"
#define QEVEN0 "s_add_u32 s62, %[xl], %[xo]\n\ts_addc_u32 s63, %[xh], 0\n\ts_lshl_b32 s66, %[xo], 12\n\ts_add_u32 s60, %[wl], s66\n\ts_addc_u32 s61, %[wh], 0\n\ts_add_i32 m0, %[sb], %[dx]\n\ts_add_i32 s66, %[sb], %[dw]\n\t"
#define QEVEN1 "global_load_lds_dwordx4 %[x0], s[62:63]\n\tglobal_load_lds_dwordx4 %[x1], s[62:63] offset:1024\n\ts_mov_b32 m0, s66\n\ts_nop 0\n\t" QW4
#define QODD0 "s_add_u32 s60, s60, 0x40000\n\ts_addc_u32 s61, s61, 0\n\ts_add_i32 m0, m0, 0x4000\n\t"
            asm volatile(
                "s_mov_b32 s70, m0\n\t"
                QEVEN0 QEVEN1 QODD0 "s_nop 0\n\t" QW4 QADV QEVEN0 QEVEN1 QODD0
                "s_waitcnt vmcnt(10)\n"
                ".Lqr%=:\n\t"
                "s_barrier\n\t" QW4 QADV QEVEN0 "s_waitcnt vmcnt(10)\n\t"
                "s_barrier\n\t" QEVEN1 QODD0 "s_sub_i32 %[cnt], %[cnt], 1\n\ts_cmp_gt_i32 %[cnt], 0\n\ts_waitcnt vmcnt(10)\n\ts_cbranch_scc1 .Lqr%=\n\t"
                "s_barrier\n\t" QW4 "s_waitcnt vmcnt(10)\n\t"
                "s_barrier\n\ts_waitcnt vmcnt(4)\n\t"
                "s_barrier\n\ts_waitcnt vmcnt(0)\n\t"
                "s_barrier\n\ts_mov_b32 m0, s70"
                : [xo] "+&s"(xo), [sb] "+&s"(sb), [cnt] "+&s"(cnt)
                : [xl] "s"((unsigned)(uintptr_t)xb), [xh] "s"((unsigned)((uintptr_t)xb >> 32)), [wl] "s"((unsigned)(uintptr_t)wb), [wh] "s"((unsigned)((uintptr_t)wb >> 32)),
                  [dx] "s"(lds0 + j * 2048), [dw] "s"(lds0 + USLOT + j * 4096),
                  [x0] "v"(vX[0]), [x1] "v"(vX[1]), [w0] "v"(vW[0]), [w1] "v"(vW[1]), [w2] "v"(vW[2]), [w3] "v"(vW[3])
                : "memory", "scc", "s60", "s61", "s62", "s63", "s66", "s70");
        } else {
            DMA4(" nt", kp, vK[0], vK[1], vK[2], vK[3], lds0 + j * 4096); DMA4(" nt", vp, vV, vV, vV, vV, lds0 + USLOT + j * 4096);
            BAR();
        }
        const char* wb = Wo + (size_t)ks * 512 * 1152;
        unsigned oF[4], oH[4];
#pragma unroll
        for (int i = 0; i < 4; ++i) { oF[i] = (unsigned)(((4 * j + i) * 4 + lr) * 1152 + nq * 256 + 16 * (lc ^ (lr << 2)) - 1024 * i);
            oH[i] = (unsigned)((j * 128 + i * 32 + (lane >> 1)) * 1152 + 1024 + nq * 32 + (lane & 1) * 16 - 1024 * i); }
        const unsigned wl = (unsigned)(lds0 + j * 4096 + lane * 16), a_gate = lds0 + L_GATE;
        const int nkv = 2 * NT, ntot = roleB ? nkv : nkv + NSL, n0 = roleB ? 0 : 2;
        int ni = n0, nl = 0, nw = 0, bi = 0, bw = 0, gate = roleB ? 0 : GATEA, si = n0 * USLOT, sw = 0, lim = NSL, wc = roleB ? 9 : 0;
        static_assert(GATEA >= NSL && GATEA <= NSL + 4, "four register buffers");
#define WQUOTA(L) "s_sub_i32 s67, %[ni], " STR(GATEA) "\n\ts_lshr_b32 s67, s67, 1\n\ts_add_i32 s67, s67, 3\n\ts_cmp_ge_i32 %[wc], s67\n\ts_cbranch_scc1 " L "\n\t"
        asm volatile(
            "s_mov_b32 s70, m0\n\ts_mov_b64 s[72:73], 0\n\ts_mov_b32 s75, 0\n\ts_getpc_b64 s[64:65]\n"
            ".Lp%=:\n\t"
            "s_branch .Lprep%=\n"
            ".Ltop%=:\n\t"
            "s_sub_i32 s66, %[ni], %[nl]\n\ts_cmp_lt_i32 s66, 3\n\ts_cbranch_scc1 .Lc1%=\n\t"
            "s_waitcnt vmcnt(8)\n\ts_add_i32 %[nl], %[nl], 1\n\t"
            ".Lc1%=:\n\t"
            "s_waitcnt lgkmcnt(0)\n\tv_min_u32 v42, v42, v43\n\tv_min3_u32 v40, v40, v41, v42\n\ts_mov_b32 s71, 0\n\ts_cmp_lt_i32 %[nw], %[nl]\n\tv_readfirstlane_b32 s67, v40\n\t"
            "s_cbranch_scc0 .Lc2%=\n\t"
            "s_bitcmp1_b64 s[72:73], %[nw]\n\ts_cbranch_scc1 .Lwr%=\n"
            ".Lpb%=:\n\t"
            "s_add_i32 %[nw], %[nw], 1\n\tv_mov_b32 v38, %[nw]\n\tds_write_b32 %[aland], v38\n\t"
            "s_add_i32 %[sw], %[sw], 0x4000\n\ts_cmp_eq_u32 %[sw], 0x24000\n\ts_cselect_b32 %[sw], 0, %[sw]\n\ts_mov_b32 s71, 1\n\t"
            "s_cmp_lt_i32 %[nw], %[nl]\n\ts_cbranch_scc1 .Lmore%=\n"
            ".Lc2%=:\n\t"
            "s_add_i32 s67, s67, 9\n\ts_max_i32 %[lim], %[lim], s67\n\t"
            "s_cmp_ge_i32 %[ni], %[gate]\n\ts_cbranch_scc1 .Lno%=\n\t"
            "s_cmp_lt_i32 %[ni], %[lim]\n\ts_cbranch_scc0 .Lis%=\n\t"
            "global_load_lds_dwordx4 v44, s[60:61] nt\n\tglobal_load_lds_dwordx4 v45, s[60:61] offset:1024 nt\n\t"
            "global_load_lds_dwordx4 v46, s[60:61] offset:2048 nt\n\tglobal_load_lds_dwordx4 v47, s[60:61] offset:3072 nt\n"
            ".Lbk%=:\n\t"
            "s_add_i32 %[ni], %[ni], 1\n\ts_mov_b32 s75, 1\n\t"
            "s_add_i32 %[si], %[si], 0x4000\n\ts_cmp_eq_u32 %[si], 0x24000\n\ts_cselect_b32 %[si], 0, %[si]\n"
            ".Lprep%=:\n\t"
            "ds_read_b128 v[40:43], %[adone]\n\t"
            "s_add_i32 m0, %[si], %[dbase]\n\t"
            "s_lshr_b32 s66, %[ni], 1\n\ts_lshl_b32 s66, s66, 14\n\t"
            "s_bitcmp1_b32 %[ni], 0\n\ts_cselect_b32 s60, %[vpl], %[kpl]\n\ts_cselect_b32 s61, %[vph], %[kph]\n\ts_cselect_b64 vcc, -1, 0\n\t"
            "s_add_u32 s60, s60, s66\n\ts_addc_u32 s61, s61, 0\n\t"
            "v_cndmask_b32 v44, %[k0], %[vv], vcc\n\tv_cndmask_b32 v45, %[k1], %[vv], vcc\n\tv_cndmask_b32 v46, %[k2], %[vv], vcc\n\tv_cndmask_b32 v47, %[k3], %[vv], vcc\n\t"
            "s_branch .Ltop%=\n"
            ".Lmore%=:\n\ts_bitcmp1_b64 s[72:73], %[nw]\n\ts_cbranch_scc1 .Lwr%=\n\ts_branch .Lpb%=\n"
            ".Lno%=:\n\t"
            "s_cmp_ge_i32 %[gate], %[nkv]\n\ts_cbranch_scc1 .Lno2%=\n\t"
            "ds_read_b32 v40, %[agate]\n\ts_waitcnt lgkmcnt(0)\n\tv_readfirstlane_b32 s67, v40\n\ts_nop 3\n\t"
            "s_cmp_ge_u32 s67, 4\n\ts_cselect_b32 %[gate], %[g1], %[gate]\n\ts_cmp_ge_u32 s67, 8\n\ts_cselect_b32 %[gate], %[nkv], %[gate]\n\t"
            "s_cmp_lt_i32 %[ni], %[gate]\n\ts_cselect_b32 s71, 1, s71\n"
            ".Lno2%=:\n\t"
            "ds_read_b128 v[40:43], %[adone]\n\t"
            "s_cmp_lg_u32 s71, 0\n\ts_cbranch_scc1 .Ltop%=\n\t"
            "s_sub_i32 s66, %[ni], %[nl]\n\ts_cmp_eq_u32 s66, 0\n\ts_cbranch_scc1 .Lno3%=\n\t"
            "s_cmp_eq_u32 s66, 2\n\ts_cbranch_scc1 .Lw4%=\n\ts_waitcnt vmcnt(0)\n"
            ".Lw4%=:\n\ts_waitcnt vmcnt(4)\n\ts_add_i32 %[nl], %[nl], 1\n\ts_branch .Lc1%=\n"
            ".Lno3%=:\n\ts_cmp_ge_i32 %[nw], %[ntot]\n\ts_cbranch_scc1 .Lend%=\n\t"
            "s_cmp_lt_i32 %[gate], %[nkv]\n\ts_cbranch_scc1 .Lidle%=\n\t"
            "s_cmp_lt_i32 %[wc], 9\n\ts_cbranch_scc1 .Lwo0%=\n\t"
            "s_cmp_eq_u32 %[ni], %[nkv]\n\ts_cbranch_scc0 .Lidle%=\n\ts_cmp_eq_u32 %[ni], %[ntot]\n\ts_cbranch_scc1 .Lidle%=\n\t"
            "s_waitcnt vmcnt(0)\n\ts_lshl_b64 s[62:63], 0x1ff, %[nkv]\n\ts_or_b64 s[72:73], s[72:73], s[62:63]\n\ts_mov_b32 %[ni], %[ntot]\n\ts_mov_b32 %[nl], %[ntot]\n\ts_branch .Lc1%=\n"
            ".Lidle%=:\n\t"
            "s_sleep 1\n\ts_sub_i32 %[bud], %[bud], 1\n\ts_cmp_gt_i32 %[bud], 0\n\ts_cbranch_scc1 .Lc1%=\n\t"
            "s_mov_b32 %[lim], 0x3fffffff\n\ts_mov_b32 %[gate], %[nkv]\n\ts_branch .Lc1%=\n"
            ".Lrf%=:\n\t"
            "s_cmp_eq_u32 s75, 0\n\ts_cbranch_scc1 .Lno2%=\n\ts_cmp_ge_i32 %[wc], 9\n\ts_cbranch_scc1 .Lno2%=\n\t"
            WQUOTA(".Lno2%=")
            "s_mov_b32 s75, 0\n\tds_read_b128 v[40:43], %[adone]\n\ts_branch .Lwo%=\n"
            ".Lwo0%=:\n\t"
            WQUOTA(".Lidle%=")
            "s_waitcnt vmcnt(8)\n"
            ".Lwo%=:\n\t"
            "s_and_b32 s66, %[wc], 7\n\ts_mul_i32 s66, s66, 73728\n\ts_add_u32 s76, %[wpl], s66\n\ts_addc_u32 s77, %[wph], 0\n\t"
            "s_mul_i32 s66, %[wc], 36\n\ts_add_u32 s66, s66, .Lit2%=-.Lp%=\n\ts_add_u32 s62, s64, s66\n\ts_addc_u32 s63, s65, 0\n\t"
            "s_add_i32 %[wc], %[wc], 1\n\t"
            "s_setpc_b64 s[62:63]\n"
            ".Lit2%=:\n\t"
            ".set r_, 112\n\t.rept 8\n\t"
            "global_load_dwordx4 v[r_:r_+3], %[f0], s[76:77] nt\n\tglobal_load_dwordx4 v[r_+4:r_+7], %[f1], s[76:77] offset:1024 nt\n\t"
            "global_load_dwordx4 v[r_+8:r_+11], %[f2], s[76:77] offset:2048 nt\n\tglobal_load_dwordx4 v[r_+12:r_+15], %[f3], s[76:77] offset:3072 nt\n\ts_branch .Lc1%=\n\t"
            ".set r_, r_+16\n\t.endr\n\t"
            "global_load_dwordx4 v[240:243], %[h0], s[76:77] nt\n\tglobal_load_dwordx4 v[244:247], %[h1], s[76:77] offset:1024 nt\n\t"
            "global_load_dwordx4 v[248:251], %[h2], s[76:77] offset:2048 nt\n\tglobal_load_dwordx4 v[252:255], %[h3], s[76:77] offset:3072 nt\n\ts_branch .Lc1%=\n"
            ".Lis%=:\n\t"
            "s_cmp_ge_i32 %[ni], " STR(GATEA) "\n\ts_cbranch_scc1 .Lrf%=\n\t"
            "s_add_u32 s67, %[bi], .Lit%=-.Lp%=\n\ts_add_u32 s62, s64, s67\n\ts_addc_u32 s63, s65, 0\n\t"
            "s_bitset1_b64 s[72:73], %[ni]\n\ts_add_i32 %[bi], %[bi], 36\n\t"
            "s_setpc_b64 s[62:63]\n"
            ".Lit%=:\n\t"
            ".set r_, 48\n\t.rept 4\n\t"
            "global_load_dwordx4 v[r_:r_+3], v44, s[60:61] nt\n\tglobal_load_dwordx4 v[r_+4:r_+7], v45, s[60:61] offset:1024 nt\n\t"
            "global_load_dwordx4 v[r_+8:r_+11], v46, s[60:61] offset:2048 nt\n\tglobal_load_dwordx4 v[r_+12:r_+15], v47, s[60:61] offset:3072 nt\n\ts_branch .Lbk%=\n\t"
            ".set r_, r_+16\n\t.endr\n"
            ".Lwr%=:\n\t"
            "s_add_i32 s66, s67, 9\n\ts_max_i32 %[lim], %[lim], s66\n\t"
            "s_cmp_lt_i32 %[nw], %[lim]\n\ts_cbranch_scc0 .Lc2%=\n\t"
            "v_add_u32 v39, %[sw], %[wl]\n\t"
            "s_lshr_b32 s66, %[sw], 14\n\ts_mul_i32 s66, s66, 36\n\ts_add_i32 s66, s66, 144\n\ts_cmp_lt_i32 %[nw], %[nkv]\n\ts_cselect_b32 s66, %[bw], s66\n\t"
            "s_add_u32 s66, s66, .Lwt%=-.Lp%=\n\ts_add_u32 s62, s64, s66\n\ts_addc_u32 s63, s65, 0\n\t"
            "s_add_i32 %[bw], %[bw], 36\n\t"
            "s_setpc_b64 s[62:63]\n"
            ".Lwt%=:\n\t"
            ".set r_, 48\n\t.rept 13\n\t"
            "ds_write_b128 v39, v[r_:r_+3]\n\tds_write_b128 v39, v[r_+4:r_+7] offset:1024\n\tds_write_b128 v39, v[r_+8:r_+11] offset:2048\n\tds_write_b128 v39, v[r_+12:r_+15] offset:3072\n\ts_branch .Lpb%=\n\t"
            ".set r_, r_+16\n\t.endr\n"
            ".Lend%=:\n\ts_mov_b32 m0, s70\n\ts_waitcnt lgkmcnt(0)"
            : [ni] "+&s"(ni), [nl] "+&s"(nl), [nw] "+&s"(nw), [bi] "+&s"(bi), [bw] "+&s"(bw), [gate] "+&s"(gate), [si] "+&s"(si), [sw] "+&s"(sw), [lim] "+&s"(lim),
              [wc] "+&s"(wc), [bud] "+&s"(budget)
            : [nkv] "s"(nkv), [ntot] "s"(ntot), [g1] "s"(roleB ? HPRE : GATEA), [dbase] "s"(lds0 + j * 4096),
              [kpl] "s"((unsigned)(uintptr_t)kp), [kph] "s"((unsigned)((uintptr_t)kp >> 32)), [vpl] "s"((unsigned)(uintptr_t)vp), [vph] "s"((unsigned)((uintptr_t)vp >> 32)),
              [wpl] "s"((unsigned)(uintptr_t)wb), [wph] "s"((unsigned)((uintptr_t)wb >> 32)),
              [k0] "v"(vK[0]), [k1] "v"(vK[1]), [k2] "v"(vK[2]), [k3] "v"(vK[3]), [vv] "v"(vV), [f0] "v"(oF[0]), [f1] "v"(oF[1]), [f2] "v"(oF[2]), [f3] "v"(oF[3]),
              [h0] "v"(oH[0]), [h1] "v"(oH[1]), [h2] "v"(oH[2]), [h3] "v"(oH[3]),
              [wl] "v"(wl), [adone] "v"(a_done), [agate] "v"(a_gate), [aland] "v"(a_landed)
            : "memory", "scc", "vcc", "s60", "s61", "s62", "s63", "s64", "s65", "s66", "s67", "s70", "s71", "s72", "s73", "s75", "s76", "s77", CLOB_V38_255);
        if (roleB) {
            const unsigned li = (unsigned)((idx * 4 + j) * 40 + (lane < 40 ? lane : 0)), row = li / 5, c = li - row * 5; unsigned d;
            asm volatile("global_load_dword %0, %1, %2\n\ts_waitcnt vmcnt(0)" : "=&v"(d) : "v"(row * 1152 + (c < 4 ? c * 128 : 1024)), "s"(wb) : "memory");
            wait_lds(lds + L_GATE, 12, budget);
            DMA4("", wb, oH[0], oH[1], oH[2], oH[3], lds0 + 8 * USLOT + j * 4096);
#pragma nounroll
            for (int k = 0; k < 8; ++k) { const int p = (b + k) & 7; DMA4("", wb + (size_t)p * 73728, oF[0], oF[1], oF[2], oF[3], lds0 + p * USLOT + j * 4096); }
            WAITV(0); lds_put(a_landed, (unsigned)(nkv + NSL));
        }
        if (j != 0) return;
        wait_lds(lds + L_CNT + 8, 1, budget);
        tagf = rfl64(*(volatile LAS const u64*)(lds + L_TAG));
    } else {
        const int rg = wid;
        __builtin_amdgcn_s_setprio(2);
        if (!roleB) {
            unsigned d[5];
            const unsigned i0 = (idx - 16) * 288 + rg * 72 + lane, i1 = i0 - lane + 64 + (lane & 7);
            unsigned oc = (rg * 64 + lane) * 128; oc = oc < code_len && (idx & 3) == 0 ? oc : 0;
            asm volatile("global_load_dword %0, %5, %10 nt\n\tglobal_load_dword %1, %6, %11\n\tglobal_load_dword %2, %7, %10 nt\n\tglobal_load_dword %3, %8, %11\n\t"
                         "s_waitcnt lgkmcnt(0)\n\ts_barrier\n\tglobal_load_dword %4, %9, %12\n\ts_waitcnt vmcnt(0)"
                         : "=&v"(d[0]), "=&v"(d[1]), "=&v"(d[2]), "=&v"(d[3]), "=&v"(d[4])
                         : "v"((i0 >> 3) * 8192 + ks * 1024 + (i0 & 7) * 128), "v"(i0 * 128), "v"((i1 >> 3) * 8192 + ks * 1024 + (i1 & 7) * 128), "v"(i1 * 128), "v"(oc),
                           "s"(Wq), "s"(X), "s"(pc0) : "memory");
        }
        const u64 epoch = __hip_atomic_load((GAS const u64*)epo, RLX_AGENT);
        u64 tag;
        const int q4 = (lane & 15) >> 2, p4 = lane & 3, blk = (lane >> 4) & 1;
        int u = 0, avail = 0;
        unsigned so = lds0;
        unsigned a_landed = lds0 + L_LANDED, a_done = lds0 + L_DONE + 4 * rg; PIN(a_landed); PIN(a_done);
        auto acquire = [&]() AINL -> unsigned {
            avail = __builtin_amdgcn_readfirstlane(avail); budget = __builtin_amdgcn_readfirstlane(budget);
            asm volatile("s_cmp_gt_i32 %0, %2\n\ts_cbranch_scc0 .Las%=\n"
                         ".Lab%=:\n\t.subsection 1\n"
                         ".Las%=:\n\tds_read_b128 v[140:143], %3\n\ts_waitcnt lgkmcnt(0)\n\tv_min_u32 v142, v142, v143\n\tv_min3_u32 v140, v140, v141, v142\n\ts_nop 1\n\tv_readfirstlane_b32 %0, v140\n\ts_nop 3\n\t"
                         "s_cmp_gt_i32 %0, %2\n\ts_cbranch_scc1 .Lab%=\n\ts_sub_i32 %1, %1, 1\n\ts_cmp_lt_i32 %1, 1\n\ts_cbranch_scc1 .Lab%=\n\ts_sleep 1\n\ts_branch .Las%=\n\t.subsection 0"
                         : "+&s"(avail), "+&s"(budget) : "s"(__builtin_amdgcn_readfirstlane(u)), "v"(a_landed) : "memory", "scc", "v140", "v141", "v142", "v143");
            const unsigned o = so; ++u; so = so == lds0 + (NSL - 1) * USLOT ? lds0 : so + USLOT; return o; };
        auto release = [&](int c) AINL { lds_put(a_done, (unsigned)c); };
        auto signoff = [&](int cnt_idx, char* flag) AINL { WAITV(0);
            unsigned old = 0; if (lane == 0) old = __hip_atomic_fetch_add((LAS unsigned*)(lds + L_CNT + 4 * cnt_idx), 1u, RLX_WG);
            if (__builtin_amdgcn_readfirstlane(old) == 3 && lane == 0) __hip_atomic_store((GAS u64*)flag, tag, RLX_AGENT); };
        auto opengate = [&](unsigned n) AINL { asm volatile("" ::: "memory"); if (lane == 0) __hip_atomic_fetch_add((LAS unsigned*)(lds + L_GATE), n, RLX_WG); };
        const unsigned krd = (unsigned)(256 * r32 + 16 * (hi ^ (r32 & 15)));
        bf16x8 qf[8], kf[8]; unsigned ka[8];
#define KRD(s, off) kf[s] = ldsrd16(LP(ka[s]) + (off))
#define KADDR(s, o) do { ka[s] = (krd ^ ((s) << 5)) + (o); PIN(ka[s]); } while (0)
#define KFIRST() do { const unsigned ko_ = acquire(); _Pragma("unroll") for (int s_ = 0; s_ < 8; ++s_) { KADDR(s_, ko_); KRD(s_, 0); } } while (0)
        const unsigned fx = (unsigned)((pair * 4 + rg) * 8192 + lane * 16);
        if (roleB) {
            const int p4s = ((p4 & 1) << 1) | (p4 >> 1);
            unsigned wrd[4], xa[4];
#pragma unroll
            for (int dt = 0; dt < 4; ++dt) wrd[dt] = (unsigned)((rg >> 1) * 256 + 512 * (8 * hi + q4) + 16 * ((4 * dt + 2 * blk + (p4s >> 1)) ^ (q4 << 2)) + 8 * (p4s & 1));
            { const int row = (rg & 1) * 32 + r32;
#pragma unroll
              for (int s = 0; s < 4; ++s) xa[s] = (unsigned)(row * 128 + 16 * ((2 * s + hi) ^ ((row >> 1) & 7))); }
            f32x16 acc[4] = {};
            bf16x8 xf[4], xn[4], wf[8];
#define WRD(i, wo) wf[i] = trrd2(LP((wo) + wrd[(i) & 3]) + ((i) >> 2) * 8192, LP((wo) + wrd[(i) & 3]) + ((i) >> 2) * 8192 + 2048)
#define XRD(s, xo) ldsrd16(LP((xo) + xa[s]))
#define QG(i, wi, wo, X_) do { acc[(i) & 3] = MFMA32(wf[i], xf[2 * (wi) + ((i) >> 2)], acc[(i) & 3]); PIN(acc[(i) & 3]); SBAR(); if ((i) == 0) BAR(); WRD(i, wo); X_; SBAR(); } while (0)
            unsigned sb = lds0;
            asm volatile("s_waitcnt lgkmcnt(0)\n\ts_barrier" ::: "memory");
#pragma unroll
            for (int s = 0; s < 4; ++s) xf[s] = XRD(s, sb);
#pragma unroll
            for (int i = 0; i < 8; ++i) WRD(i, sb + USLOT);
            LGKM(0);
#define QODDPH() do { const unsigned wo = sb + 2 * USLOT; SBAR(); \
                QG(0, 0, wo, (void)0); QG(1, 0, wo, (void)0); QG(2, 0, wo, (void)0); QG(3, 0, wo, (void)0); QG(4, 0, wo, (void)0); QG(5, 0, wo, (void)0); QG(6, 0, wo, (void)0); QG(7, 0, wo, (void)0); } while (0)
#pragma nounroll
            for (int gi = 0; gi < 8; ++gi) {
                QODDPH();
                sb = sb == lds0 + 6 * USLOT ? lds0 : sb + 3 * USLOT;
                SBAR();
                QG(0, 1, sb + USLOT, xn[0] = XRD(0, sb)); QG(1, 1, sb + USLOT, xn[1] = XRD(1, sb)); QG(2, 1, sb + USLOT, (void)0); QG(3, 1, sb + USLOT, (void)0);
                QG(4, 1, sb + USLOT, xn[2] = XRD(2, sb)); QG(5, 1, sb + USLOT, (void)0); QG(6, 1, sb + USLOT, (void)0); QG(7, 1, sb + USLOT, xn[3] = XRD(3, sb));
                xf[0] = xn[0]; xf[1] = xn[1]; xf[2] = xn[2]; xf[3] = xn[3];
            }
            QODDPH();
#pragma unroll
            for (int dt = 0; dt < 4; ++dt) { acc[dt] = MFMA32(wf[dt], xf[2], acc[dt]); acc[dt] = MFMA32(wf[4 + dt], xf[3], acc[dt]); }
            tag = next_tag(rfl64(epoch));
#pragma unroll
            for (int f = 0; f < 8; ++f) { const f32x16& a = acc[f >> 1]; const int r = 8 * (f & 1);
                const u32x4 w = {pk2(a[r] * QSCALE, a[r + 1] * QSCALE), pk2(a[r + 2] * QSCALE, a[r + 3] * QSCALE), pk2(a[r + 4] * QSCALE, a[r + 5] * QSCALE), pk2(a[r + 6] * QSCALE, a[r + 7] * QSCALE)};
                qf[f] = __builtin_bit_cast(bf16x8, w);
                __builtin_amdgcn_raw_buffer_store_b128(w, wsr, (unsigned)WS_QX + fx + f * 1024, 0, AUX_SC1); }
            opengate(1);
            signoff(0, ctl + 256); opengate(1);
            KFIRST();
        } else {
            tag = next_tag(rfl64(epoch));
            KFIRST();
            poll_flags(ctl + 256, tag, budget);
#pragma unroll
            for (int s = 0; s < 8; ++s) qf[s] = __builtin_bit_cast(bf16x8, __builtin_amdgcn_raw_buffer_load_b128(wsr, (unsigned)WS_QX + fx + s * 1024, 0, AUX_SC1));
            opengate(2);
        }
        f32x16 O0 = {}, O1 = {}, O2 = {}, O3 = {}, negm, SA, SB; float lsum = 0.f, mrow, ls0, ls1, mx0, mx1; u32x4 pw0, pw1, pk_; unsigned va[4]; s16x4 vh[16];
#define R_O0 "{v[192:207]}"
#define R_O1 "{v[208:223]}"
#define R_O2 "{v[224:239]}"
#define R_O3 "{v[240:255]}"
#define R_SA "{v[176:191]}"
#define R_SB "{v[160:175]}"
#define N_SA "176"
#define N_SB "160"
#define R_NM "{v[144:159]}"
#define PINR(x) asm volatile("" : "+" R_##x(x))
#define EXP2(x) __builtin_amdgcn_exp2f(x)
#define MX3(a, b, c) __builtin_fmaxf(__builtin_fmaxf((a), (b)), (c))
#define VRDH(g, t, off) vh[2 * (g) + (t)] = trrd(LP(va[(g) & 3]) + (off) + ((g) >> 2) * 4096 + (t) * 2048)
#define V2(g, vo) do { VRDH(g, 0, vo); VRDH(g, 1, vo); } while (0)
#define VF(g) __builtin_shufflevector(vh[2 * (g)], vh[2 * (g) + 1], 0, 1, 2, 3, 4, 5, 6, 7)
#define KSTEP(s, d) do { ka[s] += (d); PIN(ka[s]); } while (0)
#define VSTEP(c, d) do { va[c] += (d); PIN(va[c]); } while (0)
#define RESCALE(S, mx, mxb) do { float t0_; asm volatile( \
            "v_max_f32 %8, %8, %11\n\tv_cmp_lt_f32 vcc, %10, %8\n\ts_cbranch_vccnz .Lrs%=\n" \
            ".Lrb%=:\n\t.subsection 1\n" \
            ".Lrs%=:\n\ts_nop 15\n\tv_mov_b32 %9, %8\n\ts_nop 1\n\tv_permlane32_swap_b32 %8, %9\n\ts_nop 1\n\tv_max3_f32 %8, %8, %9, 0\n\t" \
            "v_add_f32 %6, %6, %8\n\tv_exp_f32 %9, -%8\n\t" \
            ".set r_, 0\n\t.rept 16\n\tv_sub_f32 v[" N_##S "+r_], v[" N_##S "+r_], %8\n\tv_sub_f32 v[144+r_], 0, %6\n\t.set r_, r_+1\n\t.endr\n\t" \
            "v_mul_f32 %7, %7, %9\n\t" \
            ".set r_, 192\n\t.rept 64\n\tv_mul_f32 v[r_], v[r_], %9\n\t.set r_, r_+1\n\t.endr\n\ts_nop 1\n\ts_branch .Lrb%=\n\t.subsection 0" \
            : "+" R_O0(O0), "+" R_O1(O1), "+" R_O2(O2), "+" R_O3(O3), "+" R_##S(S), "+" R_NM(negm), "+v"(mrow), "+v"(lsum), "+v"(mx), "=&v"(t0_) : "s"(THR), "v"(mxb) : "vcc"); } while (0)
#define G1(SN, SO, s, CIN, PK, KL) do { SN = MFMA32(kf[s], qf[s], CIN); PINR(SN); SBAR(); PINR(SO); KL; SO[2 * (s)] = EXP2(SO[2 * (s)]); SO[2 * (s) + 1] = EXP2(SO[2 * (s) + 1]); PK; PINR(SO); SBAR(); } while (0)
#define PKW(SO, W, i, r) do { W[i] = pk2(SO[r], SO[(r) + 1]); PIN(W); } while (0)
#define PH1(SN, SO, KL) do { SBAR(); \
        G1(SN, SO, 0, negm, (void)0, KL(0));          G1(SN, SO, 1, SN, PKW(SO, pw0, 0, 0), KL(1)); G1(SN, SO, 2, SN, PKW(SO, pw0, 1, 2), KL(2));  G1(SN, SO, 3, SN, PKW(SO, pw0, 2, 4), KL(3)); \
        G1(SN, SO, 4, SN, PKW(SO, pw0, 3, 6), KL(4)); G1(SN, SO, 5, SN, PKW(SO, pw1, 0, 8), KL(5)); G1(SN, SO, 6, SN, PKW(SO, pw1, 1, 10), KL(6)); G1(SN, SO, 7, SN, PKW(SO, pw1, 2, 12), KL(7)); } while (0)
#define G2(g, OC, W, X_) do { OC = MFMA32(VF(g), __builtin_bit_cast(bf16x8, W), OC); PINR(OC); SBAR(); X_; SBAR(); } while (0)
#define ADD4(SO, r) do { ls0 += SO[r]; ls1 += SO[(r) + 1]; ls0 += SO[(r) + 2]; ls1 += SO[(r) + 3]; PIN(ls0); PIN(ls1); } while (0)
#define PH2(SN, SO, vo, A0, A1, A2, A3) do { SBAR(); \
        G2(0, O0, pw0, { V2(0, vo); PKW(SO, pw1, 3, 14); ls0 = lsum + SO[0]; ls1 = SO[1] + SO[3]; ls0 += SO[2]; PIN(ls0); PIN(ls1); }); G2(1, O1, pw0, { V2(1, vo); ADD4(SO, 4); }); \
        G2(2, O2, pw0, { V2(2, vo); ADD4(SO, 8); }); G2(3, O3, pw0, { V2(3, vo); ADD4(SO, 12); }); \
        G2(4, O0, pw1, { V2(4, vo); mx0 = MX3(SN[0], SN[1], SN[2]); mx1 = MX3(SN[3], SN[4], SN[5]); lsum = ls0 + ls1; PIN(mx0); PIN(mx1); PIN(lsum); A0; }); \
        G2(5, O1, pw1, { V2(5, vo); mx0 = MX3(mx0, SN[6], SN[7]); mx1 = MX3(mx1, SN[8], SN[9]); PIN(mx0); PIN(mx1); A1; }); \
        G2(6, O2, pw1, { V2(6, vo); mx0 = MX3(mx0, SN[10], SN[11]); mx1 = MX3(mx1, SN[12], SN[13]); PIN(mx0); PIN(mx1); A2; }); \
        G2(7, O3, pw1, { V2(7, vo); mx0 = MX3(mx0, SN[14], SN[15]); A3; }); \
        RESCALE(SN, mx0, mx1); } while (0)
#define KL0(s) KRD(s, 0)
#define KL1(s) KRD(s, 8192)
#define KLN(s) (void)0
#define NOP_ (void)0
#define PEEK() do { pk_ = *(volatile LAS const u32x4*)LP(a_landed); } while (0)
#define SEEN() do { unsigned m_ = pk_[0] < pk_[1] ? pk_[0] : pk_[1], n_ = pk_[2] < pk_[3] ? pk_[2] : pk_[3]; const int a_ = __builtin_amdgcn_readfirstlane(m_ < n_ ? m_ : n_); avail = a_ > avail ? a_ : avail; } while (0)
        {
            SA = MFMA32(kf[0], qf[0], (f32x16){});
#pragma unroll
            for (int s = 1; s < 8; ++s) SA = MFMA32(kf[s], qf[s], SA);
#pragma unroll
            for (int s = 0; s < 8; ++s) KRD(s, 8192);
            float mx = MX3(SA[0], SA[1], SA[2]);
#pragma unroll
            for (int r = 3; r < 15; r += 2) mx = MX3(mx, SA[r], SA[r + 1]);
            mx = swap_max(__builtin_fmaxf(mx, SA[15])); mrow = mx;
#pragma unroll
            for (int r = 0; r < 16; ++r) { SA[r] -= mx; negm[r] = -mx; }
            asm volatile("" : "+" R_NM(negm));
        }
        unsigned vcur = acquire(), kcur = so;
#pragma unroll
        for (int c = 0; c < 4; ++c) { va[c] = (unsigned)(256 * (4 * hi + q4) + 16 * ((4 * c + 2 * blk + (p4 >> 1)) ^ (q4 << 2)) + 8 * (p4 & 1)) + vcur; PIN(va[c]); }
#pragma unroll
        for (int g = 0; g < 8; ++g) V2(g, 0);
#pragma unroll
        for (int s = 0; s < 8; ++s) KADDR(s, kcur);
        unsigned tprev = 0, gap = 0; int per = roleB ? PB0 : PA0;
#pragma nounroll
        for (int t = 0; t < NT - 1; ++t) {
            (void)acquire();
            if (roleB || t <= PEND) {
                unsigned now = (unsigned)__builtin_amdgcn_s_memrealtime();
#pragma nounroll
                for (int k = 0; k < 256 && now - tprev < gap; ++k) { __builtin_amdgcn_s_sleep(1); now = (unsigned)__builtin_amdgcn_s_memrealtime(); }
                tprev = now; gap = (unsigned)per; const int d_ = roleB ? PB1 : PA1, m_ = roleB ? PMINB : PMINA; per = per - d_ > m_ ? per - d_ : m_;
            }
            LGKM(15);
            PH1(SB, SA, KL0);
            LGKM(8);
            PH2(SB, SA, 8192, PEEK(), NOP_, NOP_, NOP_);
            LGKM(15);
            PH1(SA, SB, KL1);
            SEEN();
            release(u - 1);
            const unsigned vo = acquire();
            const unsigned dv = vo - vcur, dk = so - kcur; vcur = vo; kcur = so;
            VSTEP(0, dv); VSTEP(1, dv); VSTEP(2, dv); VSTEP(3, dv);
            LGKM(8);
            PH2(SA, SB, 0, { KSTEP(0, dk); KSTEP(1, dk); }, { KSTEP(2, dk); KSTEP(3, dk); }, { KSTEP(4, dk); KSTEP(5, dk); }, { KSTEP(6, dk); KSTEP(7, dk); });
        }
        u64 fpv = 0;
        if (!roleB) fpv = __hip_atomic_load((GAS const u64*)(ctl + 512), RLX_AGENT);
        PH1(SB, SA, KLN);
        release(u - 1);
        u32x4 pf[8]; float plse = 0.f; bool early = false;
#define PLOAD() do { _Pragma("unroll") for (int i = 0; i < 8; ++i) pf[i] = __builtin_amdgcn_raw_buffer_load_b128(wsr, (unsigned)WS_PX + fx + i * 1024, 0, AUX_SC1); \
            plse = __uint_as_float(__builtin_amdgcn_raw_buffer_load_b32(wsr, (unsigned)WS_PL + (pair * 4 + rg) * 256 + lane * 4, 0, AUX_SC1)); } while (0)
        if (!roleB && rfl64(fpv) == tag) { early = true; asm volatile("" ::: "memory"); PLOAD(); }
        PH2(SB, SA, 8192, NOP_, NOP_, NOP_, NOP_);
        {
#pragma unroll
            for (int r = 0; r < 16; ++r) { SB[r] = EXP2(SB[r]); lsum += SB[r]; }
            pw0 = (u32x4){pk2(SB[0], SB[1]), pk2(SB[2], SB[3]), pk2(SB[4], SB[5]), pk2(SB[6], SB[7])};
            pw1 = (u32x4){pk2(SB[8], SB[9]), pk2(SB[10], SB[11]), pk2(SB[12], SB[13]), pk2(SB[14], SB[15])};
            const bf16x8 p0 = __builtin_bit_cast(bf16x8, pw0), p1 = __builtin_bit_cast(bf16x8, pw1);
            O0 = MFMA32(VF(0), p0, O0); O1 = MFMA32(VF(1), p0, O1); O2 = MFMA32(VF(2), p0, O2); O3 = MFMA32(VF(3), p0, O3);
            O0 = MFMA32(VF(4), p1, O0); O1 = MFMA32(VF(5), p1, O1); O2 = MFMA32(VF(6), p1, O2); O3 = MFMA32(VF(7), p1, O3);
        }
        release(u);
        lsum = swap_add(lsum);
        const float lse = mrow + __builtin_amdgcn_logf(lsum);
        if (!roleB) {
            if (!early) { poll_flags(ctl + 512, tag, budget); PLOAD(); }
            const float M = __builtin_fmaxf(lse, plse), eo = EXP2(lse - M), ep = EXP2(plse - M), r = __builtin_amdgcn_rcpf(eo + ep);
            const float fo = eo * r * __builtin_amdgcn_rcpf(lsum), fp = ep * r;
#pragma unroll
            for (int f = 0; f < 8; ++f) { const f32x16& a = f < 2 ? O0 : f < 4 ? O1 : f < 6 ? O2 : O3; const int q = 8 * (f & 1); u32x4 w;
#pragma unroll
                for (int e = 0; e < 4; ++e) w[e] = pk2(__builtin_fmaf(a[q + 2 * e], fo, fp * bflo(pf[f][e])), __builtin_fmaf(a[q + 2 * e + 1], fo, fp * bfhi(pf[f][e])));
                __builtin_amdgcn_raw_buffer_store_b128(w, wsr, (unsigned)WS_OX + fx + f * 1024, 0, AUX_SC1); }
        } else {
            const float inv = __builtin_amdgcn_rcpf(lsum);
#pragma unroll
            for (int f = 0; f < 8; ++f) { const f32x16& a = f < 2 ? O0 : f < 4 ? O1 : f < 6 ? O2 : O3; const int r = 8 * (f & 1);
                const u32x4 w = {pk2(a[r] * inv, a[r + 1] * inv), pk2(a[r + 2] * inv, a[r + 3] * inv), pk2(a[r + 4] * inv, a[r + 5] * inv), pk2(a[r + 6] * inv, a[r + 7] * inv)};
                __builtin_amdgcn_raw_buffer_store_b128(w, wsr, (unsigned)WS_PX + fx + f * 1024, 0, AUX_SC1); }
            __builtin_amdgcn_raw_buffer_store_b32(__float_as_uint(lse), wsr, (unsigned)WS_PL + (pair * 4 + rg) * 256 + lane * 4, 0, AUX_SC1);
        }
        signoff(1, roleB ? ctl + 512 : ctl + 768);
        if (roleB) opengate(1);
        const int qh = rg & 1, nh = rg >> 1;
        *(volatile LAS u64*)(lds + L_TAG) = tag;
        u += NSL - 1; (void)acquire();
        unsigned wa[4], wh = lds0 + 131072 + (nh * 64 + hi) * 128 + 32 * q4 + 128 * blk + 8 * p4; PIN(wh);
#pragma unroll
        for (int c = 0; c < 4; ++c) { wa[c] = lds0 + ((((nh ^ (c >> 1)) * 64 + hi) * 4 + q4) * 256 + 64 * ((2 * nh + (c & 1)) ^ q4) + 32 * blk + 8 * p4); PIN(wa[c]); }
#define AKS(i) ((i) < 48 ? (i) / 3 : 16 + ((i) - 48) / 2)
#define ATL(i) ((i) < 48 ? (i) % 3 : ((i) - 48) % 2)
#define AWA(i) wa[((i) < 48 ? 0 : 2) + (ATL(i) & 1)]
#define ARD(i) (ATL(i) == 2 ? trrd2(LP(wh) + AKS(i) * 512, LP(wh) + AKS(i) * 512 + 256) : trrd2(LP(AWA(i)) + (AKS(i) & 15) * 4096, LP(AWA(i)) + (AKS(i) & 15) * 4096 + 2048))
        constexpr int AD = 7, OPRE = 10;
        bf16x8 af[AD], of[32];
#pragma unroll
        for (int i = 0; i < AD; ++i) af[i] = ARD(i);
        const int pr0 = b * HKV + ks * 2 + nh;
        poll_flags(ws + WS_CT + (size_t)(pr0 ^ (lane & 1)) * 1024 + 768, tag, budget);
        const unsigned ob0 = (unsigned)WS_OX + (pr0 * 4 + qh) * 8192 + lane * 16, ob1 = (unsigned)WS_OX + ((pr0 ^ 1) * 4 + qh) * 8192 + lane * 16;
#define OLD(f) of[f] = __builtin_bit_cast(bf16x8, __builtin_amdgcn_raw_buffer_load_b128(wsr, ((f) < 16 ? ob0 : ob1) + ((f) & 3) * 1024, (((f) >> 3) & 1) * 16384 + (((f) >> 2) & 1) * 4096, AUX_SC1))
#pragma unroll
        for (int f = 0; f < OPRE; ++f) OLD(f);
        f32x16 acc[3] = {};
        SBAR();
#pragma unroll
        for (int i = 0; i < 80; ++i) { acc[ATL(i)] = MFMA32(af[i % AD], of[AKS(i)], acc[ATL(i)]); PIN(acc[ATL(i)]); SBAR(); if (i + AD < 80) af[i % AD] = ARD(i + AD);
            if ((i & 1) == 0 && OPRE + i / 2 < 32) OLD(OPRE + i / 2); SBAR(); }
        const unsigned sb = (unsigned)WS_SLAB + ((b * 4 + nq) * 8 + (r32 >> 2)) * 20480 + ks * 2560 + ((lane & 3) * 2 + hi) * 16;
#pragma unroll
        for (int k = 0; k < 5; ++k) { const int kk = k == 0 ? 4 : k - 1; const f32x16& a = acc[kk >> 1]; const int r = 8 * (kk & 1);
            const u32x4 v = {pk2(a[r], a[r + 1]), pk2(a[r + 2], a[r + 3]), pk2(a[r + 4], a[r + 5]), pk2(a[r + 6], a[r + 7])};
            __builtin_amdgcn_raw_buffer_store_b128(v, wsr, sb + (kk < 4 ? (qh * 8 + nh * 4 + kk) * 128 : 2048 + (nh * 2 + qh) * 128), 0, AUX_SC1); }
        signoff(2, fsl + ks * 256);
        tagf = tag;
    }
    {
        poll_flags(fsl + (lane & 7) * 256, tagf, budget);
        const int e = wid * 32 + r32;
        const unsigned sb = (unsigned)WS_SLAB + ((b * 4 + nq) * 8 + ks) * 20480 + hi * 10240 + e * 16;
        u32x4 v[4];
#pragma unroll
        for (int k = 0; k < 4; ++k) v[k] = __builtin_amdgcn_raw_buffer_load_b128(wsr, sb + k * 2560, 0, AUX_SC1);
        if (ks == 0 && nq == 0 && tid == 0) __hip_atomic_store((GAS u64*)epo, tagf, RLX_AGENT);
        float s[8], t[4];
#pragma unroll
        for (int c = 0; c < 4; ++c) { s[2 * c] = (bflo(v[0][c]) + bflo(v[1][c])) + (bflo(v[2][c]) + bflo(v[3][c])); s[2 * c + 1] = (bfhi(v[0][c]) + bfhi(v[1][c])) + (bfhi(v[2][c]) + bfhi(v[3][c])); }
#pragma unroll
        for (int c = 0; c < 4; ++c) { auto rr = __builtin_amdgcn_permlane32_swap(__float_as_uint(s[c]), __float_as_uint(s[4 + c]), false, false); t[c] = __uint_as_float(rr[0]) + __uint_as_float(rr[1]); }
        int qh = e >> 6, col = nq * 128 + ((e >> 3) & 7) * 16;
        if (wid == 4) {
#pragma unroll
            for (int c = 0; c < 4; ++c) t[c] += __shfl_xor(t[c], 16);
            qh = (e >> 3) & 1; col = 512 + nq * 16; }
        if (e < 144) *(u32x2*)(out + ((size_t)(b * SQ + qh * 32 + ks * 4 + ((e >> 1) & 3)) * DM + col + hi * 8 + (e & 1) * 4) * 2) = (u32x2){pk2(t[0], t[1]), pk2(t[2], t[3])};
    }
}

extern "C" void kernel_launch(void* const* d_in, const int* in_sizes, int n_in, void* d_out, int out_size, void* d_ws, size_t ws_size, hipStream_t stream) {
    (void)hipFuncSetAttribute((const void*)gqa_block_k, hipFuncAttributeMaxDynamicSharedMemorySize, LDS_BYTES);
    hipLaunchKernelGGL(gqa_block_k, dim3(256), dim3(512), LDS_BYTES, stream, (const char*)d_in[0], (const char*)d_in[1], (const char*)d_in[2], (const char*)d_in[3],
                       (const char*)d_in[4], (char*)d_out, (char*)d_ws);
}
```

```cpp
#include <hip/hip_runtime.h>
#include <hip/hip_bf16.h>
#include <stdint.h>

typedef short bf16x8 __attribute__((ext_vector_type(8)));
typedef short s16x4 __attribute__((ext_vector_type(4)));
typedef float f32x16 __attribute__((ext_vector_type(16)));
typedef float f32x2 __attribute__((ext_vector_type(2)));
typedef unsigned u32x4 __attribute__((ext_vector_type(4)));
typedef unsigned u32x2 __attribute__((ext_vector_type(2)));
typedef __bf16 bf2_t __attribute__((ext_vector_type(2)));
typedef unsigned long long u64;
#define LAS __attribute__((address_space(3)))
#define GAS __attribute__((address_space(1)))
typedef LAS const char* lcptr;
typedef LAS char* lptr;
#define DI __device__ __forceinline__
#define AINL __attribute__((always_inline))
#define MFMA32(a, b, c) __builtin_amdgcn_mfma_f32_32x32x16_bf16((a), (b), (c), 0, 0, 0)
#define WAITV(N) asm volatile("s_waitcnt vmcnt(" #N ")" ::: "memory")
#define BAR() asm volatile("s_barrier" ::: "memory")
#define PIN(x) asm volatile("" : "+v"(x))
#define SBAR() __builtin_amdgcn_sched_barrier(0)
#define LGKM(n) do { __builtin_amdgcn_s_waitcnt(0xC07F | ((n) << 8)); SBAR(); } while (0)
#define LP(a) ((lcptr)(uintptr_t)(a))
#define STR2(x) #x
#define STR(x) STR2(x)
#define CLOB_V38_255 "v38", "v39", "v40", "v41", "v42", "v43", "v44", "v45", "v46", "v47", "v48", "v49", "v50", "v51", "v52", "v53", "v54", "v55", "v56", "v57", "v58", "v59", "v60", "v61", "v62", "v63", "v64", "v65", "v66", "v67", "v68", "v69", "v70", "v71", "v72", "v73", "v74", "v75", "v76", "v77", "v78", "v79", "v80", "v81", "v82", "v83", "v84", "v85", "v86", "v87", "v88", "v89", "v90", "v91", "v92", "v93", "v94", "v95", "v96", "v97", "v98", "v99", "v100", "v101", "v102", "v103", "v104", "v105", "v106", "v107", "v108", "v109", "v110", "v111", "v112", "v113", "v114", "v115", "v116", "v117", "v118", "v119", "v120", "v121", "v122", "v123", "v124", "v125", "v126", "v127", "v128", "v129", "v130", "v131", "v132", "v133", "v134", "v135", "v136", "v137", "v138", "v139", "v140", "v141", "v142", "v143", "v144", "v145", "v146", "v147", "v148", "v149", "v150", "v151", "v152", "v153", "v154", "v155", "v156", "v157", "v158", "v159", "v160", "v161", "v162", "v163", "v164", "v165", "v166", "v167", "v168", "v169", "v170", "v171", "v172", "v173", "v174", "v175", "v176", "v177", "v178", "v179", "v180", "v181", "v182", "v183", "v184", "v185", "v186", "v187", "v188", "v189", "v190", "v191", "v192", "v193", "v194", "v195", "v196", "v197", "v198", "v199", "v200", "v201", "v202", "v203", "v204", "v205", "v206", "v207", "v208", "v209", "v210", "v211", "v212", "v213", "v214", "v215", "v216", "v217", "v218", "v219", "v220", "v221", "v222", "v223", "v224", "v225", "v226", "v227", "v228", "v229", "v230", "v231", "v232", "v233", "v234", "v235", "v236", "v237", "v238", "v239", "v240", "v241", "v242", "v243", "v244", "v245", "v246", "v247", "v248", "v249", "v250", "v251", "v252", "v253", "v254", "v255"
#define RLX_AGENT __ATOMIC_RELAXED, __HIP_MEMORY_SCOPE_AGENT
#define RLX_WG __ATOMIC_RELAXED, __HIP_MEMORY_SCOPE_WORKGROUP
#define AUX_SC1 16

#define QN 2
#define QNOP asm volatile("s_nop " STR(QN))
#define NTA 18
#define GATEA 10
#define HPRE 2
#define PA0 110
#define PA1 2
#define PMINA 101
#define PB0 150
#define PB1 9
#define PMINB 106
#define PEND 13

constexpr int SQ = 64, DM = 576, HKV = 16, SK = 2048;
constexpr float QSCALE = 0.08838834764831845f * 1.4426950408889634f;
constexpr float THR = 8.f;
constexpr int NT_A = NTA, NT_B = 32 - NTA;
constexpr int USLOT = 16384, NSL = 9, MISC = NSL * USLOT, LDS_BYTES = 163840;
constexpr int L_LANDED = MISC, L_DONE = MISC + 16, L_CNT = MISC + 32, L_GATE = MISC + 44, L_TAG = MISC + 64;
constexpr int SPIN_CAP = 1 << 20;
constexpr size_t WS_CT = 0;
constexpr size_t WS_EP = 128 * 1024, WS_FS = WS_EP + 8 * 256;
constexpr size_t WS_QX = 1 << 20;
constexpr size_t WS_PX = WS_QX + ((size_t)4 << 20);
constexpr size_t WS_PL = WS_PX + ((size_t)4 << 20);
constexpr size_t WS_OX = WS_PL + ((size_t)1 << 20);
constexpr size_t WS_SLAB = WS_OX + ((size_t)4 << 20);
constexpr int WS_USED = 32 << 20;

DI unsigned pk2(float a, float b) { bf2_t r = __builtin_convertvector((f32x2){a, b}, bf2_t); return __builtin_bit_cast(unsigned, r); }
DI float bflo(unsigned v) { return __uint_as_float(v << 16); }
DI float bfhi(unsigned v) { return __uint_as_float(v & 0xffff0000u); }
DI s16x4 trrd(lcptr p) { typedef short v4i16_t __attribute__((ext_vector_type(4))); return __builtin_bit_cast(s16x4, __builtin_amdgcn_ds_read_tr16_b64_v4i16((LAS v4i16_t*)p)); }
DI bf16x8 trrd2(lcptr p0, lcptr p1) { s16x4 a = trrd(p0), b = trrd(p1); return __builtin_shufflevector(a, b, 0, 1, 2, 3, 4, 5, 6, 7); }
DI bf16x8 ldsrd16(lcptr p) { return *(LAS const bf16x8*)p; }
DI float swap_max(float m) { auto rr = __builtin_amdgcn_permlane32_swap(__float_as_uint(m), __float_as_uint(m), false, false); return __builtin_fmaxf(__uint_as_float(rr[0]), __uint_as_float(rr[1])); }
DI float swap_add(float m) { auto rr = __builtin_amdgcn_permlane32_swap(__float_as_uint(m), __float_as_uint(m), false, false); return __uint_as_float(rr[0]) + __uint_as_float(rr[1]); }
DI u64 next_tag(u64 e) { return e * 6364136223846793005ull + 1442695040888963407ull; }
DI u64 rfl64(u64 v) { return ((u64)(unsigned)__builtin_amdgcn_readfirstlane((unsigned)(v >> 32)) << 32) | (unsigned)__builtin_amdgcn_readfirstlane((unsigned)v); }
DI void lds_put(unsigned a, unsigned val) { asm volatile("ds_write_b32 %0, %1" :: "v"(a), "v"(val) : "memory"); }
DI void poll_flags(const char* p, u64 tag, int& budget) {
#pragma nounroll
    while (!__all(__hip_atomic_load((GAS const u64*)p, RLX_AGENT) == tag) && --budget > 0) __builtin_amdgcn_s_sleep(4);
    asm volatile("" ::: "memory"); }
DI void wait_lds(lcptr p, unsigned need, int& budget) {
#pragma nounroll
    while ((unsigned)__builtin_amdgcn_readfirstlane(*(volatile LAS const unsigned*)p) < need && --budget > 0) __builtin_amdgcn_s_sleep(1); }

#define DMA4(POL, sbase, v0, v1, v2, v3, ldst) do { unsigned sv_; asm volatile("s_mov_b32 %0, m0\n\ts_mov_b32 m0, %6\n\ts_nop 0\n\t" \
    "global_load_lds_dwordx4 %1, %5" POL "\n\tglobal_load_lds_dwordx4 %2, %5 offset:1024" POL "\n\tglobal_load_lds_dwordx4 %3, %5 offset:2048" POL "\n\tglobal_load_lds_dwordx4 %4, %5 offset:3072" POL "\n\t" \
    "s_mov_b32 m0, %0" : "=&s"(sv_) : "v"(v0), "v"(v1), "v"(v2), "v"(v3), "s"(sbase), "s"(__builtin_amdgcn_readfirstlane(ldst)) : "memory"); } while (0)

__global__ __launch_bounds__(512, 2) void gqa_block_k(const char* __restrict__ X, const char* __restrict__ Kc, const char* __restrict__ Vc, const char* __restrict__ Wq,
                                                      const char* __restrict__ Wo, char* __restrict__ out, char* __restrict__ ws) {
    extern __shared__ __attribute__((aligned(16))) char lds_[];
    const int tid = threadIdx.x, lane = tid & 63, r32 = lane & 31, hi = lane >> 5;
    const int wid = __builtin_amdgcn_readfirstlane(tid >> 6);
    const int bid = blockIdx.x, ks = bid & 7, idx = bid >> 3;
    const int kvh = ks * 2 + (idx & 1), b = (idx >> 1) & 7;
    const bool roleB = idx < 16;
    const int pair = b * HKV + kvh;
    const int nq = (idx & 1) + (roleB ? 0 : 2);
    const lptr lds = (lptr)lds_;
    const unsigned lds0 = (unsigned)(uintptr_t)lds;
    const int NT = roleB ? NT_B : NT_A;
    char* const ctl = ws + WS_CT + (size_t)pair * 1024;
    char* const epo = ws + WS_EP + b * 256;
    char* const fsl = ws + WS_FS + (size_t)(b * 4 + nq) * 8 * 256;
    const __amdgpu_buffer_rsrc_t wsr = __builtin_amdgcn_make_buffer_rsrc(ws, 0, WS_USED, 0x00020000);
    u64 pc0; unsigned code_len;
    asm volatile("s_getpc_b64 %0\n.Lcs%=:\n\ts_mov_b32 %1, .Lce%=-.Lcs%=\n\t.subsection 2\n.Lce%=:\n\t.fill 128, 4, 0xBF800000\n\t.subsection 0" : "=s"(pc0), "=s"(code_len));
    unsigned ctd;
    { unsigned oc = (unsigned)(idx * 8 + wid) * 128; oc = oc < code_len ? oc : 0;
      asm volatile("global_load_dword %0, %1, %2" : "={v37}"(ctd) : "v"(oc), "s"(pc0) : "memory"); }
    if (tid < 16) *(LAS unsigned*)(lds + MISC + tid * 4) = 0u;
    int budget = SPIN_CAP;
    u64 tagf;

    if (wid >= 4) {
        const int j = wid - 4, lr = lane >> 4, lc = lane & 15;
        const size_t koff = ((size_t)pair * SK + (roleB ? NT_A * 64 : 0)) * 256;
        const char* kp = Kc + koff; const char* vp = Vc + koff;
        unsigned vK[4];
#pragma unroll
        for (int i = 0; i < 4; ++i) vK[i] = (unsigned)((16 * j + lr) * 256 + 16 * (lc ^ (4 * i + lr)));
        const unsigned vV = (unsigned)((16 * j + lr) * 256 + 16 * (lc ^ (lr << 2)));
        unsigned a_landed = lds0 + L_LANDED + 4 * j, a_done = lds0 + L_DONE; PIN(a_landed); PIN(a_done);
        if (roleB) {
            unsigned vX[2], vW[4];
#pragma unroll
            for (int i = 0; i < 2; ++i) { const int row = 16 * j + 8 * i + (lane >> 3); vX[i] = (unsigned)(row * 1152 + 16 * ((lane & 7) ^ ((row >> 1) & 7)) - 1024 * i); }
#pragma unroll
            for (int i = 0; i < 4; ++i) vW[i] = (unsigned)((8 * j + 2 * i + hi) * 8192 + 16 * (r32 ^ (((2 * i + hi) & 3) << 2)) - 1024 * i);
            const char* xb = X + (size_t)b * SQ * DM * 2; const char* wb = Wq + kvh * 512;
            int xo = b * 128, sb = 0, cnt = 7;
#define QADV "s_add_i32 %[xo], %[xo], 128\n\ts_cmp_eq_u32 %[xo], 1152\n\ts_cselect_b32 %[xo], 0, %[xo]\n\ts_add_i32 %[sb], %[sb], 0xc000\n\ts_cmp_eq_u32 %[sb], 0x24000\n\ts_cselect_b32 %[sb], 0, %[sb]\n\t"
#define QW4 "global_load_lds_dwordx4 %[w0], s[60:61]\n\tglobal_load_lds_dwordx4 %[w1], s[60:61] offset:1024\n\tglobal_load_lds_dwordx4 %[w2], s[60:61] offset:2048\n\tglobal_load_lds_dwordx4 %[w3], s[60:61] offset:3072\n\t"
#define QEVEN0 "s_add_u32 s62, %[xl], %[xo]\n\ts_addc_u32 s63, %[xh], 0\n\ts_lshl_b32 s66, %[xo], 12\n\ts_add_u32 s60, %[wl], s66\n\ts_addc_u32 s61, %[wh], 0\n\ts_add_i32 m0, %[sb], %[dx]\n\ts_add_i32 s66, %[sb], %[dw]\n\t"
#define QEVEN1 "global_load_lds_dwordx4 %[x0], s[62:63]\n\tglobal_load_lds_dwordx4 %[x1], s[62:63] offset:1024\n\ts_mov_b32 m0, s66\n\ts_nop 0\n\t" QW4
#define QODD0 "s_add_u32 s60, s60, 0x40000\n\ts_addc_u32 s61, s61, 0\n\ts_add_i32 m0, m0, 0x4000\n\t"
            asm volatile(
                "s_mov_b32 s70, m0\n\t"
                QEVEN0 QEVEN1 QODD0 "s_nop 0\n\t" QW4 QADV QEVEN0 QEVEN1 QODD0
                "s_waitcnt vmcnt(10)\n"
                ".Lqr%=:\n\t"
                "s_barrier\n\t" QW4 QADV QEVEN0 "s_waitcnt vmcnt(10)\n\t"
                "s_barrier\n\t" QEVEN1 QODD0 "s_sub_i32 %[cnt], %[cnt], 1\n\ts_cmp_gt_i32 %[cnt], 0\n\ts_waitcnt vmcnt(10)\n\ts_cbranch_scc1 .Lqr%=\n\t"
                "s_barrier\n\t" QW4 "s_waitcnt vmcnt(10)\n\t"
                "s_barrier\n\ts_waitcnt vmcnt(4)\n\t"
                "s_barrier\n\ts_waitcnt vmcnt(0)\n\t"
                "s_barrier\n\ts_mov_b32 m0, s70"
                : [xo] "+&s"(xo), [sb] "+&s"(sb), [cnt] "+&s"(cnt)
                : [xl] "s"((unsigned)(uintptr_t)xb), [xh] "s"((unsigned)((uintptr_t)xb >> 32)), [wl] "s"((unsigned)(uintptr_t)wb), [wh] "s"((unsigned)((uintptr_t)wb >> 32)),
                  [dx] "s"(lds0 + j * 2048), [dw] "s"(lds0 + USLOT + j * 4096),
                  [x0] "v"(vX[0]), [x1] "v"(vX[1]), [w0] "v"(vW[0]), [w1] "v"(vW[1]), [w2] "v"(vW[2]), [w3] "v"(vW[3])
                : "memory", "scc", "s60", "s61", "s62", "s63", "s66", "s70");
        } else {
            DMA4(" nt", kp, vK[0], vK[1], vK[2], vK[3], lds0 + j * 4096); DMA4(" nt", vp, vV, vV, vV, vV, lds0 + USLOT + j * 4096);
            BAR();
        }
        const char* wb = Wo + (size_t)ks * 512 * 1152;
        unsigned oF[4], oH[4];
#pragma unroll
        for (int i = 0; i < 4; ++i) { oF[i] = (unsigned)(((4 * j + i) * 4 + lr) * 1152 + nq * 256 + 16 * (lc ^ (lr << 2)) - 1024 * i);
            oH[i] = (unsigned)((j * 128 + i * 32 + (lane >> 1)) * 1152 + 1024 + nq * 32 + (lane & 1) * 16 - 1024 * i); }
        const unsigned wl = (unsigned)(lds0 + j * 4096 + lane * 16), a_gate = lds0 + L_GATE;
        const int nkv = 2 * NT, ntot = roleB ? nkv : nkv + NSL, n0 = roleB ? 0 : 2;
        int ni = n0, nl = 0, nw = 0, bi = 0, bw = 0, gate = roleB ? 0 : GATEA, si = n0 * USLOT, sw = 0, lim = NSL, wc = roleB ? 9 : 0;
        static_assert(GATEA >= NSL && GATEA <= NSL + 4, "four register buffers");
#define WQUOTA(L) "s_sub_i32 s67, %[ni], " STR(GATEA) "\n\ts_lshr_b32 s67, s67, 1\n\ts_add_i32 s67, s67, 5\n\ts_cmp_ge_i32 %[wc], s67\n\ts_cbranch_scc1 " L "\n\t"
        asm volatile(
            "s_mov_b32 s70, m0\n\ts_mov_b64 s[72:73], 0\n\ts_mov_b32 s75, 0\n\ts_getpc_b64 s[64:65]\n"
            ".Lp%=:\n\t"
            "s_branch .Lprep%=\n"
            ".Ltop%=:\n\t"
            "s_sub_i32 s66, %[ni], %[nl]\n\ts_cmp_lt_i32 s66, 3\n\ts_cbranch_scc1 .Lc1%=\n\t"
            "s_waitcnt vmcnt(8)\n\ts_add_i32 %[nl], %[nl], 1\n\t"
            ".Lc1%=:\n\t"
            "s_waitcnt lgkmcnt(0)\n\tv_min_u32 v42, v42, v43\n\tv_min3_u32 v40, v40, v41, v42\n\ts_mov_b32 s71, 0\n\ts_cmp_lt_i32 %[nw], %[nl]\n\tv_readfirstlane_b32 s67, v40\n\t"
            "s_cbranch_scc0 .Lc2%=\n\t"
            "s_bitcmp1_b64 s[72:73], %[nw]\n\ts_cbranch_scc1 .Lwr%=\n"
            ".Lpb%=:\n\t"
            "s_add_i32 %[nw], %[nw], 1\n\tv_mov_b32 v38, %[nw]\n\tds_write_b32 %[aland], v38\n\t"
            "s_add_i32 %[sw], %[sw], 0x4000\n\ts_cmp_eq_u32 %[sw], 0x24000\n\ts_cselect_b32 %[sw], 0, %[sw]\n\ts_mov_b32 s71, 1\n\t"
            "s_cmp_lt_i32 %[nw], %[nl]\n\ts_cbranch_scc1 .Lmore%=\n"
            ".Lc2%=:\n\t"
            "s_add_i32 s67, s67, 9\n\ts_max_i32 %[lim], %[lim], s67\n\t"
            "s_cmp_ge_i32 %[ni], %[gate]\n\ts_cbranch_scc1 .Lno%=\n\t"
            "s_cmp_lt_i32 %[ni], %[lim]\n\ts_cbranch_scc0 .Lis%=\n\t"
            "global_load_lds_dwordx4 v44, s[60:61] nt\n\tglobal_load_lds_dwordx4 v45, s[60:61] offset:1024 nt\n\t"
            "global_load_lds_dwordx4 v46, s[60:61] offset:2048 nt\n\tglobal_load_lds_dwordx4 v47, s[60:61] offset:3072 nt\n"
            ".Lbk%=:\n\t"
            "s_add_i32 %[ni], %[ni], 1\n\ts_mov_b32 s75, 1\n\t"
            "s_add_i32 %[si], %[si], 0x4000\n\ts_cmp_eq_u32 %[si], 0x24000\n\ts_cselect_b32 %[si], 0, %[si]\n"
            ".Lprep%=:\n\t"
            "ds_read_b128 v[40:43], %[adone]\n\t"
            "s_add_i32 m0, %[si], %[dbase]\n\t"
            "s_lshr_b32 s66, %[ni], 1\n\ts_lshl_b32 s66, s66, 14\n\t"
            "s_bitcmp1_b32 %[ni], 0\n\ts_cselect_b32 s60, %[vpl], %[kpl]\n\ts_cselect_b32 s61, %[vph], %[kph]\n\ts_cselect_b64 vcc, -1, 0\n\t"
            "s_add_u32 s60, s60, s66\n\ts_addc_u32 s61, s61, 0\n\t"
            "v_cndmask_b32 v44, %[k0], %[vv], vcc\n\tv_cndmask_b32 v45, %[k1], %[vv], vcc\n\tv_cndmask_b32 v46, %[k2], %[vv], vcc\n\tv_cndmask_b32 v47, %[k3], %[vv], vcc\n\t"
            "s_branch .Ltop%=\n"
            ".Lmore%=:\n\ts_bitcmp1_b64 s[72:73], %[nw]\n\ts_cbranch_scc1 .Lwr%=\n\ts_branch .Lpb%=\n"
            ".Lno%=:\n\t"
            "s_cmp_ge_i32 %[gate], %[nkv]\n\ts_cbranch_scc1 .Lno2%=\n\t"
            "ds_read_b32 v40, %[agate]\n\ts_waitcnt lgkmcnt(0)\n\tv_readfirstlane_b32 s67, v40\n\ts_nop 3\n\t"
            "s_cmp_ge_u32 s67, 4\n\ts_cselect_b32 %[gate], %[g1], %[gate]\n\ts_cmp_ge_u32 s67, 8\n\ts_cselect_b32 %[gate], %[nkv], %[gate]\n\t"
            "s_cmp_lt_i32 %[ni], %[gate]\n\ts_cselect_b32 s71, 1, s71\n"
            ".Lno2%=:\n\t"
            "ds_read_b128 v[40:43], %[adone]\n\t"
            "s_cmp_lg_u32 s71, 0\n\ts_cbranch_scc1 .Ltop%=\n\t"
            "s_sub_i32 s66, %[ni], %[nl]\n\ts_cmp_eq_u32 s66, 0\n\ts_cbranch_scc1 .Lno3%=\n\t"
            "s_cmp_eq_u32 s66, 2\n\ts_cbranch_scc1 .Lw4%=\n\ts_waitcnt vmcnt(0)\n"
            ".Lw4%=:\n\ts_waitcnt vmcnt(4)\n\ts_add_i32 %[nl], %[nl], 1\n\ts_branch .Lc1%=\n"
            ".Lno3%=:\n\ts_cmp_ge_i32 %[nw], %[ntot]\n\ts_cbranch_scc1 .Lend%=\n\t"
            "s_cmp_lt_i32 %[gate], %[nkv]\n\ts_cbranch_scc1 .Lidle%=\n\t"
            "s_cmp_lt_i32 %[wc], 9\n\ts_cbranch_scc1 .Lwo0%=\n\t"
            "s_cmp_eq_u32 %[ni], %[nkv]\n\ts_cbranch_scc0 .Lidle%=\n\ts_cmp_eq_u32 %[ni], %[ntot]\n\ts_cbranch_scc1 .Lidle%=\n\t"
            "s_cmp_lt_i32 %[lim], %[ntot]\n\ts_cbranch_scc1 .Lidle%=\n\t"
            "v_add_u32 v39, 0x10000, %[wl]\n\tv_add_u32 v38, 0x20000, %[wl]\n\ts_waitcnt vmcnt(0)\n\t"
            ".set r_, 0\n\t.rept 16\n\t"
            "ds_write_b128 %[wl], v[112+4*r_:115+4*r_] offset:(r_>>2)*16384+(r_&3)*1024\n\tds_write_b128 v39, v[176+4*r_:179+4*r_] offset:(r_>>2)*16384+(r_&3)*1024\n\t"
            ".set r_, r_+1\n\t.endr\n\t"
            "ds_write_b128 v38, v[240:243]\n\tds_write_b128 v38, v[244:247] offset:1024\n\tds_write_b128 v38, v[248:251] offset:2048\n\tds_write_b128 v38, v[252:255] offset:3072\n\t"
            "v_mov_b32 v39, %[ntot]\n\tds_write_b32 %[aland], v39\n\ts_branch .Lend%=\n"
            ".Lidle%=:\n\t"
            "s_sleep 1\n\ts_sub_i32 %[bud], %[bud], 1\n\ts_cmp_gt_i32 %[bud], 0\n\ts_cbranch_scc1 .Lc1%=\n\t"
            "s_mov_b32 %[lim], 0x3fffffff\n\ts_mov_b32 %[gate], %[nkv]\n\ts_branch .Lc1%=\n"
            ".Lrf%=:\n\t"
            "s_cmp_eq_u32 s75, 0\n\ts_cbranch_scc1 .Lno2%=\n\ts_cmp_ge_i32 %[wc], 9\n\ts_cbranch_scc1 .Lno2%=\n\t"
            WQUOTA(".Lno2%=")
            "s_mov_b32 s75, 0\n\tds_read_b128 v[40:43], %[adone]\n\ts_branch .Lwo%=\n"
            ".Lwo0%=:\n\t"
            WQUOTA(".Lidle%=")
            "s_waitcnt vmcnt(8)\n"
            ".Lwo%=:\n\t"
            "s_and_b32 s66, %[wc], 7\n\ts_mul_i32 s66, s66, 73728\n\ts_add_u32 s76, %[wpl], s66\n\ts_addc_u32 s77, %[wph], 0\n\t"
            "s_mul_i32 s66, %[wc], 36\n\ts_add_u32 s66, s66, .Lit2%=-.Lp%=\n\ts_add_u32 s62, s64, s66\n\ts_addc_u32 s63, s65, 0\n\t"
            "s_add_i32 %[wc], %[wc], 1\n\t"
            "s_setpc_b64 s[62:63]\n"
            ".Lit2%=:\n\t"
            ".set r_, 112\n\t.rept 8\n\t"
            "global_load_dwordx4 v[r_:r_+3], %[f0], s[76:77]\n\tglobal_load_dwordx4 v[r_+4:r_+7], %[f1], s[76:77] offset:1024\n\t"
            "global_load_dwordx4 v[r_+8:r_+11], %[f2], s[76:77] offset:2048\n\tglobal_load_dwordx4 v[r_+12:r_+15], %[f3], s[76:77] offset:3072\n\ts_branch .Lc1%=\n\t"
            ".set r_, r_+16\n\t.endr\n\t"
            "global_load_dwordx4 v[240:243], %[h0], s[76:77]\n\tglobal_load_dwordx4 v[244:247], %[h1], s[76:77] offset:1024\n\t"
            "global_load_dwordx4 v[248:251], %[h2], s[76:77] offset:2048\n\tglobal_load_dwordx4 v[252:255], %[h3], s[76:77] offset:3072\n\ts_branch .Lc1%=\n"
            ".Lis%=:\n\t"
            "s_cmp_ge_i32 %[ni], " STR(GATEA) "\n\ts_cbranch_scc1 .Lrf%=\n\t"
            "s_add_u32 s67, %[bi], .Lit%=-.Lp%=\n\ts_add_u32 s62, s64, s67\n\ts_addc_u32 s63, s65, 0\n\t"
            "s_bitset1_b64 s[72:73], %[ni]\n\ts_add_i32 %[bi], %[bi], 36\n\t"
            "s_setpc_b64 s[62:63]\n"
            ".Lit%=:\n\t"
            ".set r_, 48\n\t.rept 4\n\t"
            "global_load_dwordx4 v[r_:r_+3], v44, s[60:61] nt\n\tglobal_load_dwordx4 v[r_+4:r_+7], v45, s[60:61] offset:1024 nt\n\t"
            "global_load_dwordx4 v[r_+8:r_+11], v46, s[60:61] offset:2048 nt\n\tglobal_load_dwordx4 v[r_+12:r_+15], v47, s[60:61] offset:3072 nt\n\ts_branch .Lbk%=\n\t"
            ".set r_, r_+16\n\t.endr\n"
            ".Lwr%=:\n\t"
            "s_add_i32 s66, s67, 9\n\ts_max_i32 %[lim], %[lim], s66\n\t"
            "s_cmp_lt_i32 %[nw], %[lim]\n\ts_cbranch_scc0 .Lc2%=\n\t"
            "v_add_u32 v39, %[sw], %[wl]\n\t"
            "s_add_u32 s66, %[bw], .Lwt%=-.Lp%=\n\ts_add_u32 s62, s64, s66\n\ts_addc_u32 s63, s65, 0\n\t"
            "s_add_i32 %[bw], %[bw], 36\n\t"
            "s_setpc_b64 s[62:63]\n"
            ".Lwt%=:\n\t"
            ".set r_, 48\n\t.rept 4\n\t"
            "ds_write_b128 v39, v[r_:r_+3]\n\tds_write_b128 v39, v[r_+4:r_+7] offset:1024\n\tds_write_b128 v39, v[r_+8:r_+11] offset:2048\n\tds_write_b128 v39, v[r_+12:r_+15] offset:3072\n\ts_branch .Lpb%=\n\t"
            ".set r_, r_+16\n\t.endr\n"
            ".Lend%=:\n\ts_mov_b32 m0, s70\n\ts_waitcnt lgkmcnt(0)"
            : [ni] "+&s"(ni), [nl] "+&s"(nl), [nw] "+&s"(nw), [bi] "+&s"(bi), [bw] "+&s"(bw), [gate] "+&s"(gate), [si] "+&s"(si), [sw] "+&s"(sw), [lim] "+&s"(lim),
              [wc] "+&s"(wc), [bud] "+&s"(budget)
            : [nkv] "s"(nkv), [ntot] "s"(ntot), [g1] "s"(roleB ? HPRE : GATEA), [dbase] "s"(lds0 + j * 4096),
              [kpl] "s"((unsigned)(uintptr_t)kp), [kph] "s"((unsigned)((uintptr_t)kp >> 32)), [vpl] "s"((unsigned)(uintptr_t)vp), [vph] "s"((unsigned)((uintptr_t)vp >> 32)),
              [wpl] "s"((unsigned)(uintptr_t)wb), [wph] "s"((unsigned)((uintptr_t)wb >> 32)),
              [k0] "v"(vK[0]), [k1] "v"(vK[1]), [k2] "v"(vK[2]), [k3] "v"(vK[3]), [vv] "v"(vV), [f0] "v"(oF[0]), [f1] "v"(oF[1]), [f2] "v"(oF[2]), [f3] "v"(oF[3]),
              [h0] "v"(oH[0]), [h1] "v"(oH[1]), [h2] "v"(oH[2]), [h3] "v"(oH[3]),
              [wl] "v"(wl), [adone] "v"(a_done), [agate] "v"(a_gate), [aland] "v"(a_landed)
            : "memory", "scc", "vcc", "s60", "s61", "s62", "s63", "s64", "s65", "s66", "s67", "s70", "s71", "s72", "s73", "s75", "s76", "s77", CLOB_V38_255);
        asm volatile("" :: "{v37}"(ctd));
        if (roleB) {
            const unsigned li = (unsigned)((idx * 4 + j) * 40 + (lane < 40 ? lane : 0)), row = li / 5, c = li - row * 5; unsigned d;
            asm volatile("global_load_dword %0, %1, %2\n\ts_waitcnt vmcnt(0)" : "=&v"(d) : "v"(row * 1152 + (c < 4 ? c * 128 : 1024)), "s"(wb) : "memory");
            wait_lds(lds + L_GATE, 12, budget);
            DMA4("", wb, oH[0], oH[1], oH[2], oH[3], lds0 + 8 * USLOT + j * 4096);
#pragma nounroll
            for (int k = 0; k < 8; ++k) { const int p = (b + k) & 7; DMA4("", wb + (size_t)p * 73728, oF[0], oF[1], oF[2], oF[3], lds0 + p * USLOT + j * 4096); }
            WAITV(0); lds_put(a_landed, (unsigned)(nkv + NSL));
        }
        if (j != 0) return;
        wait_lds(lds + L_CNT + 8, 1, budget);
        tagf = rfl64(*(volatile LAS const u64*)(lds + L_TAG));
    } else {
        const int rg = wid;
        __builtin_amdgcn_s_setprio(2);
        if (!roleB) {
            unsigned d[4];
            const unsigned i0 = (idx - 16) * 288 + rg * 72 + lane, i1 = i0 - lane + 64 + (lane & 7);
            asm volatile("global_load_dword %0, %4, %8\n\tglobal_load_dword %1, %5, %9\n\tglobal_load_dword %2, %6, %8\n\tglobal_load_dword %3, %7, %9\n\t"
                         "s_waitcnt lgkmcnt(0)\n\ts_barrier\n\ts_waitcnt vmcnt(0)"
                         : "=&v"(d[0]), "=&v"(d[1]), "=&v"(d[2]), "=&v"(d[3])
                         : "v"((i0 >> 3) * 8192 + ks * 1024 + (i0 & 7) * 128), "v"(i0 * 128), "v"((i1 >> 3) * 8192 + ks * 1024 + (i1 & 7) * 128), "v"(i1 * 128), "s"(Wq), "s"(X) : "memory");
        }
        const u64 epoch = __hip_atomic_load((GAS const u64*)epo, RLX_AGENT);
        u64 tag;
        const int q4 = (lane & 15) >> 2, p4 = lane & 3, blk = (lane >> 4) & 1;
        int u = 0, avail = 0;
        unsigned so = lds0;
        unsigned a_landed = lds0 + L_LANDED, a_done = lds0 + L_DONE + 4 * rg; PIN(a_landed); PIN(a_done);
        auto acquire = [&]() AINL -> unsigned {
            avail = __builtin_amdgcn_readfirstlane(avail); budget = __builtin_amdgcn_readfirstlane(budget);
            asm volatile("s_cmp_gt_i32 %0, %2\n\ts_cbranch_scc0 .Las%=\n"
                         ".Lab%=:\n\t.subsection 1\n"
                         ".Las%=:\n\tds_read_b128 v[140:143], %3\n\ts_waitcnt lgkmcnt(0)\n\tv_min_u32 v142, v142, v143\n\tv_min3_u32 v140, v140, v141, v142\n\ts_nop 1\n\tv_readfirstlane_b32 %0, v140\n\ts_nop 3\n\t"
                         "s_cmp_gt_i32 %0, %2\n\ts_cbranch_scc1 .Lab%=\n\ts_sub_i32 %1, %1, 1\n\ts_cmp_lt_i32 %1, 1\n\ts_cbranch_scc1 .Lab%=\n\ts_sleep 1\n\ts_branch .Las%=\n\t.subsection 0"
                         : "+&s"(avail), "+&s"(budget) : "s"(__builtin_amdgcn_readfirstlane(u)), "v"(a_landed) : "memory", "scc", "v140", "v141", "v142", "v143");
            const unsigned o = so; ++u; so = so == lds0 + (NSL - 1) * USLOT ? lds0 : so + USLOT; return o; };
        auto release = [&](int c) AINL { lds_put(a_done, (unsigned)c); };
        auto signoff = [&](int cnt_idx, char* flag) AINL { WAITV(0);
            unsigned old = 0; if (lane == 0) old = __hip_atomic_fetch_add((LAS unsigned*)(lds + L_CNT + 4 * cnt_idx), 1u, RLX_WG);
            if (__builtin_amdgcn_readfirstlane(old) == 3 && lane == 0) __hip_atomic_store((GAS u64*)flag, tag, RLX_AGENT); };
        auto opengate = [&](unsigned n) AINL { asm volatile("" ::: "memory"); if (lane == 0) __hip_atomic_fetch_add((LAS unsigned*)(lds + L_GATE), n, RLX_WG); };
        const unsigned krd = (unsigned)(256 * r32 + 16 * (hi ^ (r32 & 15)));
        bf16x8 qf[8], kf[8]; unsigned ka[8];
#define KRD(s, off) kf[s] = ldsrd16(LP(ka[s]) + (off))
#define KADDR(s, o) do { ka[s] = (krd ^ ((s) << 5)) + (o); PIN(ka[s]); } while (0)
#define KFIRST() do { const unsigned ko_ = acquire(); _Pragma("unroll") for (int s_ = 0; s_ < 8; ++s_) { KADDR(s_, ko_); KRD(s_, 0); } } while (0)
        const unsigned fx = (unsigned)((pair * 4 + rg) * 8192 + lane * 16);
        if (roleB) {
            const int p4s = ((p4 & 1) << 1) | (p4 >> 1);
            unsigned wrd[4], xa[4];
#pragma unroll
            for (int dt = 0; dt < 4; ++dt) wrd[dt] = (unsigned)((rg >> 1) * 256 + 512 * (8 * hi + q4) + 16 * ((4 * dt + 2 * blk + (p4s >> 1)) ^ (q4 << 2)) + 8 * (p4s & 1));
            { const int row = (rg & 1) * 32 + r32;
#pragma unroll
              for (int s = 0; s < 4; ++s) xa[s] = (unsigned)(row * 128 + 16 * ((2 * s + hi) ^ ((row >> 1) & 7))); }
            f32x16 acc[4] = {};
            bf16x8 xf[4], xn[4], wf[8];
#define WRD(i, wo) wf[i] = trrd2(LP((wo) + wrd[(i) & 3]) + ((i) >> 2) * 8192, LP((wo) + wrd[(i) & 3]) + ((i) >> 2) * 8192 + 2048)
#define XRD(s, xo) ldsrd16(LP((xo) + xa[s]))
#define QG(i, wi, wo, X_) do { acc[(i) & 3] = MFMA32(wf[i], xf[2 * (wi) + ((i) >> 2)], acc[(i) & 3]); PIN(acc[(i) & 3]); SBAR(); if ((i) == 0) BAR(); WRD(i, wo); X_; QNOP; SBAR(); } while (0)
            unsigned sb = lds0;
            asm volatile("s_waitcnt lgkmcnt(0)\n\ts_barrier" ::: "memory");
#pragma unroll
            for (int s = 0; s < 4; ++s) xf[s] = XRD(s, sb);
#pragma unroll
            for (int i = 0; i < 8; ++i) WRD(i, sb + USLOT);
            LGKM(0);
#define QODDPH() do { const unsigned wo = sb + 2 * USLOT; SBAR(); \
                QG(0, 0, wo, (void)0); QG(1, 0, wo, (void)0); QG(2, 0, wo, (void)0); QG(3, 0, wo, (void)0); QG(4, 0, wo, (void)0); QG(5, 0, wo, (void)0); QG(6, 0, wo, (void)0); QG(7, 0, wo, (void)0); } while (0)
#pragma nounroll
            for (int gi = 0; gi < 8; ++gi) {
                QODDPH();
                sb = sb == lds0 + 6 * USLOT ? lds0 : sb + 3 * USLOT;
                SBAR();
                QG(0, 1, sb + USLOT, xn[0] = XRD(0, sb)); QG(1, 1, sb + USLOT, xn[1] = XRD(1, sb)); QG(2, 1, sb + USLOT, (void)0); QG(3, 1, sb + USLOT, (void)0);
                QG(4, 1, sb + USLOT, xn[2] = XRD(2, sb)); QG(5, 1, sb + USLOT, (void)0); QG(6, 1, sb + USLOT, (void)0); QG(7, 1, sb + USLOT, xn[3] = XRD(3, sb));
                xf[0] = xn[0]; xf[1] = xn[1]; xf[2] = xn[2]; xf[3] = xn[3];
            }
            QODDPH();
#pragma unroll
            for (int dt = 0; dt < 4; ++dt) { acc[dt] = MFMA32(wf[dt], xf[2], acc[dt]); acc[dt] = MFMA32(wf[4 + dt], xf[3], acc[dt]); }
            tag = next_tag(rfl64(epoch));
#pragma unroll
            for (int f = 0; f < 8; ++f) { const f32x16& a = acc[f >> 1]; const int r = 8 * (f & 1);
                const u32x4 w = {pk2(a[r] * QSCALE, a[r + 1] * QSCALE), pk2(a[r + 2] * QSCALE, a[r + 3] * QSCALE), pk2(a[r + 4] * QSCALE, a[r + 5] * QSCALE), pk2(a[r + 6] * QSCALE, a[r + 7] * QSCALE)};
                qf[f] = __builtin_bit_cast(bf16x8, w);
                __builtin_amdgcn_raw_buffer_store_b128(w, wsr, (unsigned)WS_QX + fx + f * 1024, 0, AUX_SC1); }
            opengate(1);
            signoff(0, ctl + 256); opengate(1);
            KFIRST();
        } else {
            tag = next_tag(rfl64(epoch));
            KFIRST();
            poll_flags(ctl + 256, tag, budget);
#pragma unroll
            for (int s = 0; s < 8; ++s) qf[s] = __builtin_bit_cast(bf16x8, __builtin_amdgcn_raw_buffer_load_b128(wsr, (unsigned)WS_QX + fx + s * 1024, 0, AUX_SC1));
            opengate(2);
        }
        asm volatile("" :: "{v37}"(ctd));
        f32x16 O0 = {}, O1 = {}, O2 = {}, O3 = {}, negm, SA, SB; float lsum = 0.f, mrow, ls0, ls1, mx0, mx1; u32x4 pw0, pw1, pk_; unsigned va[4]; s16x4 vh[16];
#define R_O0 "{v[192:207]}"
#define R_O1 "{v[208:223]}"
#define R_O2 "{v[224:239]}"
#define R_O3 "{v[240:255]}"
#define R_SA "{v[176:191]}"
#define R_SB "{v[160:175]}"
#define N_SA "176"
#define N_SB "160"
#define R_NM "{v[144:159]}"
#define PINR(x) asm volatile("" : "+" R_##x(x))
#define EXP2(x) __builtin_amdgcn_exp2f(x)
#define MX3(a, b, c) __builtin_fmaxf(__builtin_fmaxf((a), (b)), (c))
#define VRDH(g, t, off) vh[2 * (g) + (t)] = trrd(LP(va[(g) & 3]) + (off) + ((g) >> 2) * 4096 + (t) * 2048)
#define V2(g, vo) do { VRDH(g, 0, vo); VRDH(g, 1, vo); } while (0)
#define VF(g) __builtin_shufflevector(vh[2 * (g)], vh[2 * (g) + 1], 0, 1, 2, 3, 4, 5, 6, 7)
#define KSTEP(s, d) do { ka[s] += (d); PIN(ka[s]); } while (0)
#define VSTEP(c, d) do { va[c] += (d); PIN(va[c]); } while (0)
#define RESCALE(S, mx, mxb) do { float t0_; asm volatile( \
            "v_max_f32 %8, %8, %11\n\tv_cmp_lt_f32 vcc, %10, %8\n\ts_cbranch_vccnz .Lrs%=\n" \
            ".Lrb%=:\n\t.subsection 1\n" \
            ".Lrs%=:\n\ts_nop 15\n\tv_mov_b32 %9, %8\n\ts_nop 1\n\tv_permlane32_swap_b32 %8, %9\n\ts_nop 1\n\tv_max3_f32 %8, %8, %9, 0\n\t" \
            "v_add_f32 %6, %6, %8\n\tv_exp_f32 %9, -%8\n\t" \
            ".set r_, 0\n\t.rept 16\n\tv_sub_f32 v[" N_##S "+r_], v[" N_##S "+r_], %8\n\tv_sub_f32 v[144+r_], 0, %6\n\t.set r_, r_+1\n\t.endr\n\t" \
            "v_mul_f32 %7, %7, %9\n\t" \
            ".set r_, 192\n\t.rept 64\n\tv_mul_f32 v[r_], v[r_], %9\n\t.set r_, r_+1\n\t.endr\n\ts_nop 1\n\ts_branch .Lrb%=\n\t.subsection 0" \
            : "+" R_O0(O0), "+" R_O1(O1), "+" R_O2(O2), "+" R_O3(O3), "+" R_##S(S), "+" R_NM(negm), "+v"(mrow), "+v"(lsum), "+v"(mx), "=&v"(t0_) : "s"(THR), "v"(mxb) : "vcc"); } while (0)
#define G1(SN, SO, s, CIN, PK, KL) do { SN = MFMA32(kf[s], qf[s], CIN); PINR(SN); SBAR(); PINR(SO); KL; SO[2 * (s)] = EXP2(SO[2 * (s)]); SO[2 * (s) + 1] = EXP2(SO[2 * (s) + 1]); PK; PINR(SO); SBAR(); } while (0)
#define PKW(SO, W, i, r) do { W[i] = pk2(SO[r], SO[(r) + 1]); PIN(W); } while (0)
#define PH1(SN, SO, KL) do { SBAR(); \
        G1(SN, SO, 0, negm, (void)0, KL(0));          G1(SN, SO, 1, SN, PKW(SO, pw0, 0, 0), KL(1)); G1(SN, SO, 2, SN, PKW(SO, pw0, 1, 2), KL(2));  G1(SN, SO, 3, SN, PKW(SO, pw0, 2, 4), KL(3)); \
        G1(SN, SO, 4, SN, PKW(SO, pw0, 3, 6), KL(4)); G1(SN, SO, 5, SN, PKW(SO, pw1, 0, 8), KL(5)); G1(SN, SO, 6, SN, PKW(SO, pw1, 1, 10), KL(6)); G1(SN, SO, 7, SN, PKW(SO, pw1, 2, 12), KL(7)); } while (0)
#define G2(g, OC, W, X_) do { OC = MFMA32(VF(g), __builtin_bit_cast(bf16x8, W), OC); PINR(OC); SBAR(); X_; SBAR(); } while (0)
#define ADD4(SO, r) do { ls0 += SO[r]; ls1 += SO[(r) + 1]; ls0 += SO[(r) + 2]; ls1 += SO[(r) + 3]; PIN(ls0); PIN(ls1); } while (0)
#define PH2(SN, SO, vo, A0, A1, A2, A3) do { SBAR(); \
        G2(0, O0, pw0, { V2(0, vo); PKW(SO, pw1, 3, 14); ls0 = lsum + SO[0]; ls1 = SO[1] + SO[3]; ls0 += SO[2]; PIN(ls0); PIN(ls1); }); G2(1, O1, pw0, { V2(1, vo); ADD4(SO, 4); }); \
        G2(2, O2, pw0, { V2(2, vo); ADD4(SO, 8); }); G2(3, O3, pw0, { V2(3, vo); ADD4(SO, 12); }); \
        G2(4, O0, pw1, { V2(4, vo); mx0 = MX3(SN[0], SN[1], SN[2]); mx1 = MX3(SN[3], SN[4], SN[5]); lsum = ls0 + ls1; PIN(mx0); PIN(mx1); PIN(lsum); A0; }); \
        G2(5, O1, pw1, { V2(5, vo); mx0 = MX3(mx0, SN[6], SN[7]); mx1 = MX3(mx1, SN[8], SN[9]); PIN(mx0); PIN(mx1); A1; }); \
        G2(6, O2, pw1, { V2(6, vo); mx0 = MX3(mx0, SN[10], SN[11]); mx1 = MX3(mx1, SN[12], SN[13]); PIN(mx0); PIN(mx1); A2; }); \
        G2(7, O3, pw1, { V2(7, vo); mx0 = MX3(mx0, SN[14], SN[15]); A3; }); \
        RESCALE(SN, mx0, mx1); } while (0)
#define KL0(s) KRD(s, 0)
#define KL1(s) KRD(s, 8192)
#define KLN(s) (void)0
#define NOP_ (void)0
#define PEEK() do { pk_ = *(volatile LAS const u32x4*)LP(a_landed); } while (0)
#define SEEN() do { unsigned m_ = pk_[0] < pk_[1] ? pk_[0] : pk_[1], n_ = pk_[2] < pk_[3] ? pk_[2] : pk_[3]; const int a_ = __builtin_amdgcn_readfirstlane(m_ < n_ ? m_ : n_); avail = a_ > avail ? a_ : avail; } while (0)
        {
            SA = MFMA32(kf[0], qf[0], (f32x16){});
#pragma unroll
            for (int s = 1; s < 8; ++s) SA = MFMA32(kf[s], qf[s], SA);
#pragma unroll
            for (int s = 0; s < 8; ++s) KRD(s, 8192);
            float mx = MX3(SA[0], SA[1], SA[2]);
#pragma unroll
            for (int r = 3; r < 15; r += 2) mx = MX3(mx, SA[r], SA[r + 1]);
            mx = swap_max(__builtin_fmaxf(mx, SA[15])); mrow = mx;
#pragma unroll
            for (int r = 0; r < 16; ++r) { SA[r] -= mx; negm[r] = -mx; }
            asm volatile("" : "+" R_NM(negm));
        }
        unsigned vcur = acquire(), kcur = so;
#pragma unroll
        for (int c = 0; c < 4; ++c) { va[c] = (unsigned)(256 * (4 * hi + q4) + 16 * ((4 * c + 2 * blk + (p4 >> 1)) ^ (q4 << 2)) + 8 * (p4 & 1)) + vcur; PIN(va[c]); }
#pragma unroll
        for (int g = 0; g < 8; ++g) V2(g, 0);
#pragma unroll
        for (int s = 0; s < 8; ++s) KADDR(s, kcur);
        u64 fpv = 0;
        unsigned tprev = 0, gap = 0; int per = roleB ? PB0 : PA0;
#pragma nounroll
        for (int t = 0; t < NT - 1; ++t) {
            (void)acquire();
            if (roleB || t <= PEND) {
                unsigned now = (unsigned)__builtin_amdgcn_s_memrealtime();
#pragma nounroll
                for (int k = 0; k < 256 && now - tprev < gap; ++k) { __builtin_amdgcn_s_sleep(1); now = (unsigned)__builtin_amdgcn_s_memrealtime(); }
                tprev = now; gap = (unsigned)per; const int d_ = roleB ? PB1 : PA1, m_ = roleB ? PMINB : PMINA; per = per - d_ > m_ ? per - d_ : m_;
            }
            LGKM(15);
            PH1(SB, SA, KL0);
            LGKM(8);
            PH2(SB, SA, 8192, PEEK(), NOP_, NOP_, NOP_);
            if (t == NT - 2) fpv = __hip_atomic_load((GAS const u64*)(ctl + 512), RLX_AGENT);
            LGKM(15);
            PH1(SA, SB, KL1);
            SEEN();
            release(u - 1);
            const unsigned vo = acquire();
            const unsigned dv = vo - vcur, dk = so - kcur; vcur = vo; kcur = so;
            VSTEP(0, dv); VSTEP(1, dv); VSTEP(2, dv); VSTEP(3, dv);
            LGKM(8);
            PH2(SA, SB, 0, { KSTEP(0, dk); KSTEP(1, dk); }, { KSTEP(2, dk); KSTEP(3, dk); }, { KSTEP(4, dk); KSTEP(5, dk); }, { KSTEP(6, dk); KSTEP(7, dk); });
        }
        PH1(SB, SA, KLN);
        release(u - 1);
        u32x4 pf[8]; float plse = 0.f; bool early = false;
#define PLOAD() do { _Pragma("unroll") for (int i = 0; i < 8; ++i) pf[i] = __builtin_amdgcn_raw_buffer_load_b128(wsr, (unsigned)WS_PX + fx + i * 1024, 0, AUX_SC1); \
            plse = __uint_as_float(__builtin_amdgcn_raw_buffer_load_b32(wsr, (unsigned)WS_PL + (pair * 4 + rg) * 256 + lane * 4, 0, AUX_SC1)); } while (0)
        if (!roleB) { if (rfl64(fpv) == tag) { early = true; asm volatile("" ::: "memory"); PLOAD(); } else fpv = __hip_atomic_load((GAS const u64*)(ctl + 512), RLX_AGENT); }
        PH2(SB, SA, 8192, NOP_, NOP_, NOP_, NOP_);
        release(u);
        if (!roleB && !early && rfl64(fpv) == tag) { early = true; asm volatile("" ::: "memory"); PLOAD(); }
        {
#pragma unroll
            for (int r = 0; r < 16; ++r) { SB[r] = EXP2(SB[r]); lsum += SB[r]; }
            pw0 = (u32x4){pk2(SB[0], SB[1]), pk2(SB[2], SB[3]), pk2(SB[4], SB[5]), pk2(SB[6], SB[7])};
            pw1 = (u32x4){pk2(SB[8], SB[9]), pk2(SB[10], SB[11]), pk2(SB[12], SB[13]), pk2(SB[14], SB[15])};
            const bf16x8 p0 = __builtin_bit_cast(bf16x8, pw0), p1 = __builtin_bit_cast(bf16x8, pw1);
            O0 = MFMA32(VF(0), p0, O0); O1 = MFMA32(VF(1), p0, O1); O2 = MFMA32(VF(2), p0, O2); O3 = MFMA32(VF(3), p0, O3);
            O0 = MFMA32(VF(4), p1, O0); O1 = MFMA32(VF(5), p1, O1); O2 = MFMA32(VF(6), p1, O2); O3 = MFMA32(VF(7), p1, O3);
        }
        lsum = swap_add(lsum);
        const float lse = mrow + __builtin_amdgcn_logf(lsum);
        if (!roleB) {
            if (!early) { poll_flags(ctl + 512, tag, budget); PLOAD(); }
            const float M = __builtin_fmaxf(lse, plse), eo = EXP2(lse - M), ep = EXP2(plse - M), r = __builtin_amdgcn_rcpf(eo + ep);
            const float fo = eo * r * __builtin_amdgcn_rcpf(lsum), fp = ep * r;
#pragma unroll
            for (int f = 0; f < 8; ++f) { const f32x16& a = f < 2 ? O0 : f < 4 ? O1 : f < 6 ? O2 : O3; const int q = 8 * (f & 1); u32x4 w;
#pragma unroll
                for (int e = 0; e < 4; ++e) w[e] = pk2(__builtin_fmaf(a[q + 2 * e], fo, fp * bflo(pf[f][e])), __builtin_fmaf(a[q + 2 * e + 1], fo, fp * bfhi(pf[f][e])));
                __builtin_amdgcn_raw_buffer_store_b128(w, wsr, (unsigned)WS_OX + fx + f * 1024, 0, AUX_SC1); }
        } else {
            const float inv = __builtin_amdgcn_rcpf(lsum);
#pragma unroll
            for (int f = 0; f < 8; ++f) { const f32x16& a = f < 2 ? O0 : f < 4 ? O1 : f < 6 ? O2 : O3; const int r = 8 * (f & 1);
                const u32x4 w = {pk2(a[r] * inv, a[r + 1] * inv), pk2(a[r + 2] * inv, a[r + 3] * inv), pk2(a[r + 4] * inv, a[r + 5] * inv), pk2(a[r + 6] * inv, a[r + 7] * inv)};
                __builtin_amdgcn_raw_buffer_store_b128(w, wsr, (unsigned)WS_PX + fx + f * 1024, 0, AUX_SC1); }
            __builtin_amdgcn_raw_buffer_store_b32(__float_as_uint(lse), wsr, (unsigned)WS_PL + (pair * 4 + rg) * 256 + lane * 4, 0, AUX_SC1);
        }
        signoff(1, roleB ? ctl + 512 : ctl + 768);
        if (roleB) opengate(1);
        const int qh = rg & 1, nh = rg >> 1;
        *(volatile LAS u64*)(lds + L_TAG) = tag;
        unsigned wa[4], wh = lds0 + 131072 + (nh * 64 + hi) * 128 + 32 * q4 + 128 * blk + 8 * p4; PIN(wh);
#pragma unroll
        for (int c = 0; c < 4; ++c) { wa[c] = lds0 + ((((nh ^ (c >> 1)) * 64 + hi) * 4 + q4) * 256 + 64 * ((2 * nh + (c & 1)) ^ q4) + 32 * blk + 8 * p4); PIN(wa[c]); }
#define AKS(i) ((i) < 48 ? (i) / 3 : 16 + ((i) - 48) / 2)
#define ATL(i) ((i) < 48 ? (i) % 3 : ((i) - 48) % 2)
#define AWA(i) wa[((i) < 48 ? 0 : 2) + (ATL(i) & 1)]
#define ARD(i) (ATL(i) == 2 ? trrd2(LP(wh) + AKS(i) * 512, LP(wh) + AKS(i) * 512 + 256) : trrd2(LP(AWA(i)) + (AKS(i) & 15) * 4096, LP(AWA(i)) + (AKS(i) & 15) * 4096 + 2048))
        constexpr int AD = 7, OPRE = 10;
        bf16x8 af[AD], of[32];
        const int pr0 = b * HKV + ks * 2 + nh;
        poll_flags(ws + WS_CT + (size_t)(pr0 ^ (lane & 1)) * 1024 + 768, tag, budget);
        const unsigned ob0 = (unsigned)WS_OX + (pr0 * 4 + qh) * 8192 + lane * 16, ob1 = (unsigned)WS_OX + ((pr0 ^ 1) * 4 + qh) * 8192 + lane * 16;
#define OLD(f) of[f] = __builtin_bit_cast(bf16x8, __builtin_amdgcn_raw_buffer_load_b128(wsr, ((f) < 16 ? ob0 : ob1) + ((f) & 3) * 1024, (((f) >> 3) & 1) * 16384 + (((f) >> 2) & 1) * 4096, AUX_SC1))
#pragma unroll
        for (int f = 0; f < OPRE; ++f) OLD(f);
        u += NSL - 1; (void)acquire();
#pragma unroll
        for (int i = 0; i < AD; ++i) af[i] = ARD(i);
        f32x16 acc[3] = {};
        SBAR();
#pragma unroll
        for (int i = 0; i < 80; ++i) { acc[ATL(i)] = MFMA32(af[i % AD], of[AKS(i)], acc[ATL(i)]); PIN(acc[ATL(i)]); SBAR(); if (i + AD < 80) af[i % AD] = ARD(i + AD);
            if ((i & 1) == 0 && OPRE + i / 2 < 32) OLD(OPRE + i / 2); SBAR(); }
        const unsigned sb = (unsigned)WS_SLAB + ((b * 4 + nq) * 8 + (r32 >> 2)) * 20480 + ks * 2560 + ((lane & 3) * 2 + hi) * 16;
#pragma unroll
        for (int k = 0; k < 5; ++k) { const int kk = k == 0 ? 4 : k - 1; const f32x16& a = acc[kk >> 1]; const int r = 8 * (kk & 1);
            const u32x4 v = {pk2(a[r], a[r + 1]), pk2(a[r + 2], a[r + 3]), pk2(a[r + 4], a[r + 5]), pk2(a[r + 6], a[r + 7])};
            __builtin_amdgcn_raw_buffer_store_b128(v, wsr, sb + (kk < 4 ? (qh * 8 + nh * 4 + kk) * 128 : 2048 + (nh * 2 + qh) * 128), 0, AUX_SC1); }
        signoff(2, fsl + ks * 256);
        tagf = tag;
    }
    {
        poll_flags(fsl + (lane & 7) * 256, tagf, budget);
        const int e = wid * 32 + r32;
        const unsigned sb = (unsigned)WS_SLAB + ((b * 4 + nq) * 8 + ks) * 20480 + hi * 10240 + e * 16;
        u32x4 v[4];
#pragma unroll
        for (int k = 0; k < 4; ++k) v[k] = __builtin_amdgcn_raw_buffer_load_b128(wsr, sb + k * 2560, 0, AUX_SC1);
        if (ks == 0 && nq == 0 && tid == 0) __hip_atomic_store((GAS u64*)epo, tagf, RLX_AGENT);
        float s[8], t[4];
#pragma unroll
        for (int c = 0; c < 4; ++c) { s[2 * c] = (bflo(v[0][c]) + bflo(v[1][c])) + (bflo(v[2][c]) + bflo(v[3][c])); s[2 * c + 1] = (bfhi(v[0][c]) + bfhi(v[1][c])) + (bfhi(v[2][c]) + bfhi(v[3][c])); }
#pragma unroll
        for (int c = 0; c < 4; ++c) { auto rr = __builtin_amdgcn_permlane32_swap(__float_as_uint(s[c]), __float_as_uint(s[4 + c]), false, false); t[c] = __uint_as_float(rr[0]) + __uint_as_float(rr[1]); }
        int qh = e >> 6, col = nq * 128 + ((e >> 3) & 7) * 16;
        if (wid == 4) {
#pragma unroll
            for (int c = 0; c < 4; ++c) t[c] += __shfl_xor(t[c], 16);
            qh = (e >> 3) & 1; col = 512 + nq * 16; }
        if (e < 144) *(u32x2*)(out + ((size_t)(b * SQ + qh * 32 + ks * 4 + ((e >> 1) & 3)) * DM + col + hi * 8 + (e & 1) * 4) * 2) = (u32x2){pk2(t[0], t[1]), pk2(t[2], t[3])};
    }
}

extern "C" void kernel_launch(void* const* d_in, const int* in_sizes, int n_in, void* d_out, int out_size, void* d_ws, size_t ws_size, hipStream_t stream) {
    (void)hipFuncSetAttribute((const void*)gqa_block_k, hipFuncAttributeMaxDynamicSharedMemorySize, LDS_BYTES);
    hipLaunchKernelGGL(gqa_block_k, dim3(256), dim3(512), LDS_BYTES, stream, (const char*)d_in[0], (const char*)d_in[1], (const char*)d_in[2], (const char*)d_in[3],
                       (const char*)d_in[4], (char*)d_out, (char*)d_ws);
}
```
